```python
import torch

def _softmax(x, axis=-1):
    return torch.softmax(x, dim=axis)
def _gelu(x):
    return torch.nn.functional.gelu(x, approximate="tanh")
def _silu(x):
    return torch.nn.functional.silu(x)
def _sigmoid(x):
    return torch.sigmoid(x)
def _layernorm(x, axis=-1, eps=1e-5):
    m = x.mean(dim=axis, keepdim=True)
    v = x.var(dim=axis, keepdim=True, unbiased=False)
    return (x - m) / torch.sqrt(v + eps)
def _scatter_add(tgt, idx, src, axis=0):
    return tgt.clone().index_add_(axis, idx, src)
def _segment_sum(src, seg, n):
    out = torch.zeros((n,) + src.shape[1:], dtype=src.dtype)
    return out.index_add_(0, seg, src)
def _pad(x, pw):
    flat = tuple(v for pair in reversed(pw) for v in pair)
    return torch.nn.functional.pad(x, flat)

kernel_name = 'gated_mix__mha_block__mixer_s_mlp__prompt'

def setup_inputs():

    _w = (lambda a: (lambda t: t.to(torch.bfloat16) if t.is_floating_point() else t)(torch.as_tensor(a)))
    def _A_setup():
        B, S, D, H = 1, 2048, 448, 16
        return {
            "x": _w((lambda *s: torch.randn(*s, dtype=torch.bfloat16) * 0.1)(B, S, D)),
            "wqkv": _w((lambda *s: torch.randn(*s, dtype=torch.bfloat16) * 0.8)(D, 3 * D)),
            "wo": _w((lambda *s: torch.randn(*s, dtype=torch.bfloat16) * 0.4)(D, D)),
        }
    _A_in = _A_setup()

    def _B_setup():
        B, S, D, F = 1, 2048, 448, 1792
        return {
            "x":  _w((lambda *s: torch.randn(*s, dtype=torch.bfloat16) * 0.1)(B, S, D)),
            "w1": _w((lambda *s: torch.randn(*s, dtype=torch.bfloat16) * 3.2)(D, D) * (D ** -0.5)),
            "w2": _w((lambda *s: torch.randn(*s, dtype=torch.bfloat16) * 3.2)(D, D) * (D ** -0.5)),
            "w3": _w((lambda *s: torch.randn(*s, dtype=torch.bfloat16) * 3.2)(D, F) * (D ** -0.5)),
            "w4": _w((lambda *s: torch.randn(*s, dtype=torch.bfloat16) * 3.2)(F, D) * (F ** -0.5)),
        }
    _B_in = _B_setup()

    return {
        "A_x": _A_in["x"],
        "A_wqkv": _A_in["wqkv"],
        "A_wo": _A_in["wo"],
        "B_x": _B_in["x"],
        "B_w1": _B_in["w1"],
        "B_w2": _B_in["w2"],
        "B_w3": _B_in["w3"],
        "B_w4": _B_in["w4"],
        "gate_w": _w((lambda *s: torch.randn(*s, dtype=torch.bfloat16) * 0.1)(448) * 16.0),
    }

def reference(A_x, A_wqkv, A_wo, B_x, B_w1, B_w2, B_w3, B_w4, gate_w):
    def _A_ref(x, wqkv, wo):
        B, S, D = x.shape
        H = 16
        Dh = D // H
        qkv = (lambda x, *d: x.permute(*d))((x @ wqkv).reshape(B, S, 3, H, Dh), 2, 0, 3, 1, 4)
        q, k, v = qkv[0], qkv[1], qkv[2]
        scores = torch.einsum("bhqd,bhkd->bhqk", q, k) * (Dh ** -0.5)
        p = _softmax(scores, axis=-1)
        o = torch.einsum("bhqk,bhkd->bhqd", p, v)
        return (lambda x, *d: x.permute(*d))(o, 0, 2, 1, 3).reshape(B, S, D) @ wo

    def _B_ref(x, w1, w2, w3, w4):
        B, S, D = x.shape
        h = _gelu(_layernorm(x, axis=-1) @ w1)
        c = D // 4
        g0 = _pad(h[:, :, 0*c:1*c], ((0, 0), (1, 0), (0, 0)))[:, :S, :]
        g1 = _pad(h[:, :, 1*c:2*c], ((0, 0), (0, 1), (0, 0)))[:, 1:, :]
        g2 = _pad(h[:, :, 2*c:3*c], ((0, 0), (2, 0), (0, 0)))[:, :S, :]
        g3 = _pad(h[:, :, 3*c:   ], ((0, 0), (0, 2), (0, 0)))[:, 2:, :]
        hs = torch.concatenate([g0, g1, g2, g3], axis=-1) @ w2
        x1 = x + hs
        h2 = _gelu(_layernorm(x1, axis=-1) @ w3) @ w4
        return x1 + h2

    x = A_x
    g = _sigmoid((x * gate_w).sum(-1, keepdims=True))
    a = _A_ref(x, A_wqkv, A_wo)
    b = _B_ref(x, B_w1, B_w2, B_w3, B_w4)
    return g * a + (1 - g) * b
```

```cpp
#include <hip/hip_runtime.h>
#include <hip/hip_bf16.h>
#include <stdint.h>

typedef unsigned short u16;
__device__ __forceinline__ float bf2f(u16 v){ return __uint_as_float(((unsigned)v)<<16); }
__device__ __forceinline__ u16 f2bf(float f){ __hip_bfloat16 h = __float2bfloat16(f); return *reinterpret_cast<u16*>(&h); }

constexpr int S=2048, D=448, H=16, DH=28, F=1792;

__global__ void gemm_naive(const u16* A, const u16* W, float* C, int M, int N, int K){
  int n = blockIdx.x*blockDim.x + threadIdx.x; int m = blockIdx.y;
  if(n>=N) return;
  float acc=0.f;
  for(int k=0;k<K;k++) acc += bf2f(A[m*K+k])*bf2f(W[k*N+n]);
  C[m*N+n]=acc;
}
__global__ void to_bf16(const float* in, u16* out, int n){ int i=blockIdx.x*blockDim.x+threadIdx.x; if(i<n) out[i]=f2bf(in[i]); }
__device__ __forceinline__ float gelu(float x){ float u = 0.7978845608028654f*(x+0.044715f*x*x*x); return 0.5f*x*(1.f+tanhf(u)); }
__global__ void gelu_bf16(const float* in, u16* out, int n){ int i=blockIdx.x*blockDim.x+threadIdx.x; if(i<n) out[i]=f2bf(gelu(bf2f(f2bf(in[i])))); }
__global__ void ln_kernel(const u16* x, u16* y){
  int r=blockIdx.x; int t=threadIdx.x; float s=0; for(int i=t;i<D;i+=64) s+=bf2f(x[r*D+i]);
  for(int o=32;o;o>>=1) s+=__shfl_xor(s,o); float m=s/D; float v=0; for(int i=t;i<D;i+=64){ float d=bf2f(x[r*D+i])-m; v+=d*d; }
  for(int o=32;o;o>>=1) v+=__shfl_xor(v,o); v/=D; float rs=1.f/sqrtf(v+1e-5f);
  for(int i=t;i<D;i+=64) y[r*D+i]=f2bf((bf2f(x[r*D+i])-m)*rs);
}
__global__ void shift_kernel(const u16* h, u16* g){ int i=blockIdx.x*blockDim.x+threadIdx.x; if(i>=S*D) return; int s=i/D,c=i%D; int grp=c/112; int d = grp==0?-1:grp==1?1:grp==2?-2:2; int ss=s+d; g[i]=(ss>=0&&ss<S)?h[ss*D+c]:(u16)0; }
__global__ void add_res(const u16* x, const float* hs, u16* x1, int n){ int i=blockIdx.x*blockDim.x+threadIdx.x; if(i<n) x1[i]=f2bf(bf2f(x[i])+bf2f(f2bf(hs[i]))); }
__global__ void attn_naive(const u16* qkv, u16* o){
  int q=blockIdx.x, h=blockIdx.y, t=threadIdx.x;
  __shared__ float sc[S];
  float qv[DH];
  for(int d=0;d<DH;d++) qv[d]=bf2f(qkv[q*3*D + h*DH + d]);
  float mx=-1e30f;
  for(int k=t;k<S;k+=64){ float a=0; for(int d=0;d<DH;d++) a+=qv[d]*bf2f(qkv[k*3*D + D + h*DH+d]); a*=0.1889822365f; sc[k]=a; mx=fmaxf(mx,a);}
  for(int o2=32;o2;o2>>=1) mx=fmaxf(mx,__shfl_xor(mx,o2));
  float l=0; for(int k=t;k<S;k+=64){ float p=expf(sc[k]-mx); sc[k]=p; l+=p; }
  for(int o2=32;o2;o2>>=1) l+=__shfl_xor(l,o2);
  __syncthreads();
  if(t<DH){ float a=0; for(int k=0;k<S;k++) a+=sc[k]*bf2f(qkv[k*3*D+2*D+h*DH+t]); o[q*D+h*DH+t]=f2bf(a/l); }
}
__global__ void final_kernel(const u16* x, const u16* gw, const float* a, const u16* x1, const float* h2, u16* out){
  int r=blockIdx.x; int t=threadIdx.x; float s=0; for(int i=t;i<D;i+=64) s+=bf2f(x[r*D+i])*bf2f(gw[i]);
  for(int o=32;o;o>>=1) s+=__shfl_xor(s,o); float g=1.f/(1.f+expf(-s));
  for(int i=t;i<D;i+=64){ float b=bf2f(x1[r*D+i])+h2[r*D+i]; out[r*D+i]=f2bf(g*a[r*D+i]+(1.f-g)*b); }
}

extern "C" void kernel_launch(void* const* d_in, const int* in_sizes, int n_in,
                              void* d_out, int out_size, void* d_ws, size_t ws_size,
                              hipStream_t stream) {
  const u16* x=(const u16*)d_in[0]; const u16* wqkv=(const u16*)d_in[1]; const u16* wo=(const u16*)d_in[2];
  const u16* w1=(const u16*)d_in[4]; const u16* w2=(const u16*)d_in[5]; const u16* w3=(const u16*)d_in[6]; const u16* w4=(const u16*)d_in[7]; const u16* gw=(const u16*)d_in[8];
  char* ws=(char*)d_ws;
  float* f0=(float*)ws; ws+= (size_t)S*F*4;
  float* fa=(float*)ws; ws+= (size_t)S*D*4;
  float* fh2=(float*)ws; ws+= (size_t)S*D*4;
  u16* qkv=(u16*)ws; ws+=(size_t)S*3*D*2;
  u16* o=(u16*)ws; ws+=(size_t)S*D*2;
  u16* ln=(u16*)ws; ws+=(size_t)S*D*2;
  u16* h=(u16*)ws; ws+=(size_t)S*D*2;
  u16* g=(u16*)ws; ws+=(size_t)S*D*2;
  u16* x1=(u16*)ws; ws+=(size_t)S*D*2;
  u16* h3=(u16*)ws; ws+=(size_t)S*F*2;
  gemm_naive<<<dim3((3*D+255)/256,S),256,0,stream>>>(x,wqkv,f0,S,3*D,D);
  to_bf16<<<(S*3*D+255)/256,256,0,stream>>>(f0,qkv,S*3*D);
  attn_naive<<<dim3(S,H),64,0,stream>>>(qkv,o);
  gemm_naive<<<dim3((D+255)/256,S),256,0,stream>>>(o,wo,fa,S,D,D);
  ln_kernel<<<S,64,0,stream>>>(x,ln);
  gemm_naive<<<dim3((D+255)/256,S),256,0,stream>>>(ln,w1,f0,S,D,D);
  gelu_bf16<<<(S*D+255)/256,256,0,stream>>>(f0,h,S*D);
  shift_kernel<<<(S*D+255)/256,256,0,stream>>>(h,g);
  gemm_naive<<<dim3((D+255)/256,S),256,0,stream>>>(g,w2,f0,S,D,D);
  add_res<<<(S*D+255)/256,256,0,stream>>>(x,f0,x1,S*D);
  ln_kernel<<<S,64,0,stream>>>(x1,ln);
  gemm_naive<<<dim3((F+255)/256,S),256,0,stream>>>(ln,w3,f0,S,F,D);
  gelu_bf16<<<(S*F+255)/256,256,0,stream>>>(f0,h3,S*F);
  gemm_naive<<<dim3((D+255)/256,S),256,0,stream>>>(h3,w4,fh2,S,D,F);
  final_kernel<<<S,64,0,stream>>>(x,gw,fa,x1,fh2,(u16*)d_out);
}
```

```cpp
#include <hip/hip_runtime.h>
#include <stdint.h>

typedef __attribute__((ext_vector_type(8))) __bf16 bf16x8;
typedef __attribute__((ext_vector_type(2))) __bf16 bf16x2;
typedef __attribute__((ext_vector_type(4))) short s16x4;
typedef __attribute__((ext_vector_type(16))) float f32x16;
typedef __attribute__((ext_vector_type(4))) float f32x4;
typedef __attribute__((ext_vector_type(2))) float f32x2;
typedef __attribute__((ext_vector_type(4))) unsigned u32x4;
typedef __attribute__((ext_vector_type(2))) unsigned u32x2;
#define LAS __attribute__((address_space(3)))
#define GAS __attribute__((address_space(1)))
#define DI __device__ __forceinline__

constexpr int SEQ = 2048, DM = 448, NH = 16, DH = 28, FF = 1792;

constexpr size_t WS_CNT   = 0;
constexpr size_t WS_GATE  = 16384;
constexpr size_t WS_STATS = 32768;
constexpr size_t WS_Q     = 1u << 20;
constexpr size_t WS_K     = WS_Q + (2u << 20);
constexpr size_t WS_V     = WS_K + (2u << 20);
constexpr size_t WS_H     = WS_V + (2u << 20);
constexpr size_t WS_X1    = WS_H + (2u << 20);
constexpr size_t WS_O     = WS_X1 + (2u << 20);
constexpr size_t WS_H3    = WS_O + (2u << 20);
constexpr unsigned TBYTES = 64u * 56u * 512u;
constexpr unsigned T3BYTES = 64u * 224u * 512u;

DI f32x16 mfma(u32x4 a, u32x4 b, f32x16 c) {
  return __builtin_amdgcn_mfma_f32_32x32x16_bf16(__builtin_bit_cast(bf16x8, a), __builtin_bit_cast(bf16x8, b), c, 0, 0, 0);
}
DI unsigned pack2(float a, float b) {
  f32x2 v; v[0] = a; v[1] = b;
  return __builtin_bit_cast(unsigned, __builtin_convertvector(v, bf16x2));
}
DI u32x4 tr_frag(const LAS char* p, int hi_off) {
  s16x4 lo = __builtin_amdgcn_ds_read_tr16_b64_v4i16((LAS s16x4*)p);
  s16x4 hi = __builtin_amdgcn_ds_read_tr16_b64_v4i16((LAS s16x4*)(p + hi_off));
  u32x2 a = __builtin_bit_cast(u32x2, lo), b = __builtin_bit_cast(u32x2, hi);
  u32x4 r; r[0] = a[0]; r[1] = a[1]; r[2] = b[0]; r[3] = b[1];
  return r;
}
DI float gelu_f(float x) {
  const float C0 = -2.302208198f;
  const float C1 = -0.1029432397f;
  float z = x * __builtin_fmaf(x * x, C1, C0);
  float e = __builtin_amdgcn_exp2f(z);
  return x * __builtin_amdgcn_rcpf(1.0f + e);
}
DI float other_half(float v, int hh) {
  unsigned u = __builtin_bit_cast(unsigned, v);
  auto r = __builtin_amdgcn_permlane32_swap(u, u, false, false);
  return __builtin_bit_cast(float, hh ? r[0] : r[1]);
}
DI void sq2(unsigned v, float& a, float& b) {
  const float lo = __builtin_bit_cast(float, v << 16), hi = __builtin_bit_cast(float, v & 0xffff0000u);
  a = __builtin_fmaf(lo, lo, a); b = __builtin_fmaf(hi, hi, b);
}
#define STAMP(i)
#define STAMP_DECL
#define STAMP_FLUSH(base, n)
DI f32x16 zero16() { f32x16 z; for (int i = 0; i < 16; i++) z[i] = 0.f; return z; }

DI void dma16_masked(unsigned voff, const void* sbase, unsigned ldsdst, uint64_t mask) {
  unsigned keep;
  asm volatile("s_mov_b32 %0, m0\n\ts_mov_b32 m0, %3\n\ts_mov_b64 exec, %4\n\tglobal_load_lds_dwordx4 %1, %2\n\ts_mov_b64 exec, -1\n\ts_mov_b32 m0, %0"
               : "=&s"(keep) : "v"(voff), "s"(sbase), "s"(ldsdst), "s"(mask) : "memory");
}
DI void dma16(unsigned voff, const void* sbase, unsigned ldsdst) {
  unsigned keep;
  asm volatile("s_mov_b32 %0, m0\n\ts_mov_b32 m0, %3\n\ts_nop 0\n\tglobal_load_lds_dwordx4 %1, %2\n\ts_mov_b32 m0, %0"
               : "=&s"(keep) : "v"(voff), "s"(sbase), "s"(ldsdst) : "memory");
}

constexpr int K1_SLOT    = 32768;
constexpr int K1_AUX_OFF = 4 * K1_SLOT;
constexpr int K1_CS_OFF  = K1_AUX_OFF + 3584;
constexpr int K1_SQ_OFF  = K1_CS_OFF + 512;
constexpr int K1_LDS     = K1_SQ_OFF + 512;

DI void dma16v(uint64_t vaddr, unsigned ldsdst) {
  unsigned keep;
  asm volatile("s_mov_b32 %0, m0\n\ts_mov_b32 m0, %2\n\ts_nop 0\n\tglobal_load_lds_dwordx4 %1, off\n\ts_mov_b32 m0, %0"
               : "=&s"(keep) : "v"(vaddr), "s"(ldsdst) : "memory");
}
DI u32x4 tr_frag2(const LAS char* p0, const LAS char* p1) {
  s16x4 lo = __builtin_amdgcn_ds_read_tr16_b64_v4i16((LAS s16x4*)p0);
  s16x4 hi = __builtin_amdgcn_ds_read_tr16_b64_v4i16((LAS s16x4*)p1);
  u32x2 a = __builtin_bit_cast(u32x2, lo), b = __builtin_bit_cast(u32x2, hi);
  u32x4 r; r[0] = a[0]; r[1] = a[1]; r[2] = b[0]; r[3] = b[1];
  return r;
}

__global__ __launch_bounds__(768, 3) void k1_kernel(const char* __restrict__ x, const char* __restrict__ wqkv,
                                                    const char* __restrict__ w1, const char* __restrict__ gate_w,
                                                    char* __restrict__ ws) {
  extern __shared__ __attribute__((aligned(16))) char smem[];
  LAS char* lds = (LAS char*)smem;
  const unsigned lds0 = (unsigned)(uintptr_t)lds;
  const int tid = threadIdx.x, lane = tid & 63;
  const int wv = __builtin_amdgcn_readfirstlane(tid >> 6);
  const int w = wv & 3;
  const int b = blockIdx.x, xcd = b & 7, bi = b >> 3;
  const int g = (xcd & 3) * 4 + (bi & 3);
  const int tg = (xcd >> 2) * 8 + (bi >> 2);
  const int r = lane & 31, hh = lane >> 5;
  STAMP_DECL STAMP(0);

  if (wv < 4) {
    uint64_t wa; unsigned wstep;
    {
      const int drow = lane >> 4, lc = (lane & 15) ^ (drow << 2), seg = lc >> 2, c4 = lc & 3;
      const int win = (56 * g) & ~15;
      wstep = seg < 3 ? 2688u : 896u;
      const char* base = seg < 3 ? wqkv + seg * 896 : w1;
      wa = (uint64_t)(uintptr_t)base + win + c4 * 16 + (unsigned)(16 * w + drow) * wstep;
    }
    unsigned xv[2];
    {
      const int rin = lane >> 3, pc = lane & 7;
#pragma unroll
      for (int e = 0; e < 2; e++) xv[e] = rin * 896 + ((pc ^ ((4 * e + (rin >> 1)) & 7)) * 16);
    }
    const char* xbase = x + (size_t)(tg * 128 + w * 32) * 896;
    auto issue_stage = [&](int st) {
      const unsigned slot = lds0 + (st & 3) * K1_SLOT;
#pragma unroll
      for (int jj = 0; jj < 4; jj++) {
        dma16v(wa + (uint64_t)((64 * st + 4 * jj) * wstep), slot + (16 * w + 4 * jj) * 256);
        dma16(xv[jj & 1], xbase + jj * (8 * 896) + st * 128, slot + 16384 + w * 4096 + jj * 1024);
      }
    };
    unsigned cb;
    {
      const int blk = (lane >> 4) & 1, q = (lane & 15) >> 2, p = lane & 3;
      const int u = (blk == 1 && p == 3) ? 0 : (g & 1) + 4 * blk + p;
      cb = (8 * hh + q) * 256 + u * 8 + ((3 ^ q) * 64) + w * 4096;
    }
    f32x16 cs = zero16();
    u32x4 ones; ones[0] = ones[1] = ones[2] = ones[3] = 0x3F803F80u;
    issue_stage(0); issue_stage(1);
    STAMP(1);
    asm volatile("s_waitcnt vmcnt(8)\n\ts_barrier" ::: "memory");
    issue_stage(2); issue_stage(3);
    cs = mfma(ones, tr_frag(lds + cb, 1024), cs);
#pragma unroll
    for (int st = 1; st < 7; st++) {
      if (st <= 4)      asm volatile("s_waitcnt vmcnt(16) lgkmcnt(0)" ::: "memory");
      else if (st == 5) asm volatile("s_waitcnt vmcnt(8) lgkmcnt(0)" ::: "memory");
      else              asm volatile("s_waitcnt vmcnt(0) lgkmcnt(0)" ::: "memory");
      STAMP(2 + st);
      asm volatile("s_barrier" ::: "memory");
      if (st + 3 < 7) issue_stage(st + 3);
      cs = mfma(ones, tr_frag(lds + cb + (st & 3) * K1_SLOT, 1024), cs);
    }
    if (hh == 0) *(LAS float*)(lds + K1_CS_OFF + (w * 32 + r) * 4) = cs[0];
    asm volatile("s_waitcnt lgkmcnt(0)\n\ts_barrier" ::: "memory");
    if (b == 0 && tid < 48) *(unsigned*)(ws + WS_CNT + tid * 256) = 0u;
    STAMP(2); STAMP_FLUSH(blockIdx.x, 9);
    return;
  }

  const int tile = tg * 4 + w;
  unsigned xb[4];
#pragma unroll
  for (int ks = 0; ks < 4; ks++) xb[ks] = 16384 + w * 4096 + r * 128 + (((2 * ks + hh) ^ ((r >> 1) & 7)) * 16);
  const int blk = (lane >> 4) & 1, q = (lane & 15) >> 2, p = lane & 3;
  const bool isaux = (blk == 1 && p == 3);
  const int u = isaux ? 0 : (g & 1) + 4 * blk + p;
  const unsigned wrow = (8 * hh + q) * 256 + u * 8;
  f32x16 acc[2]; acc[0] = zero16(); acc[1] = zero16();
  u32x4 wf[2][4][2], xf[2][4];

  if (wv < 8) {
    {
      const int t2 = tid - 256;
      const unsigned short* gwp = (const unsigned short*)gate_w;
      const int k1i = t2 < 192 ? t2 + 256 : 447;
      u32x2 a; a[0] = 0x3F80u | ((unsigned)gwp[t2] << 16); a[1] = 0u;
      u32x2 c; c[0] = 0x3F80u | ((unsigned)gwp[k1i] << 16); c[1] = 0u;
      *(LAS u32x2*)(lds + K1_AUX_OFF + t2 * 8) = a;
      *(LAS u32x2*)(lds + K1_AUX_OFF + k1i * 8) = c;
    }
    const unsigned wb0 = wrow + ((0 ^ q) * 64), wb1 = wrow + ((1 ^ q) * 64);
    float sqa[4] = {0.f, 0.f, 0.f, 0.f};
    auto read_stage = [&](int st) {
      const int so = (st & 3) * K1_SLOT;
#pragma unroll
      for (int ks = 0; ks < 4; ks++) {
        xf[st & 1][ks] = *(const LAS u32x4*)(lds + xb[ks] + so);
        wf[st & 1][ks][0] = tr_frag(lds + wb0 + so + ks * 4096, 1024);
        wf[st & 1][ks][1] = tr_frag(lds + wb1 + so + ks * 4096, 1024);
      }
    };
    asm volatile("s_waitcnt lgkmcnt(0)\n\ts_barrier" ::: "memory");
    STAMP(2);
    read_stage(0);
#pragma unroll
    for (int st = 0; st < 7; st++) {
      __builtin_amdgcn_sched_barrier(0);
      if (st < 6) { asm volatile("s_waitcnt lgkmcnt(0)\n\ts_barrier" ::: "memory"); STAMP(3 + st); read_stage(st + 1); }
#pragma unroll
      for (int ks = 0; ks < 4; ks++) {
        acc[0] = mfma(wf[st & 1][ks][0], xf[st & 1][ks], acc[0]);
        sq2(xf[st & 1][ks][0], sqa[0], sqa[1]);
        acc[1] = mfma(wf[st & 1][ks][1], xf[st & 1][ks], acc[1]);
        sq2(xf[st & 1][ks][1], sqa[2], sqa[3]);
      }
      asm volatile("" : "+v"(sqa[0]), "+v"(sqa[1]), "+v"(sqa[2]), "+v"(sqa[3]));
#pragma unroll
      for (int i = 0; i < 8; i++) {
        __builtin_amdgcn_sched_group_barrier(0x8, 1, 0);
        __builtin_amdgcn_sched_group_barrier(0x100, 3, 0);
        __builtin_amdgcn_sched_group_barrier(0x2, 5, 0);
      }
    }
    __builtin_amdgcn_sched_barrier(0);
    STAMP(9);
    float sq = (sqa[0] + sqa[1]) + (sqa[2] + sqa[3]);
    sq += other_half(sq, hh);
    if (hh == 0) *(LAS float*)(lds + K1_SQ_OFF + (w * 32 + r) * 4) = sq;
    asm volatile("s_waitcnt lgkmcnt(0)\n\ts_barrier" ::: "memory");
#pragma unroll
    for (int t = 0; t < 2; t++) {
      const float sc = t == 0 ? 0.27264314f : 1.0f;
      u32x4 f0, f1;
#pragma unroll
      for (int d = 0; d < 4; d++) {
        f0[d] = pack2(acc[t][2 * d] * sc, acc[t][2 * d + 1] * sc);
        f1[d] = pack2(acc[t][8 + 2 * d] * sc, acc[t][8 + 2 * d + 1] * sc);
      }
      f1[2] = hh ? (t == 1 ? 0x3F803F80u : 0u) : f1[2];
      f1[3] = hh ? (t == 1 ? 0x00003F80u : 0u) : f1[3];
      u32x4* o = (u32x4*)(ws + WS_Q + (size_t)t * (2u << 20) + (size_t)(g * 64 + tile) * 2048 + lane * 16);
      o[0] = f0; o[64] = f1;
    }
  } else {
    const unsigned wb2 = wrow + ((2 ^ q) * 64);
    const unsigned wb3 = isaux ? K1_AUX_OFF + (8 * hh + q) * 8 : wrow + ((3 ^ q) * 64);
    const unsigned kstep = isaux ? 128u : 4096u, hoff = isaux ? 32u : 1024u;
    const unsigned sstep = isaux ? 512u : (unsigned)K1_SLOT, wrapst = isaux ? 512u : (unsigned)(-3 * K1_SLOT);
    float sqa[4] = {0.f, 0.f, 0.f, 0.f};
    unsigned a3[4];
#pragma unroll
    for (int ks = 0; ks < 4; ks++) a3[ks] = wb3 + ks * kstep;
    auto read_stage = [&](int st) {
      const int so = (st & 3) * K1_SLOT;
#pragma unroll
      for (int ks = 0; ks < 4; ks++) {
        xf[st & 1][ks] = *(const LAS u32x4*)(lds + xb[ks] + so);
        wf[st & 1][ks][0] = tr_frag(lds + wb2 + so + ks * 4096, 1024);
        wf[st & 1][ks][1] = tr_frag2(lds + a3[ks], lds + a3[ks] + hoff);
        a3[ks] += (st == 3) ? wrapst : sstep;
      }
    };
    asm volatile("s_barrier" ::: "memory");
    STAMP(2);
    read_stage(0);
#pragma unroll
    for (int st = 0; st < 7; st++) {
      __builtin_amdgcn_sched_barrier(0);
      if (st < 6) { asm volatile("s_waitcnt lgkmcnt(0)\n\ts_barrier" ::: "memory"); STAMP(3 + st); read_stage(st + 1); }
#pragma unroll
      for (int ks = 0; ks < 4; ks++) {
        acc[0] = mfma(xf[st & 1][ks], wf[st & 1][ks][0], acc[0]);
        sq2(xf[st & 1][ks][2], sqa[0], sqa[1]);
        acc[1] = mfma(wf[st & 1][ks][1], xf[st & 1][ks], acc[1]);
        sq2(xf[st & 1][ks][3], sqa[2], sqa[3]);
      }
      asm volatile("" : "+v"(sqa[0]), "+v"(sqa[1]), "+v"(sqa[2]), "+v"(sqa[3]));
#pragma unroll
      for (int i = 0; i < 8; i++) {
        __builtin_amdgcn_sched_group_barrier(0x8, 1, 0);
        __builtin_amdgcn_sched_group_barrier(0x2, 2, 0);
        __builtin_amdgcn_sched_group_barrier(0x100, 3, 0);
        __builtin_amdgcn_sched_group_barrier(0x2, 4, 0);
      }
    }
    __builtin_amdgcn_sched_barrier(0);
    STAMP(9);
    {
      u32x4 f0, f1;
      const unsigned v = r == 28 ? 0x3F803F80u : 0u;
#pragma unroll
      for (int d = 0; d < 4; d++) {
        f0[d] = r >= 28 ? v : pack2(acc[0][2 * d], acc[0][2 * d + 1]);
        f1[d] = r >= 28 ? v : pack2(acc[0][8 + 2 * d], acc[0][8 + 2 * d + 1]);
      }
      u32x4* o = (u32x4*)(ws + WS_V + (size_t)(g * 64 + tile) * 2048 + lane * 16);
      o[0] = f0; o[64] = f1;
    }
    if (g == 0 && hh == 1) {
      float gt = __builtin_amdgcn_rcpf(1.0f + __builtin_amdgcn_exp2f(-1.442695041f * acc[1][13]));
      *(float*)(ws + WS_GATE + (tile * 32 + r) * 4) = gt;
    }
    float sx = acc[1][12];
    { float o = other_half(sx, hh); sx = hh ? sx : o; }
    const float mean = sx * (1.0f / 448.0f);
    float sq = (sqa[0] + sqa[1]) + (sqa[2] + sqa[3]);
    sq += other_half(sq, hh);
    asm volatile("s_barrier" ::: "memory");
    sq += *(LAS float*)(lds + K1_SQ_OFF + (w * 32 + r) * 4);
    const float var = fmaxf(sq * (1.0f / 448.0f) - mean * mean, 0.f);
    const float rstd = __builtin_amdgcn_rsqf(var + 1e-5f);
    char* dst = ws + WS_H + (size_t)tile * (56 * 512) + r * 16;
#pragma unroll
    for (int gg = 0; gg < 4; gg++) {
      const LAS char* cp = lds + K1_CS_OFF + (8 * gg + 4 * hh) * 4;
      f32x4 c = (*(const LAS f32x4*)cp + *(const LAS f32x4*)(cp + 128)) + (*(const LAS f32x4*)(cp + 256) + *(const LAS f32x4*)(cp + 384));
      float y[4];
#pragma unroll
      for (int i = 0; i < 4; i++) y[i] = gelu_f(rstd * __builtin_fmaf(-mean, c[i], acc[1][4 * gg + i]));
      u32x2 o; o[0] = pack2(y[0], y[1]); o[1] = pack2(y[2], y[3]);
      const int uu = 7 * g + 2 * gg + hh;
      if (gg < 3 || hh == 0) *(u32x2*)(dst + (uu >> 1) * 512 + (uu & 1) * 8) = o;
    }
  }
  STAMP(10);
  STAMP(11);
  STAMP_FLUSH(blockIdx.x, 12);
}

constexpr int K2_B_OFF   = 0;
constexpr int K2_A_OFF   = 114688;
constexpr int K2_CSP_OFF = K2_A_OFF + 32768;
constexpr int K2_CS_OFF  = K2_CSP_OFF + 2048;
constexpr int K2_M_OFF   = K2_CS_OFF + 512;
constexpr int K2_SY_OFF  = K2_M_OFF + 2048;
constexpr int K2_LDS     = K2_SY_OFF + 256;
enum { SY_A = 0, SY_B = 1, SY_PUBA = 2, SY_PUBB = 3, SY_RDY_X1 = 4, SY_RDY_H3 = 5, SY_RDY_O = 6, SY_AFIN = 7, SY_BAD = 8 };
constexpr unsigned SPIN_CAP = 1u << 22;

DI unsigned lds_ld(LAS unsigned* p) { return __hip_atomic_load(p, __ATOMIC_RELAXED, __HIP_MEMORY_SCOPE_WORKGROUP); }
DI void group_sync(LAS unsigned* cnt, unsigned target, int lane) {
  asm volatile("s_waitcnt lgkmcnt(0)" ::: "memory");
  if (lane == 0) __hip_atomic_fetch_add(cnt, 1u, __ATOMIC_RELAXED, __HIP_MEMORY_SCOPE_WORKGROUP);
  for (unsigned s = 0; (unsigned)__builtin_amdgcn_readfirstlane(lds_ld(cnt)) < target && s < SPIN_CAP; s++) __builtin_amdgcn_s_sleep(0);
  asm volatile("" ::: "memory");
}
DI void publish(LAS unsigned* lcnt, unsigned* gcnt, int lane) {
  asm volatile("s_waitcnt vmcnt(0)" ::: "memory");
  if (lane == 0) {
    unsigned old = __hip_atomic_fetch_add(lcnt, 1u, __ATOMIC_RELAXED, __HIP_MEMORY_SCOPE_WORKGROUP);
    if ((old & 3u) == 3u) __hip_atomic_fetch_add((GAS unsigned*)gcnt, 1u, __ATOMIC_RELAXED, __HIP_MEMORY_SCOPE_AGENT);
  }
}
DI void wait_ready(LAS unsigned* lflag, unsigned* gcnt, unsigned target, bool leader, int lane) {
  if (leader) {
    for (unsigned s = 0; (unsigned)__builtin_amdgcn_readfirstlane(__hip_atomic_load((GAS unsigned*)gcnt, __ATOMIC_RELAXED, __HIP_MEMORY_SCOPE_AGENT)) < target && s < SPIN_CAP; s++)
      __builtin_amdgcn_s_sleep(1);
    asm volatile("" ::: "memory");
    if (lane == 0) __hip_atomic_store(lflag, 1u, __ATOMIC_RELAXED, __HIP_MEMORY_SCOPE_WORKGROUP);
  } else {
    for (unsigned s = 0; __builtin_amdgcn_readfirstlane(lds_ld(lflag)) == 0 && s < SPIN_CAP; s++) __builtin_amdgcn_s_sleep(1);
  }
  asm volatile("" ::: "memory");
}
DI u32x4 mkrsrc(const void* p, unsigned bytes) {
  const uint64_t a = (uint64_t)(uintptr_t)p;
  u32x4 r; r[0] = (unsigned)a; r[1] = (unsigned)(a >> 32) & 0xffffu; r[2] = bytes; r[3] = 0x00020000u;
  return r;
}
#define BLOAD(dst, voff, rs, soff, imm) \
  asm volatile("buffer_load_dwordx4 %0, %1, %2, %3 offen offset:%4" : "=v"(dst) : "v"(voff), "s"(rs), "s"(soff), "i"(imm) : "memory")
#define BLOAD_SC1(dst, voff, rs, soff, imm) \
  asm volatile("buffer_load_dwordx4 %0, %1, %2, %3 offen offset:%4 sc1" : "=v"(dst) : "v"(voff), "s"(rs), "s"(soff), "i"(imm) : "memory")
DI void st16_sc1(u32x4 v, __amdgpu_buffer_rsrc_t rs, unsigned off) { __builtin_amdgcn_raw_buffer_store_b128(v, rs, off, 0, 16); }
DI void st8_sc1(u32x2 v, __amdgpu_buffer_rsrc_t rs, unsigned off) { __builtin_amdgcn_raw_buffer_store_b64(v, rs, off, 0, 16); }

DI void tile_put(LAS char* p, const f32x16& t, int lane) {
#pragma unroll
  for (int g = 0; g < 4; g++) { f32x4 v; v[0] = t[4 * g]; v[1] = t[4 * g + 1]; v[2] = t[4 * g + 2]; v[3] = t[4 * g + 3]; *(LAS f32x4*)(p + g * 1024 + lane * 16) = v; }
}
DI void tile_add(const LAS char* p, f32x16& t, int lane) {
#pragma unroll
  for (int g = 0; g < 4; g++) { f32x4 v = *(const LAS f32x4*)(p + g * 1024 + lane * 16); t[4 * g] += v[0]; t[4 * g + 1] += v[1]; t[4 * g + 2] += v[2]; t[4 * g + 3] += v[3]; }
}
DI void reduce_scatter_32k(LAS char* buf, f32x16 (&t)[4], int w, int lane, LAS unsigned* cnt, unsigned& epoch) {
  tile_put(buf + (w * 2 + 0) * 4096, t[2], lane);
  tile_put(buf + (w * 2 + 1) * 4096, t[3], lane);
  epoch += 4; group_sync(cnt, epoch, lane);
  tile_add(buf + ((w ^ 2) * 2 + 0) * 4096, t[0], lane);
  tile_add(buf + ((w ^ 2) * 2 + 1) * 4096, t[1], lane);
  epoch += 4; group_sync(cnt, epoch, lane);
  tile_put(buf + w * 4096, t[1], lane);
  epoch += 4; group_sync(cnt, epoch, lane);
  tile_add(buf + (w ^ 1) * 4096, t[0], lane);
}

DI unsigned w64_dma_voff(int lane, unsigned ldw) { const int rin = lane >> 3, pc = lane & 7; return rin * ldw + ((pc ^ (((rin >> 1) & 1) << 2)) * 16); }
DI unsigned w64_tr_off(int lane, int nt) {
  const int hh = lane >> 5, blk = (lane >> 4) & 1, q = (lane & 15) >> 2, p = lane & 3;
  return (8 * hh + q) * 128 + ((nt ^ ((q >> 1) & 1)) * 64) + blk * 32 + p * 8;
}

#define ATT_STEP(SC, SN, PC0, PC1, PP0, PP1, KN0, KN1, QN0, QN1, VP0, VP1, OP, DO_QK, DO_PV)                   \
  {                                                                                                             \
    if (DO_QK) SN = mfma(KN0, QN0, zero16());                                                                   \
    PC0[0] = pack2(__builtin_amdgcn_exp2f(SC[0]), __builtin_amdgcn_exp2f(SC[1]));                               \
    PC0[1] = pack2(__builtin_amdgcn_exp2f(SC[2]), __builtin_amdgcn_exp2f(SC[3]));                               \
    __builtin_amdgcn_sched_barrier(0);                                                                          \
    if (DO_PV) OP = mfma(VP0, PP0, OP);                                                                         \
    PC0[2] = pack2(__builtin_amdgcn_exp2f(SC[4]), __builtin_amdgcn_exp2f(SC[5]));                               \
    PC0[3] = pack2(__builtin_amdgcn_exp2f(SC[6]), __builtin_amdgcn_exp2f(SC[7]));                               \
    __builtin_amdgcn_sched_barrier(0);                                                                          \
    if (DO_QK) SN = mfma(KN1, QN1, SN);                                                                         \
    PC1[0] = pack2(__builtin_amdgcn_exp2f(SC[8]), __builtin_amdgcn_exp2f(SC[9]));                               \
    PC1[1] = pack2(__builtin_amdgcn_exp2f(SC[10]), __builtin_amdgcn_exp2f(SC[11]));                             \
    __builtin_amdgcn_sched_barrier(0);                                                                          \
    if (DO_PV) OP = mfma(VP1, PP1, OP);                                                                         \
    PC1[2] = pack2(__builtin_amdgcn_exp2f(SC[12]), __builtin_amdgcn_exp2f(SC[13]));                             \
    PC1[3] = pack2(__builtin_amdgcn_exp2f(SC[14]), __builtin_amdgcn_exp2f(SC[15]));                             \
    __builtin_amdgcn_sched_barrier(0);                                                                          \
  }

DI float max16(const f32x16& s) {
  float a = fmaxf(fmaxf(s[0], s[1]), s[2]), b = fmaxf(fmaxf(s[3], s[4]), s[5]), c = fmaxf(fmaxf(s[6], s[7]), s[8]);
  float d = fmaxf(fmaxf(s[9], s[10]), s[11]), e = fmaxf(fmaxf(s[12], s[13]), s[14]);
  return fmaxf(fmaxf(fmaxf(a, b), c), fmaxf(fmaxf(d, e), s[15]));
}

__global__ __launch_bounds__(512, 2) void k2_kernel(const char* __restrict__ x, const char* __restrict__ wo,
                                                    const char* __restrict__ w2, const char* __restrict__ w3,
                                                    const char* __restrict__ w4, char* __restrict__ ws,
                                                    char* __restrict__ out) {
  extern __shared__ __attribute__((aligned(16))) char smem[];
  LAS char* lds = (LAS char*)smem;
  const unsigned lds0 = (unsigned)(uintptr_t)lds;
  LAS unsigned* sy = (LAS unsigned*)(lds + K2_SY_OFF);
  const int tid = threadIdx.x, lane = tid & 63;
  const int wv = __builtin_amdgcn_readfirstlane(tid >> 6);
  const int w = wv & 3;
  const int b = blockIdx.x, xcd = b & 7, bi = b >> 3;
  const int r = lane & 31, hh = lane >> 5;
  const bool has_gemm = bi < 28;
  const int nb = bi % 7, tb64 = xcd * 4 + bi / 7;
  unsigned* cnt_x1 = (unsigned*)(ws + WS_CNT);
  unsigned* cnt_h3 = (unsigned*)(ws + WS_CNT + 16 * 256);
  unsigned* cnt_o  = (unsigned*)(ws + WS_CNT + 32 * 256);
  STAMP_DECL STAMP(0);
  if (tid < 16) sy[tid] = 0u;
  __syncthreads();

  if (wv < 4) {
    const int head = 2 * xcd + (bi >> 4), qb = bi & 15;
    const u32x4* qp = (const u32x4*)(ws + WS_Q + (size_t)(head * 64 + qb * 4) * 2048) + lane;
    const u32x4* kp = (const u32x4*)(ws + WS_K + (size_t)(head * 64 + w * 16) * 2048) + lane;
    const u32x4* vp = (const u32x4*)(ws + WS_V + (size_t)(head * 64 + w * 16) * 2048) + lane;
    unsigned ea = 0;
    u32x4 q[4][2];
#pragma unroll
    for (int i = 0; i < 4; i++) { q[i][0] = qp[((w ^ i) * 2) * 64]; q[i][1] = qp[((w ^ i) * 2 + 1) * 64]; }
    f32x16 o[4];
    LAS float* mx = (LAS float*)(lds + K2_M_OFF);
    {
      f32x16 s = mfma(kp[0], q[0][0], zero16());
      s = mfma(kp[64], q[0][1], s);
      const float m = max16(s);
      if (hh) mx[w * 32 + r] = m;
      ea += 4; group_sync(sy + SY_A, ea, lane);
#pragma unroll
      for (int i = 0; i < 4; i++) {
        const float mm = mx[(w ^ i) * 32 + r];
        q[i][1][2] = hh ? (pack2(-mm, 0.f)) : q[i][1][2];
      }
    }
    STAMP(1);
    for (int attempt = 0;; attempt++) {
#pragma unroll
      for (int i = 0; i < 4; i++) o[i] = zero16();
      u32x4 kb[4][2], vb[4][2];
#pragma unroll
      for (int j = 0; j < 3; j++) { kb[j][0] = kp[j * 128]; kb[j][1] = kp[j * 128 + 64]; vb[j][0] = vp[j * 128]; vb[j][1] = vp[j * 128 + 64]; }
      f32x16 sA, sB;
      u32x4 pA0, pA1, pB0, pB1;
      sA = mfma(kb[0][0], q[0][0], zero16());
      sA = mfma(kb[0][1], q[0][1], sA);
      pB0 = pB1 = (u32x4){0u, 0u, 0u, 0u};
      vb[3][0] = vb[3][1] = (u32x4){0u, 0u, 0u, 0u};
#pragma unroll 1
      for (int kq = 0; kq < 4; kq++) {
#pragma unroll
        for (int j = 0; j < 4; j++) {
          const int kt = kq * 4 + j;
          const int jp = (j + 3) & 3, jn = (j + 1) & 3;
          ATT_STEP(sA, sB, pA0, pA1, pB0, pB1, kb[j][0], kb[j][1], q[1][0], q[1][1], vb[jp][0], vb[jp][1], o[3], true, true);
          {
            const int kn = kt + 3 < 16 ? kt + 3 : 15;
            kb[jp][0] = kp[kn * 128]; kb[jp][1] = kp[kn * 128 + 64]; vb[jp][0] = vp[kn * 128]; vb[jp][1] = vp[kn * 128 + 64];
          }
          ATT_STEP(sB, sA, pB0, pB1, pA0, pA1, kb[j][0], kb[j][1], q[2][0], q[2][1], vb[j][0], vb[j][1], o[0], true, true);
          ATT_STEP(sA, sB, pA0, pA1, pB0, pB1, kb[j][0], kb[j][1], q[3][0], q[3][1], vb[j][0], vb[j][1], o[1], true, true);
          ATT_STEP(sB, sA, pB0, pB1, pA0, pA1, kb[jn][0], kb[jn][1], q[0][0], q[0][1], vb[j][0], vb[j][1], o[2], true, true);
        }
      }
      o[3] = mfma(vb[3][0], pB0, o[3]);
      o[3] = mfma(vb[3][1], pB1, o[3]);
      STAMP(2);
      reduce_scatter_32k(lds + K2_A_OFF, o, w, lane, sy + SY_A, ea);
      float l = o[0][12];
      { float t = other_half(l, hh); l = hh ? l : t; }
      float amax = 0.f;
#pragma unroll
      for (int i = 0; i < 12; i++) amax = fmaxf(amax, fabsf(o[0][i]));
      if (!hh) { amax = fmaxf(fmaxf(amax, fabsf(o[0][12])), fmaxf(fabsf(o[0][13]), fmaxf(fabsf(o[0][14]), fabsf(o[0][15])))); }
      const bool ok = (l > 1.0e-10f) && (l < 1.0e18f) && (amax < 1.0e37f);
      if (attempt == 1) { o[1][0] = l; break; }
      if (__any(!ok) && lane == 0) __hip_atomic_store(sy + SY_BAD, 1u, __ATOMIC_RELAXED, __HIP_MEMORY_SCOPE_WORKGROUP);
      ea += 4; group_sync(sy + SY_A, ea, lane);
      if (__builtin_amdgcn_readfirstlane(lds_ld(sy + SY_BAD)) == 0) { o[1][0] = l; break; }
      float tm[4];
#pragma unroll
      for (int i = 0; i < 4; i++) { tm[i] = -3.0e38f; q[i][1][2] = hh ? 0u : q[i][1][2]; }
#pragma unroll 1
      for (int kt = 0; kt < 16; kt++) {
        const u32x4 k0 = kp[kt * 128], k1 = kp[kt * 128 + 64];
#pragma unroll
        for (int i = 0; i < 4; i++) {
          f32x16 s = mfma(k0, q[i][0], zero16());
          s = mfma(k1, q[i][1], s);
          tm[i] = fmaxf(tm[i], max16(s));
        }
      }
#pragma unroll
      for (int i = 0; i < 4; i++) {
        tm[i] = fmaxf(tm[i], other_half(tm[i], hh));
        if (hh) mx[w * 128 + (w ^ i) * 32 + r] = tm[i];
      }
      ea += 4; group_sync(sy + SY_A, ea, lane);
#pragma unroll
      for (int i = 0; i < 4; i++) {
        const int c = (w ^ i) * 32 + r;
        const float mm = -fmaxf(fmaxf(mx[c], mx[128 + c]), fmaxf(mx[256 + c], mx[384 + c]));
        const unsigned h0 = pack2(mm, 0.f) & 0xffffu; const float f0 = __builtin_bit_cast(float, h0 << 16);
        const unsigned h1 = pack2(mm - f0, 0.f) & 0xffffu; const float f1 = __builtin_bit_cast(float, h1 << 16);
        const unsigned h2 = pack2((mm - f0) - f1, 0.f) & 0xffffu;
        q[i][1][2] = hh ? (h0 | (h1 << 16)) : q[i][1][2];
        q[i][1][3] = hh ? h2 : q[i][1][3];
      }
      ea += 4; group_sync(sy + SY_A, ea, lane);
    }
    {
      const float inv = __builtin_amdgcn_rcpf(o[1][0]);
      const __amdgpu_buffer_rsrc_t ro = __builtin_amdgcn_make_buffer_rsrc(ws + WS_O, 0, TBYTES, 0x00020000);
      const unsigned base = (unsigned)(qb * 4 + w) * (56 * 512) + r * 16;
#pragma unroll
      for (int g = 0; g < 4; g++) {
        u32x2 v; v[0] = pack2(o[0][4 * g] * inv, o[0][4 * g + 1] * inv); v[1] = pack2(o[0][4 * g + 2] * inv, o[0][4 * g + 3] * inv);
        const int u = 7 * head + 2 * g + hh;
        if (g < 3 || hh == 0) st8_sc1(v, ro, base + (u >> 1) * 512 + (u & 1) * 8);
      }
      publish(sy + SY_PUBA, cnt_o + qb * 64, lane);
    }
    STAMP(3);
    if (has_gemm) {
      ea += 4; group_sync(sy + SY_A, ea, lane);
      const unsigned stg = K2_A_OFF + w * 8192;
      const unsigned dv = w64_dma_voff(lane, 896);
      const char* wsrc = wo + (size_t)(112 * w) * 896 + nb * 128;
      const unsigned t0 = w64_tr_off(lane, (w & 1)), t1 = w64_tr_off(lane, (w & 1) ^ 1);
      u32x4 wf[7][2];
#pragma unroll
      for (int j = 0; j < 8; j++) dma16(dv, wsrc + j * (8 * 896), lds0 + stg + j * 1024);
      asm volatile("s_waitcnt vmcnt(0)" ::: "memory");
#pragma unroll
      for (int s = 0; s < 4; s++) { wf[s][0] = tr_frag(lds + stg + t0 + s * 2048, 512); wf[s][1] = tr_frag(lds + stg + t1 + s * 2048, 512); }
      asm volatile("s_waitcnt lgkmcnt(0)" : "+v"(wf[0][0]), "+v"(wf[0][1]), "+v"(wf[1][0]), "+v"(wf[1][1]), "+v"(wf[2][0]), "+v"(wf[2][1]), "+v"(wf[3][0]), "+v"(wf[3][1]) :: "memory");
#pragma unroll
      for (int j = 0; j < 6; j++) dma16(dv, wsrc + (8 + j) * (8 * 896), lds0 + stg + j * 1024);
      asm volatile("s_waitcnt vmcnt(0)" ::: "memory");
#pragma unroll
      for (int s = 0; s < 3; s++) { wf[4 + s][0] = tr_frag(lds + stg + t0 + s * 2048, 512); wf[4 + s][1] = tr_frag(lds + stg + t1 + s * 2048, 512); }
      wait_ready(sy + SY_RDY_O, cnt_o + (tb64 >> 1) * 64, 16u, w == 0, lane);
      STAMP(4);
      const __amdgpu_buffer_rsrc_t ro = __builtin_amdgcn_make_buffer_rsrc(ws + WS_O, 0, TBYTES, 0x00020000);
      u32x4 af[7][2];
#pragma unroll
      for (int s = 0; s < 7; s++)
#pragma unroll
        for (int e = 0; e < 2; e++)
          af[s][e] = __builtin_amdgcn_raw_buffer_load_b128(ro, (unsigned)((tb64 * 2 + ((w >> 1) ^ e)) * 56 + 14 * w + 2 * s) * 512 + lane * 16, 0, 16);
      f32x16 t[4];
#pragma unroll
      for (int i = 0; i < 4; i++) t[i] = zero16();
#pragma unroll
      for (int s = 0; s < 7; s++)
#pragma unroll
        for (int i = 0; i < 4; i++) t[i] = mfma(wf[s][i & 1], af[s][i >> 1], t[i]);
      ea += 4; group_sync(sy + SY_A, ea, lane);
      reduce_scatter_32k(lds + K2_A_OFF, t, w, lane, sy + SY_A, ea);
      tile_put(lds + K2_A_OFF + 16384 + w * 4096, t[0], lane);
      asm volatile("s_waitcnt lgkmcnt(0)" ::: "memory");
      if (lane == 0) __hip_atomic_fetch_add(sy + SY_AFIN, 1u, __ATOMIC_RELAXED, __HIP_MEMORY_SCOPE_WORKGROUP);
    }
    STAMP(5);
    STAMP_FLUSH(256 + blockIdx.x, 6);
    return;
  }

  if (!has_gemm) return;
  unsigned eb = 0;
  const int ni = w & 1, tj = w >> 1;
  const int nb3 = bi % 14, tb128 = xcd * 2 + bi / 14;
  const unsigned dv64 = w64_dma_voff(lane, 896);
  const unsigned t0 = w64_tr_off(lane, ni), t1 = w64_tr_off(lane, ni ^ 1);
  const unsigned dv3 = (lane >> 4) * 3584 + (((lane & 15) ^ ((lane >> 4) << 2)) * 16);
  const char* w3src = w3 + nb3 * 256;
  unsigned x1p[8];
  {
    const unsigned sl = K2_B_OFF + 57344 + w * 14336;
    const char* wsrc = w2 + (size_t)(112 * w) * 896 + nb * 128;
#pragma unroll
    for (int j = 0; j < 14; j++) dma16(dv64, wsrc + j * (8 * 896), lds0 + sl + j * 1024);
    const u32x4 rh = mkrsrc(ws + WS_H, TBYTES);
    const int delta = w == 0 ? -1 : w == 1 ? 1 : w == 2 ? -2 : 2;
    u32x4 af[7][2];
    unsigned av[2];
#pragma unroll
    for (int e = 0; e < 2; e++) {
      const int tok = (tb64 * 2 + (tj ^ e)) * 32 + r + delta;
      av[e] = (tok >= 0 && tok < SEQ) ? (unsigned)(((tok >> 5) * 56 + 14 * w + hh) * 512 + (tok & 31) * 16) : 0x80000000u;
    }
#pragma unroll
    for (int s = 0; s < 7; s++)
#pragma unroll
      for (int e = 0; e < 2; e++) BLOAD(af[s][e], av[e], rh, (s >> 2) * 4096, (s & 3) * 1024);
    u32x2 xr[4];
    {
      const char* xp = x + (size_t)((tb64 * 2 + tj) * 32) * 896 + (nb * 64 + ni * 32) * 2;
      const unsigned xo = r * 896 + hh * 8;
#pragma unroll
      for (int g = 0; g < 4; g++) asm volatile("global_load_dwordx2 %0, %1, %2 offset:%3" : "=v"(xr[g]) : "v"(xo), "s"(xp), "i"(g * 16) : "memory");
    }
#pragma unroll
    for (int j = 0; j < 14; j++) { const int row0 = 4 * (14 * w + j); dma16(dv3, w3src + (size_t)row0 * 3584, lds0 + K2_B_OFF + row0 * 256); }
    asm volatile("s_waitcnt vmcnt(14)" : "+v"(af[0][0]), "+v"(af[0][1]), "+v"(af[1][0]), "+v"(af[1][1]), "+v"(af[2][0]), "+v"(af[2][1]), "+v"(af[3][0]), "+v"(af[3][1]),
                 "+v"(af[4][0]), "+v"(af[4][1]), "+v"(af[5][0]), "+v"(af[5][1]), "+v"(af[6][0]), "+v"(af[6][1]), "+v"(xr[0]), "+v"(xr[1]), "+v"(xr[2]), "+v"(xr[3]) :: "memory");
    STAMP(1);
    f32x16 t[4];
#pragma unroll
    for (int i = 0; i < 4; i++) t[i] = zero16();
#pragma unroll
    for (int s = 0; s < 7; s++) {
      const u32x4 f0 = tr_frag(lds + sl + t0 + s * 2048, 512), f1 = tr_frag(lds + sl + t1 + s * 2048, 512);
      t[0] = mfma(f0, af[s][0], t[0]); t[1] = mfma(f1, af[s][0], t[1]);
      t[2] = mfma(f0, af[s][1], t[2]); t[3] = mfma(f1, af[s][1], t[3]);
    }
    asm volatile("s_waitcnt lgkmcnt(0)" ::: "memory");
#pragma unroll
    for (int i = 1; i < 4; i++) tile_put(lds + sl + (i - 1) * 4096, t[i], lane);
    eb += 4; group_sync(sy + SY_B, eb, lane);
#pragma unroll
    for (int i = 1; i < 4; i++) tile_add(lds + K2_B_OFF + 57344 + (w ^ i) * 14336 + (i - 1) * 4096, t[0], lane);
    float s1 = 0.f, s2 = 0.f;
#pragma unroll
    for (int g = 0; g < 4; g++) {
      const float a0 = __builtin_bit_cast(float, xr[g][0] << 16) + t[0][4 * g], a1 = __builtin_bit_cast(float, xr[g][0] & 0xffff0000u) + t[0][4 * g + 1];
      const float a2 = __builtin_bit_cast(float, xr[g][1] << 16) + t[0][4 * g + 2], a3 = __builtin_bit_cast(float, xr[g][1] & 0xffff0000u) + t[0][4 * g + 3];
      x1p[2 * g] = pack2(a0, a1); x1p[2 * g + 1] = pack2(a2, a3);
#pragma unroll
      for (int e = 0; e < 2; e++) {
        const float lo = __builtin_bit_cast(float, x1p[2 * g + e] << 16), hi = __builtin_bit_cast(float, x1p[2 * g + e] & 0xffff0000u);
        s1 += lo + hi; s2 = __builtin_fmaf(lo, lo, s2); s2 = __builtin_fmaf(hi, hi, s2);
      }
    }
    s1 += other_half(s1, hh); s2 += other_half(s2, hh);
    const __amdgpu_buffer_rsrc_t rx = __builtin_amdgcn_make_buffer_rsrc(ws + WS_X1, 0, TBYTES, 0x00020000);
    const int tile = tb64 * 2 + tj;
#pragma unroll
    for (int g = 0; g < 4; g++) { u32x2 v; v[0] = x1p[2 * g]; v[1] = x1p[2 * g + 1]; st8_sc1(v, rx, (unsigned)(tile * 56 + nb * 8 + ni * 4 + g) * 512 + r * 16 + hh * 8); }
    const __amdgpu_buffer_rsrc_t rs = __builtin_amdgcn_make_buffer_rsrc(ws + WS_STATS, 0, 64 * 14 * 32 * 8, 0x00020000);
    if (hh == 0) { u32x2 v; v[0] = __builtin_bit_cast(unsigned, s1); v[1] = __builtin_bit_cast(unsigned, s2); st8_sc1(v, rs, (unsigned)((tile * 14 + nb * 2 + ni) * 32 + r) * 8); }
    publish(sy + SY_PUBB, cnt_x1 + (tb64 >> 1) * 64, lane);
    STAMP(2);
  }
  {
    eb += 4; group_sync(sy + SY_B, eb, lane);
#pragma unroll
    for (int j = 0; j < 14; j++) { const int row0 = 224 + 4 * (14 * w + j); dma16(dv3, w3src + (size_t)row0 * 3584, lds0 + K2_B_OFF + row0 * 256); }
    asm volatile("s_waitcnt vmcnt(0)" ::: "memory");
    eb += 4; group_sync(sy + SY_B, eb, lane);
    unsigned tb[4];
    {
      const int blk = (lane >> 4) & 1, q = (lane & 15) >> 2, p = lane & 3;
#pragma unroll
      for (int n = 0; n < 4; n++) tb[n] = K2_B_OFF + (8 * hh + q) * 256 + ((n ^ q) * 64) + blk * 32 + p * 8;
    }
    {
      u32x4 ones; ones[0] = ones[1] = ones[2] = ones[3] = 0x3F803F80u;
#pragma unroll
      for (int n = 0; n < 4; n++) {
        f32x16 cs = zero16();
#pragma unroll
        for (int s = 0; s < 7; s++) cs = mfma(ones, tr_frag(lds + tb[n] + w * (7 * 4096) + s * 4096, 1024), cs);
        if (hh == 0) *(LAS float*)(lds + K2_CSP_OFF + (w * 128 + n * 32 + r) * 4) = cs[0];
      }
      eb += 4; group_sync(sy + SY_B, eb, lane);
      if (hh == 0) {
        const LAS float* cp = (const LAS float*)(lds + K2_CSP_OFF) + w * 32 + r;
        *(LAS float*)(lds + K2_CS_OFF + (w * 32 + r) * 4) = (cp[0] + cp[128]) + (cp[256] + cp[384]);
      }
      eb += 4; group_sync(sy + SY_B, eb, lane);
    }
    STAMP(3);
    wait_ready(sy + SY_RDY_X1, cnt_x1 + tb128 * 64, 14u, w == 0, lane);
    STAMP(4);
    const int tile = tb128 * 4 + w;
    const __amdgpu_buffer_rsrc_t rx = __builtin_amdgcn_make_buffer_rsrc(ws + WS_X1, 0, TBYTES, 0x00020000);
    const __amdgpu_buffer_rsrc_t rs = __builtin_amdgcn_make_buffer_rsrc(ws + WS_STATS, 0, 64 * 14 * 32 * 8, 0x00020000);
    u32x4 af[28];
#pragma unroll
    for (int s = 0; s < 28; s++) af[s] = __builtin_amdgcn_raw_buffer_load_b128(rx, (unsigned)(tile * 56 + 2 * s) * 512 + lane * 16, 0, 16);
    float s1 = 0.f, s2 = 0.f;
#pragma unroll
    for (int j = 0; j < 7; j++) {
      u32x2 v = __builtin_amdgcn_raw_buffer_load_b64(rs, (unsigned)((tile * 14 + hh * 7 + j) * 32 + r) * 8, 0, 16);
      const unsigned u0 = v[0], u1 = v[1];
      s1 += __builtin_bit_cast(float, u0); s2 += __builtin_bit_cast(float, u1);
    }
    s1 += other_half(s1, hh); s2 += other_half(s2, hh);
    const float mean = s1 * (1.0f / 448.0f);
    const float rstd = __builtin_amdgcn_rsqf(fmaxf(s2 * (1.0f / 448.0f) - mean * mean, 0.f) + 1e-5f);
    const __amdgpu_buffer_rsrc_t r3 = __builtin_amdgcn_make_buffer_rsrc(ws + WS_H3, 0, T3BYTES, 0x00020000);
#pragma unroll
    for (int n = 0; n < 4; n++) {
      f32x16 acc = zero16();
#pragma unroll
      for (int s = 0; s < 28; s++) acc = mfma(tr_frag(lds + tb[n] + s * 4096, 1024), af[s], acc);
      unsigned pk[8];
#pragma unroll
      for (int g = 0; g < 4; g++) {
        const f32x4 c = *(const LAS f32x4*)(lds + K2_CS_OFF + (n * 32 + 8 * g + 4 * hh) * 4);
        float y[4];
#pragma unroll
        for (int i = 0; i < 4; i++) y[i] = gelu_f(rstd * __builtin_fmaf(-mean, c[i], acc[4 * g + i]));
        pk[2 * g] = pack2(y[0], y[1]); pk[2 * g + 1] = pack2(y[2], y[3]);
      }
#pragma unroll
      for (int gp = 0; gp < 2; gp++) {
        auto a = __builtin_amdgcn_permlane32_swap(pk[4 * gp], pk[4 * gp + 2], false, false);
        auto c = __builtin_amdgcn_permlane32_swap(pk[4 * gp + 1], pk[4 * gp + 3], false, false);
        u32x4 v; v[0] = a[0]; v[1] = c[0]; v[2] = a[1]; v[3] = c[1];
        st16_sc1(v, r3, (unsigned)(tile * 224 + nb3 * 16 + n * 4 + 2 * gp + hh) * 512 + r * 16);
      }
    }
    publish(sy + SY_PUBB, cnt_h3 + tb128 * 64, lane);
    STAMP(5);
  }
  {
    eb += 4; group_sync(sy + SY_B, eb, lane);
    const unsigned ring = K2_B_OFF + w * 28672;
    const char* wsrc = w4 + (size_t)(448 * w) * 896 + nb * 128;
#pragma unroll
    for (int j = 0; j < 28; j++) dma16(dv64, wsrc + j * (8 * 896), lds0 + ring + j * 1024);
    const float gate = *(const float*)(ws + WS_GATE + ((tb64 * 2 + tj) * 32 + r) * 4);
    wait_ready(sy + SY_RDY_H3, cnt_h3 + (tb64 >> 1) * 64, 14u, w == 0, lane);
    asm volatile("s_waitcnt vmcnt(0)" ::: "memory");
    STAMP(6);
    const u32x4 r3 = mkrsrc(ws + WS_H3, T3BYTES);
    unsigned av[2];
#pragma unroll
    for (int e = 0; e < 2; e++) av[e] = (unsigned)((tb64 * 2 + (tj ^ e)) * 224 + 56 * w) * 512 + lane * 16;
    u32x4 af[14][2];
#pragma unroll
    for (int s = 0; s < 14; s++)
#pragma unroll
      for (int e = 0; e < 2; e++) BLOAD_SC1(af[s][e], av[e], r3, (s >> 2) * 4096, (s & 3) * 1024);
    f32x16 t[4];
#pragma unroll
    for (int i = 0; i < 4; i++) t[i] = zero16();
#pragma unroll
    for (int s = 0; s < 14; s++) {
      asm volatile("s_waitcnt vmcnt(%2)" : "+v"(af[s][0]), "+v"(af[s][1]) : "i"(2 * (13 - s) + 4 * s) : "memory");
      const u32x4 f0 = tr_frag(lds + ring + t0 + s * 2048, 512), f1 = tr_frag(lds + ring + t1 + s * 2048, 512);
      t[0] = mfma(f0, af[s][0], t[0]); t[1] = mfma(f1, af[s][0], t[1]);
      t[2] = mfma(f0, af[s][1], t[2]); t[3] = mfma(f1, af[s][1], t[3]);
      asm volatile("s_waitcnt lgkmcnt(0)" ::: "memory");
      dma16(dv64, wsrc + (28 + 2 * s) * (8 * 896), lds0 + ring + (2 * s) * 1024);
      dma16(dv64, wsrc + (29 + 2 * s) * (8 * 896), lds0 + ring + (2 * s + 1) * 1024);
      BLOAD_SC1(af[s][0], av[0], r3, ((s + 14) >> 2) * 4096, ((s + 14) & 3) * 1024);
      BLOAD_SC1(af[s][1], av[1], r3, ((s + 14) >> 2) * 4096, ((s + 14) & 3) * 1024);
    }
#pragma unroll
    for (int s = 0; s < 14; s++) {
      asm volatile("s_waitcnt vmcnt(%2)" : "+v"(af[s][0]), "+v"(af[s][1]) : "i"(4 * (13 - s)) : "memory");
      const u32x4 f0 = tr_frag(lds + ring + t0 + s * 2048, 512), f1 = tr_frag(lds + ring + t1 + s * 2048, 512);
      t[0] = mfma(f0, af[s][0], t[0]); t[1] = mfma(f1, af[s][0], t[1]);
      t[2] = mfma(f0, af[s][1], t[2]); t[3] = mfma(f1, af[s][1], t[3]);
    }
    STAMP(7);
    asm volatile("s_waitcnt lgkmcnt(0)" ::: "memory");
#pragma unroll
    for (int i = 1; i < 4; i++) tile_put(lds + ring + (i - 1) * 4096, t[i], lane);
    eb += 4; group_sync(sy + SY_B, eb, lane);
#pragma unroll
    for (int i = 1; i < 4; i++) tile_add(lds + K2_B_OFF + (w ^ i) * 28672 + (i - 1) * 4096, t[0], lane);
    for (unsigned s = 0; (unsigned)__builtin_amdgcn_readfirstlane(lds_ld(sy + SY_AFIN)) < 4u && s < SPIN_CAP; s++) __builtin_amdgcn_s_sleep(1);
    asm volatile("" ::: "memory");
    STAMP(8);
    f32x16 a = zero16();
    tile_add(lds + K2_A_OFF + 16384 + w * 4096, a, lane);
    unsigned pk[8];
    const float og = 1.0f - gate;
#pragma unroll
    for (int j = 0; j < 8; j++) {
      const float b0 = __builtin_bit_cast(float, x1p[j] << 16) + t[0][2 * j], b1 = __builtin_bit_cast(float, x1p[j] & 0xffff0000u) + t[0][2 * j + 1];
      pk[j] = pack2(gate * a[2 * j] + og * b0, gate * a[2 * j + 1] + og * b1);
    }
    char* op = out + (size_t)((tb64 * 2 + tj) * 32 + r) * 896 + (nb * 64 + ni * 32) * 2 + hh * 16;
#pragma unroll
    for (int gp = 0; gp < 2; gp++) {
      auto a2 = __builtin_amdgcn_permlane32_swap(pk[4 * gp], pk[4 * gp + 2], false, false);
      auto c2 = __builtin_amdgcn_permlane32_swap(pk[4 * gp + 1], pk[4 * gp + 3], false, false);
      u32x4 v; v[0] = a2[0]; v[1] = c2[0]; v[2] = a2[1]; v[3] = c2[1];
      *(u32x4*)(op + gp * 32) = v;
    }
    STAMP(9);
    STAMP_FLUSH(256 + blockIdx.x, 10);
  }
}

extern "C" void kernel_launch(void* const* d_in, const int* in_sizes, int n_in,
                              void* d_out, int out_size, void* d_ws, size_t ws_size,
                              hipStream_t stream) {
  const char* x = (const char*)d_in[0]; const char* wqkv = (const char*)d_in[1]; const char* wo = (const char*)d_in[2];
  const char* w1 = (const char*)d_in[4]; const char* w2 = (const char*)d_in[5]; const char* w3 = (const char*)d_in[6];
  const char* w4 = (const char*)d_in[7]; const char* gw = (const char*)d_in[8];
  (void)hipFuncSetAttribute((const void*)k1_kernel, hipFuncAttributeMaxDynamicSharedMemorySize, K1_LDS);
  (void)hipFuncSetAttribute((const void*)k2_kernel, hipFuncAttributeMaxDynamicSharedMemorySize, K2_LDS);
  k1_kernel<<<256, 768, K1_LDS, stream>>>(x, wqkv, w1, gw, (char*)d_ws);
  k2_kernel<<<256, 512, K2_LDS, stream>>>(x, wo, w2, w3, w4, (char*)d_ws, (char*)d_out);
}
```

```cpp
#include <hip/hip_runtime.h>
#include <stdint.h>

#define TOUCH 3
#define PL 2
#define LDPRIO

typedef __attribute__((ext_vector_type(8))) __bf16 bf16x8;
typedef __attribute__((ext_vector_type(2))) __bf16 bf16x2;
typedef __attribute__((ext_vector_type(4))) short s16x4;
typedef __attribute__((ext_vector_type(16))) float f32x16;
typedef __attribute__((ext_vector_type(4))) float f32x4;
typedef __attribute__((ext_vector_type(2))) float f32x2;
typedef __attribute__((ext_vector_type(4))) unsigned u32x4;
typedef __attribute__((ext_vector_type(2))) unsigned u32x2;
#define LAS __attribute__((address_space(3)))
#define GAS __attribute__((address_space(1)))
#define DI __device__ __forceinline__

constexpr int SEQ = 2048, DM = 448, NH = 16, DH = 28, FF = 1792;

constexpr size_t WS_CNT   = 0;
constexpr size_t WS_GATE  = 16384;
constexpr size_t WS_STATS = 32768;
constexpr size_t WS_Q     = 1u << 20;
constexpr size_t WS_K     = WS_Q + (2u << 20);
constexpr size_t WS_V     = WS_K + (2u << 20);
constexpr size_t WS_H     = WS_V + (2u << 20);
constexpr size_t WS_X1    = WS_H + (2u << 20);
constexpr size_t WS_O     = WS_X1 + (2u << 20);
constexpr size_t WS_H3    = WS_O + (2u << 20);
constexpr unsigned TBYTES = 64u * 56u * 512u;
constexpr unsigned T3BYTES = 64u * 224u * 512u;

DI f32x16 mfma(u32x4 a, u32x4 b, f32x16 c) {
  return __builtin_amdgcn_mfma_f32_32x32x16_bf16(__builtin_bit_cast(bf16x8, a), __builtin_bit_cast(bf16x8, b), c, 0, 0, 0);
}
DI unsigned pack2(float a, float b) {
  f32x2 v; v[0] = a; v[1] = b;
  return __builtin_bit_cast(unsigned, __builtin_convertvector(v, bf16x2));
}
DI u32x4 tr_frag(const LAS char* p, int hi_off) {
  s16x4 lo = __builtin_amdgcn_ds_read_tr16_b64_v4i16((LAS s16x4*)p);
  s16x4 hi = __builtin_amdgcn_ds_read_tr16_b64_v4i16((LAS s16x4*)(p + hi_off));
  u32x2 a = __builtin_bit_cast(u32x2, lo), b = __builtin_bit_cast(u32x2, hi);
  u32x4 r; r[0] = a[0]; r[1] = a[1]; r[2] = b[0]; r[3] = b[1];
  return r;
}
DI float gelu_f(float x) {
  const float C0 = -2.302208198f;
  const float C1 = -0.1029432397f;
  float z = x * __builtin_fmaf(x * x, C1, C0);
  float e = __builtin_amdgcn_exp2f(z);
  return x * __builtin_amdgcn_rcpf(1.0f + e);
}
DI float other_half(float v, int hh) {
  unsigned u = __builtin_bit_cast(unsigned, v);
  auto r = __builtin_amdgcn_permlane32_swap(u, u, false, false);
  return __builtin_bit_cast(float, hh ? r[0] : r[1]);
}
DI void sq2(unsigned v, float& a, float& b) {
  const float lo = __builtin_bit_cast(float, v << 16), hi = __builtin_bit_cast(float, v & 0xffff0000u);
  a = __builtin_fmaf(lo, lo, a); b = __builtin_fmaf(hi, hi, b);
}
#define STAMP(i)
#define STAMP_DECL(base)
#define STAMP_FLUSH(base, n)
DI f32x16 zero16() { f32x16 z; for (int i = 0; i < 16; i++) z[i] = 0.f; return z; }

DI void dma16_masked(unsigned voff, const void* sbase, unsigned ldsdst, uint64_t mask) {
  unsigned keep;
  asm volatile("s_mov_b32 %0, m0\n\ts_mov_b32 m0, %3\n\ts_mov_b64 exec, %4\n\tglobal_load_lds_dwordx4 %1, %2\n\ts_mov_b64 exec, -1\n\ts_mov_b32 m0, %0"
               : "=&s"(keep) : "v"(voff), "s"(sbase), "s"(ldsdst), "s"(mask) : "memory");
}
DI void dma16(unsigned voff, const void* sbase, unsigned ldsdst) {
  unsigned keep;
  asm volatile("s_mov_b32 %0, m0\n\ts_mov_b32 m0, %3\n\ts_nop 0\n\tglobal_load_lds_dwordx4 %1, %2\n\ts_mov_b32 m0, %0"
               : "=&s"(keep) : "v"(voff), "s"(sbase), "s"(ldsdst) : "memory");
}

constexpr int K1_SLOT    = 32768;
constexpr int K1_AUX_OFF = 4 * K1_SLOT;
constexpr int K1_CS_OFF  = K1_AUX_OFF + 3584;
constexpr int K1_SQ_OFF  = K1_CS_OFF + 2048;
constexpr int K1_LDS     = K1_SQ_OFF + 1024;

DI u32x4 tr_frag2(const LAS char* p0, const LAS char* p1) {
  s16x4 lo = __builtin_amdgcn_ds_read_tr16_b64_v4i16((LAS s16x4*)p0);
  s16x4 hi = __builtin_amdgcn_ds_read_tr16_b64_v4i16((LAS s16x4*)p1);
  u32x2 a = __builtin_bit_cast(u32x2, lo), b = __builtin_bit_cast(u32x2, hi);
  u32x4 r; r[0] = a[0]; r[1] = a[1]; r[2] = b[0]; r[3] = b[1];
  return r;
}
DI unsigned k1_tr_off(int lane, int t) {
  const int hh = lane >> 5, blk = (lane >> 4) & 1, q = (lane & 15) >> 2, p = lane & 3;
  int u = 7 * t + 4 * blk + p;
  if (blk == 1 && p == 3) u = 7 * t;
  return (8 * hh + q) * 256 + (((u >> 1) ^ (q << 2)) * 16) + (u & 1) * 8;
}

template <int KIND>
DI void k1_compute(LAS char* lds, char* __restrict__ ws, const int type, const int sub, const int tg, const int wv, const int stbase_) {
  const int tid = threadIdx.x, lane = tid & 63, w = wv & 3, np = (wv - 4) >> 2;
  const int r = lane & 31, hh = lane >> 5;
  const int tile = tg * 4 + w;
  unsigned xb[4];
#pragma unroll
  for (int ks = 0; ks < 4; ks++) xb[ks] = 16384 + w * 4096 + r * 128 + (((2 * ks + hh) ^ ((r >> 1) & 7)) * 16);
  const unsigned wb0 = k1_tr_off(lane, 2 * np), wb1 = k1_tr_off(lane, 2 * np + 1);
  const bool isaux = ((lane >> 4) & 1) == 1 && (lane & 3) == 3;
  const unsigned ab = isaux ? K1_AUX_OFF + (8 * hh + ((lane & 15) >> 2)) * 8 : wb1;
  const unsigned kstep = isaux ? 128u : 4096u, hoff = isaux ? 32u : 1024u;
  const unsigned sstep = isaux ? 512u : (unsigned)K1_SLOT, wrapst = isaux ? 512u : (unsigned)(-3 * K1_SLOT);
  unsigned a3[4];
#pragma unroll
  for (int ks = 0; ks < 4; ks++) a3[ks] = ab + ks * kstep;
  f32x16 acc[2]; acc[0] = zero16(); acc[1] = zero16();
  u32x4 wf[2][4][2], xf[2][4];
  float sqa[4] = {0.f, 0.f, 0.f, 0.f};
  auto read_stage = [&](int st) {
    const int so = (st & 3) * K1_SLOT;
#pragma unroll
    for (int ks = 0; ks < 4; ks++) {
      xf[st & 1][ks] = *(const LAS u32x4*)(lds + xb[ks] + so);
      wf[st & 1][ks][0] = tr_frag(lds + wb0 + so + ks * 4096, 1024);
      if (KIND == 2) { wf[st & 1][ks][1] = tr_frag2(lds + a3[ks], lds + a3[ks] + hoff); a3[ks] += (st == 3) ? wrapst : sstep; }
      else wf[st & 1][ks][1] = tr_frag(lds + wb1 + so + ks * 4096, 1024);
    }
  };
  asm volatile("s_waitcnt lgkmcnt(0)\n\ts_barrier" ::: "memory");
  STAMP(2);
  read_stage(0);
#pragma unroll
  for (int st = 0; st < 7; st++) {
    __builtin_amdgcn_sched_barrier(0);
    if (st < 6) { asm volatile("s_waitcnt lgkmcnt(0)\n\ts_barrier" ::: "memory"); STAMP(3 + st); read_stage(st + 1); }
#pragma unroll
    for (int ks = 0; ks < 4; ks++) {
      acc[0] = KIND == 1 ? mfma(xf[st & 1][ks], wf[st & 1][ks][0], acc[0]) : mfma(wf[st & 1][ks][0], xf[st & 1][ks], acc[0]);
      if (KIND == 2) sq2(np ? xf[st & 1][ks][2] : xf[st & 1][ks][0], sqa[0], sqa[1]);
      acc[1] = KIND == 1 ? mfma(xf[st & 1][ks], wf[st & 1][ks][1], acc[1]) : mfma(wf[st & 1][ks][1], xf[st & 1][ks], acc[1]);
      if (KIND == 2) sq2(np ? xf[st & 1][ks][3] : xf[st & 1][ks][1], sqa[2], sqa[3]);
    }
    if (KIND == 2) asm volatile("" : "+v"(sqa[0]), "+v"(sqa[1]), "+v"(sqa[2]), "+v"(sqa[3]));
#pragma unroll
    for (int i = 0; i < 8; i++) {
      __builtin_amdgcn_sched_group_barrier(0x8, 1, 0);
      __builtin_amdgcn_sched_group_barrier(0x100, 3, 0);
      __builtin_amdgcn_sched_group_barrier(0x2, KIND == 2 ? 6 : 2, 0);
    }
  }
  __builtin_amdgcn_sched_barrier(0);
  STAMP(9);
  if (KIND < 2) {
    const float sc = type == 0 ? 0.27264314f : 1.0f;
    char* dst = ws + WS_Q + (size_t)type * (2u << 20);
#pragma unroll
    for (int j = 0; j < 2; j++) {
      const int head = sub * 4 + 2 * np + j;
      u32x4 f0, f1;
#pragma unroll
      for (int d = 0; d < 4; d++) {
        f0[d] = pack2(acc[j][2 * d] * sc, acc[j][2 * d + 1] * sc);
        f1[d] = pack2(acc[j][8 + 2 * d] * sc, acc[j][8 + 2 * d + 1] * sc);
      }
      if (KIND == 1) {
        const unsigned v = r == 28 ? 0x3F803F80u : 0u;
#pragma unroll
        for (int d = 0; d < 4; d++) { f0[d] = r >= 28 ? v : f0[d]; f1[d] = r >= 28 ? v : f1[d]; }
      } else {
        f1[2] = hh ? (type == 1 ? 0x3F803F80u : 0u) : f1[2];
        f1[3] = hh ? (type == 1 ? 0x00003F80u : 0u) : f1[3];
      }
      u32x4* o = (u32x4*)(dst + (size_t)(head * 64 + tile) * 2048 + lane * 16);
      o[0] = f0; o[64] = f1;
    }
  } else {
    float sq = (sqa[0] + sqa[1]) + (sqa[2] + sqa[3]);
    sq += other_half(sq, hh);
    if (hh == 0) *(LAS float*)(lds + K1_SQ_OFF + ((np * 4 + w) * 32 + r) * 4) = sq;
    if (sub == 0 && np == 0 && hh == 1) {
      float gt = __builtin_amdgcn_rcpf(1.0f + __builtin_amdgcn_exp2f(-1.442695041f * acc[1][13]));
      *(float*)(ws + WS_GATE + (tile * 32 + r) * 4) = gt;
    }
    float sx = acc[1][12];
    { float o = other_half(sx, hh); sx = hh ? sx : o; }
    const float mean = sx * (1.0f / 448.0f);
    asm volatile("s_waitcnt lgkmcnt(0)\n\ts_barrier" ::: "memory");
    sq += *(LAS float*)(lds + K1_SQ_OFF + (((np ^ 1) * 4 + w) * 32 + r) * 4);
    const float var = fmaxf(sq * (1.0f / 448.0f) - mean * mean, 0.f) + 1e-5f;
    const float rstd = __builtin_amdgcn_rsqf(var);
    const float ga = -2.302208198f * rstd, gb = -0.1029432397f * rstd * rstd * rstd, irs = var * rstd;
    char* dst = ws + WS_H + (size_t)tile * (56 * 512) + r * 16;
#pragma unroll
    for (int j = 0; j < 2; j++) {
      const int t = 2 * np + j;
#pragma unroll
      for (int g = 0; g < 4; g++) {
        const LAS char* cp = lds + K1_CS_OFF + (t * 32 + 8 * g + 4 * hh) * 4;
        const f32x4 c = (*(const LAS f32x4*)cp + *(const LAS f32x4*)(cp + 512)) + (*(const LAS f32x4*)(cp + 1024) + *(const LAS f32x4*)(cp + 1536));
        float y[4];
#pragma unroll
        for (int i = 0; i < 4; i++) {
          const float tt = __builtin_fmaf(-mean, c[i], acc[j][4 * g + i]);
          const float e = __builtin_amdgcn_exp2f(tt * __builtin_fmaf(tt * tt, gb, ga));
          y[i] = tt * __builtin_amdgcn_rcpf(__builtin_fmaf(e, irs, irs));
        }
        u32x2 o; o[0] = pack2(y[0], y[1]); o[1] = pack2(y[2], y[3]);
        const int u = 28 * sub + 7 * t + 2 * g + hh;
        if (g < 3 || hh == 0) *(u32x2*)(dst + (u >> 1) * 512 + (u & 1) * 8) = o;
      }
    }
  }
  STAMP(10);
}

__global__ __launch_bounds__(768, 3) void k1_kernel(const char* __restrict__ x, const char* __restrict__ wqkv,
                                                    const char* __restrict__ w1, const char* __restrict__ gate_w,
                                                    char* __restrict__ ws, const char* __restrict__ wo,
                                                    const char* __restrict__ w2, const char* __restrict__ w3,
                                                    const char* __restrict__ w4) {
  extern __shared__ __attribute__((aligned(16))) char smem[];
  LAS char* lds = (LAS char*)smem;
  const unsigned lds0 = (unsigned)(uintptr_t)lds;
  const int tid = threadIdx.x, lane = tid & 63;
  const int wv = __builtin_amdgcn_readfirstlane(tid >> 6);
  const int w = wv & 3;
  const int b = blockIdx.x, xcd = b & 7, bi = b >> 3;
  const int type = bi & 3, sub = xcd & 3;
  const int tg = (xcd >> 2) * 8 + (bi >> 2);
  STAMP_DECL(blockIdx.x) STAMP(0);

  if (wv < 4) {
    const char* wsrc = type < 3 ? wqkv + (type * 448 + sub * 112) * 2 : w1 + sub * 224;
    const int ldw = type < 3 ? 2688 : 896;
    const int drow = lane >> 4, dlc = (lane & 15) ^ (drow << 2);
    const unsigned dvoff = drow * ldw + (dlc < 14 ? dlc : 0) * 16;
    const uint64_t dmask = ~((3ull << 14) | (3ull << (16 + 10)) | (3ull << (32 + 6)) | (3ull << (48 + 2)));
    unsigned xv[2];
    {
      const int rin = lane >> 3, pc = lane & 7;
#pragma unroll
      for (int e = 0; e < 2; e++) xv[e] = rin * 896 + ((pc ^ ((4 * e + (rin >> 1)) & 7)) * 16);
    }
    const char* xbase = x + (size_t)(tg * 128 + w * 32) * 896;
    const char* wbase = wsrc + (size_t)(16 * w) * ldw;
    auto issue_stage = [&](int st) {
      const unsigned slot = lds0 + (st & 3) * K1_SLOT;
#pragma unroll
      for (int jj = 0; jj < 4; jj++) {
        dma16_masked(dvoff, wbase + (size_t)(64 * st + 4 * jj) * ldw, slot + (16 * w + 4 * jj) * 256, dmask);
        dma16(xv[jj & 1], xbase + jj * (8 * 896) + st * 128, slot + 16384 + w * 4096 + jj * 1024);
      }
    };
    issue_stage(0); issue_stage(1);
    STAMP(1);
    f32x16 cs[4];
#pragma unroll
    for (int n = 0; n < 4; n++) cs[n] = zero16();
    u32x4 ones; ones[0] = ones[1] = ones[2] = ones[3] = 0x3F803F80u;
    unsigned cb[4], sinkl = 0;
#pragma unroll
    for (int n = 0; n < 4; n++) cb[n] = k1_tr_off(lane, n) + w * 4096;
#pragma unroll
    for (int st = 0; st < 7; st++) {
      if (st < 6) asm volatile("s_waitcnt vmcnt(8) lgkmcnt(0)\n\ts_barrier" ::: "memory");
      else        asm volatile("s_waitcnt vmcnt(1) lgkmcnt(0)\n\ts_barrier" ::: "memory");
      if (st + 2 < 7) issue_stage(st + 2);
      if (st == 5) {
        int k = b * 123 + (tid < 123 ? tid : 0); k = k < 31360 ? k : 31359;
        const char* p2 = k < 3136 ? wo + (size_t)k * 128 : k < 6272 ? w2 + (size_t)(k - 3136) * 128 : k < 18816 ? w3 + (size_t)(k - 6272) * 128 : w4 + (size_t)(k - 18816) * 128;
        asm volatile("global_load_dword %0, %1, off" : "=&v"(sinkl) : "v"(p2) : "memory");
      }
      if (type == 3) {
#pragma unroll
        for (int n = 0; n < 4; n++) cs[n] = mfma(ones, tr_frag(lds + cb[n] + (st & 3) * K1_SLOT, 1024), cs[n]);
      }
    }
    if (b < 4) *(u32x4*)(ws + WS_CNT + b * 4096 + tid * 16) = (u32x4){0u, 0u, 0u, 0u};
    asm volatile("s_waitcnt vmcnt(0)" : "+v"(sinkl) :: "memory");
    if (type == 3) {
      if (lane < 32) {
#pragma unroll
        for (int n = 0; n < 4; n++) *(LAS float*)(lds + K1_CS_OFF + (w * 128 + n * 32 + lane) * 4) = cs[n][0];
      }
      asm volatile("s_waitcnt lgkmcnt(0)\n\ts_barrier" ::: "memory");
    }
    STAMP(2);
    return;
  }

  unsigned sink = 0;
  if (wv < 8) {
    if (type == 3) {
      const int t2 = tid - 256;
      const unsigned short* gwp = (const unsigned short*)gate_w;
      const int k1i = t2 < 192 ? t2 + 256 : 447;
      u32x2 a; a[0] = 0x3F80u | ((unsigned)gwp[t2] << 16); a[1] = 0u;
      u32x2 c; c[0] = 0x3F80u | ((unsigned)gwp[k1i] << 16); c[1] = 0u;
      *(LAS u32x2*)(lds + K1_AUX_OFF + t2 * 8) = a;
      *(LAS u32x2*)(lds + K1_AUX_OFF + k1i * 8) = c;
    }
  } else {
    const int c = tid - 512;
    const char* p0 = x + (size_t)((xcd >> 2) * 1024) * 896 + (size_t)(bi * 224 + (c < 224 ? c : 0)) * 128;
    const int i = bi * 168 + (c < 168 ? c : 0), row = i / 12, rem = i % 12, seg = rem / 3, ln = rem % 3;
    const char* p1 = (seg < 3 ? wqkv + (size_t)row * 2688 + seg * 896 : w1 + (size_t)row * 896) + ((sub * 224 + ln * 110) & ~3);
    int k = b * 123 + (c < 123 ? c : 0); k = k < 31360 ? k : 31359;
    const char* p2 = k < 3136 ? wo + (size_t)k * 128 : k < 6272 ? w2 + (size_t)(k - 3136) * 128 : k < 18816 ? w3 + (size_t)(k - 6272) * 128 : w4 + (size_t)(k - 18816) * 128;
  }
  const int sb_ = 0;
  if (type < 2)       k1_compute<0>(lds, ws, type, sub, tg, wv, sb_);
  else if (type == 2) k1_compute<1>(lds, ws, type, sub, tg, wv, sb_);
  else                k1_compute<2>(lds, ws, type, sub, tg, wv, sb_);
  asm volatile("s_waitcnt vmcnt(0)" : "+v"(sink) :: "memory");
  STAMP(11);
}

constexpr int K2_B_OFF   = 0;
constexpr int K2_A_OFF   = 114688;
constexpr int K2_CSP_OFF = K2_A_OFF + 32768;
constexpr int K2_CS_OFF  = K2_CSP_OFF + 2048;
constexpr int K2_M_OFF   = K2_CS_OFF + 512;
constexpr int K2_SY_OFF  = K2_M_OFF + 512;
constexpr int K2_LDS     = K2_SY_OFF + 64;
constexpr unsigned SPIN_CAP = 1u << 22;
#define PH 2
#define PG3 2
#define PRIO_G3 __builtin_amdgcn_s_setprio(PG3)
#define PRIO_HI __builtin_amdgcn_s_setprio(PH)
#define PRIO_LO __builtin_amdgcn_s_setprio(PL)
constexpr size_t WS_FX1 = WS_CNT, WS_FH3 = WS_CNT + 4096, WS_FO = WS_CNT + 8192, WS_FCS = WS_CNT + 12288;
constexpr size_t WS_CS3 = 24576;

DI void gsync(LAS unsigned* f, int w, unsigned epoch) {
  asm volatile("s_waitcnt lgkmcnt(0)" ::: "memory");
  __hip_atomic_store(f + w, epoch, __ATOMIC_RELAXED, __HIP_MEMORY_SCOPE_WORKGROUP);
  for (unsigned s = 0; s < SPIN_CAP; s++) {
    u32x4 v;
    asm volatile("ds_read_b128 %0, %1\n\ts_waitcnt lgkmcnt(0)" : "=v"(v) : "v"((unsigned)(uintptr_t)f) : "memory");
    const unsigned a = v[0], b = v[1], c = v[2], d = v[3];
    const unsigned m = min(min(a, b), min(c, d));
    if ((unsigned)__builtin_amdgcn_readfirstlane(m) >= epoch) break;
    __builtin_amdgcn_s_sleep(1);
  }
}
DI void flag_set(char* p, int lane) {
  if (lane == 0) __hip_atomic_store((GAS unsigned*)p, 1u, __ATOMIC_RELAXED, __HIP_MEMORY_SCOPE_AGENT);
}
DI void flags_wait(const char* p) {
  for (unsigned s = 0; s < SPIN_CAP; s++) {
    const unsigned v = __hip_atomic_load((const GAS unsigned*)p, __ATOMIC_RELAXED, __HIP_MEMORY_SCOPE_AGENT);
    if (__all(v != 0u)) break;
    __builtin_amdgcn_s_sleep(2);
  }
  asm volatile("" ::: "memory");
}
DI u32x4 mkrsrc(const void* p, unsigned bytes) {
  const uint64_t a = (uint64_t)(uintptr_t)p;
  u32x4 r; r[0] = (unsigned)a; r[1] = (unsigned)(a >> 32) & 0xffffu; r[2] = bytes; r[3] = 0x00020000u;
  return r;
}
#define BLOAD(dst, voff, rs, soff, imm) \
  asm volatile("buffer_load_dwordx4 %0, %1, %2, %3 offen offset:%4" : "=v"(dst) : "v"(voff), "s"(rs), "s"(soff), "i"(imm) : "memory")
#define BLOAD_SC1(dst, voff, rs, soff, imm) \
  asm volatile("buffer_load_dwordx4 %0, %1, %2, %3 offen offset:%4 sc1" : "=v"(dst) : "v"(voff), "s"(rs), "s"(soff), "i"(imm) : "memory")
DI void st16_sc1(u32x4 v, __amdgpu_buffer_rsrc_t rs, unsigned off) { __builtin_amdgcn_raw_buffer_store_b128(v, rs, off, 0, 16); }
DI void st8_sc1(u32x2 v, __amdgpu_buffer_rsrc_t rs, unsigned off) { __builtin_amdgcn_raw_buffer_store_b64(v, rs, off, 0, 16); }

DI void tile_put(LAS char* p, const f32x16& t, int lane) {
#pragma unroll
  for (int g = 0; g < 4; g++) { f32x4 v; v[0] = t[4 * g]; v[1] = t[4 * g + 1]; v[2] = t[4 * g + 2]; v[3] = t[4 * g + 3]; *(LAS f32x4*)(p + g * 1024 + lane * 16) = v; }
}
DI void tile_add(const LAS char* p, f32x16& t, int lane) {
#pragma unroll
  for (int g = 0; g < 4; g++) { f32x4 v = *(const LAS f32x4*)(p + g * 1024 + lane * 16); t[4 * g] += v[0]; t[4 * g + 1] += v[1]; t[4 * g + 2] += v[2]; t[4 * g + 3] += v[3]; }
}
DI void reduce_scatter(LAS char* buf, f32x16 (&t)[4], int w, int lane, int x0, int x1, LAS unsigned* f, unsigned& epoch) {
  tile_put(buf + (((w ^ x0) * 2 + 0) * 4096), t[2], lane);
  tile_put(buf + (((w ^ x1) * 2 + 1) * 4096), t[3], lane);
  gsync(f, w, ++epoch);
  tile_add(buf + (((w ^ 2 ^ x0) * 2 + 0) * 4096), t[0], lane);
  tile_add(buf + (((w ^ 2 ^ x1) * 2 + 1) * 4096), t[1], lane);
  asm volatile("s_waitcnt lgkmcnt(0)" ::: "memory");
  tile_put(buf + (((w ^ 2 ^ x0) * 2 + 0) * 4096), t[1], lane);
  gsync(f, w, ++epoch);
  tile_add(buf + (((w ^ 3 ^ x0) * 2 + 0) * 4096), t[0], lane);
  asm volatile("s_waitcnt lgkmcnt(0)" ::: "memory");
}

DI unsigned w64_dma_voff(int lane, unsigned ldw) { const int rin = lane >> 3, pc = lane & 7; return rin * ldw + ((pc ^ (((rin >> 1) & 1) << 2)) * 16); }
DI unsigned w64_tr_off(int lane, int nt) {
  const int hh = lane >> 5, blk = (lane >> 4) & 1, q = (lane & 15) >> 2, p = lane & 3;
  return (8 * hh + q) * 128 + ((nt ^ ((q >> 1) & 1)) * 64) + blk * 32 + p * 8;
}

#define ATT_EXP4(SC, B, P, D)                                                                                    \
    SC[B] = __builtin_amdgcn_exp2f(SC[B]); SC[B + 1] = __builtin_amdgcn_exp2f(SC[B + 1]);                       \
    SC[B + 2] = __builtin_amdgcn_exp2f(SC[B + 2]); SC[B + 3] = __builtin_amdgcn_exp2f(SC[B + 3]);               \
    P[D] = pack2(SC[B], SC[B + 1]); P[D + 1] = pack2(SC[B + 2], SC[B + 3]);                                     \
    __builtin_amdgcn_sched_barrier(0);
#define ATT_STEP(SC, SN, PC0, PC1, PP0, PP1, KN0, KN1, QN0, QN1, VP0, VP1, OP)                                  \
  {                                                                                                             \
    SN = mfma(KN0, QN0, zero16());                                                                              \
    __builtin_amdgcn_sched_barrier(0);                                                                          \
    ATT_EXP4(SC, 0, PC0, 0)                                                                                     \
    SN = mfma(KN1, QN1, SN);                                                                                    \
    __builtin_amdgcn_sched_barrier(0);                                                                          \
    ATT_EXP4(SC, 4, PC0, 2)                                                                                     \
    OP = mfma(VP0, PP0, OP);                                                                                    \
    __builtin_amdgcn_sched_barrier(0);                                                                          \
    ATT_EXP4(SC, 8, PC1, 0)                                                                                     \
    OP = mfma(VP1, PP1, OP);                                                                                    \
    __builtin_amdgcn_sched_barrier(0);                                                                          \
    ATT_EXP4(SC, 12, PC1, 2)                                                                                    \
  }

DI float max16(const f32x16& s) {
  float a = fmaxf(fmaxf(s[0], s[1]), s[2]), b = fmaxf(fmaxf(s[3], s[4]), s[5]), c = fmaxf(fmaxf(s[6], s[7]), s[8]);
  float d = fmaxf(fmaxf(s[9], s[10]), s[11]), e = fmaxf(fmaxf(s[12], s[13]), s[14]);
  return fmaxf(fmaxf(fmaxf(a, b), c), fmaxf(fmaxf(d, e), s[15]));
}

__global__ __launch_bounds__(512, 2) void k2_kernel(const char* __restrict__ x, const char* __restrict__ wo,
                                                    const char* __restrict__ w2, const char* __restrict__ w3,
                                                    const char* __restrict__ w4, char* __restrict__ ws,
                                                    char* __restrict__ out) {
  extern __shared__ __attribute__((aligned(16))) char smem[];
  LAS char* lds = (LAS char*)smem;
  const unsigned lds0 = (unsigned)(uintptr_t)lds;
  LAS unsigned* sy = (LAS unsigned*)(lds + K2_SY_OFF);
  const int tid = threadIdx.x, lane = tid & 63;
  const int wv = __builtin_amdgcn_readfirstlane(tid >> 6);
  const int w = wv & 3;
  const int b = blockIdx.x, xcd = b & 7, bi = b >> 3;
  const int r = lane & 31, hh = lane >> 5;
  const bool has_gemm = bi < 28;
  const int nb = bi % 7, tb64 = xcd * 4 + bi / 7;
  const int ni = w & 1, tj = w >> 1;
  STAMP_DECL(256 + blockIdx.x) STAMP(0);
  if (tid < 16) sy[tid] = 0u;
  asm volatile("s_waitcnt lgkmcnt(0)\n\ts_barrier" ::: "memory");

  if (wv < 4) {
    const int head = 2 * xcd + (bi >> 4), qb = bi & 15;
    const u32x4* qp = (const u32x4*)(ws + WS_Q + (size_t)(head * 64 + qb * 4) * 2048) + lane;
    const u32x4* kh = (const u32x4*)(ws + WS_K + (size_t)(head * 64) * 2048) + lane;
    const u32x4* vh = (const u32x4*)(ws + WS_V + (size_t)(head * 64) * 2048) + lane;
    const u32x4* kp = kh + w * 16 * 128;
    const u32x4* vp = vh + w * 16 * 128;
    unsigned ea = 0;
    __builtin_amdgcn_s_setprio(3);
    u32x4 q[4][2];
#pragma unroll
    for (int i = 0; i < 4; i++) { q[i][0] = qp[((w ^ i) * 2) * 64]; q[i][1] = qp[((w ^ i) * 2 + 1) * 64]; }
    u32x4 kb[4][2], vb[4][2];
#pragma unroll
    for (int j = 0; j < 3; j++) { kb[j][0] = kp[j * 128]; kb[j][1] = kp[j * 128 + 64]; vb[j][0] = vp[j * 128]; vb[j][1] = vp[j * 128 + 64]; }
    f32x16 o[4];
    {
      LAS float* mx = (LAS float*)(lds + K2_M_OFF);
      f32x16 s = mfma(kb[0][0], q[0][0], zero16());
      s = mfma(kb[0][1], q[0][1], s);
      const float m = max16(s);
      if (hh) mx[w * 32 + r] = m;
      gsync(sy, w, ++ea);
#pragma unroll
      for (int i = 0; i < 4; i++) {
        const float mm = mx[(w ^ i) * 32 + r];
        q[i][1][2] = hh ? (pack2(-mm, 0.f)) : q[i][1][2];
      }
    }
    STAMP(1);
    __builtin_amdgcn_s_setprio(0);
    {
#pragma unroll
      for (int i = 0; i < 4; i++) o[i] = zero16();
      f32x16 sA, sB;
      u32x4 pA0, pA1, pB0, pB1;
      sA = mfma(kb[0][0], q[0][0], zero16());
      sA = mfma(kb[0][1], q[0][1], sA);
      pB0 = pB1 = (u32x4){0u, 0u, 0u, 0u};
      vb[3][0] = vb[3][1] = (u32x4){0u, 0u, 0u, 0u};
#pragma unroll 1
      for (int kq = 0; kq < 4; kq++) {
#pragma unroll
        for (int j = 0; j < 4; j++) {
          const int kt = kq * 4 + j;
          const int jp = (j + 3) & 3, jn = (j + 1) & 3;
          ATT_STEP(sA, sB, pA0, pA1, pB0, pB1, kb[j][0], kb[j][1], q[1][0], q[1][1], vb[jp][0], vb[jp][1], o[3]);
          {
            const int kn = kt + 3 < 16 ? kt + 3 : 15;
            __builtin_amdgcn_s_setprio(3);
            kb[jp][0] = kp[kn * 128]; kb[jp][1] = kp[kn * 128 + 64]; vb[jp][0] = vp[kn * 128]; vb[jp][1] = vp[kn * 128 + 64];
            __builtin_amdgcn_sched_barrier(0);
            __builtin_amdgcn_s_setprio(0);
          }
          ATT_STEP(sB, sA, pB0, pB1, pA0, pA1, kb[j][0], kb[j][1], q[2][0], q[2][1], vb[j][0], vb[j][1], o[0]);
          ATT_STEP(sA, sB, pA0, pA1, pB0, pB1, kb[j][0], kb[j][1], q[3][0], q[3][1], vb[j][0], vb[j][1], o[1]);
          ATT_STEP(sB, sA, pB0, pB1, pA0, pA1, kb[jn][0], kb[jn][1], q[0][0], q[0][1], vb[j][0], vb[j][1], o[2]);
        }
      }
      o[3] = mfma(vb[3][0], pB0, o[3]);
      o[3] = mfma(vb[3][1], pB1, o[3]);
    }
    STAMP(2);
    __builtin_amdgcn_s_setprio(3);
    const float gate_q = *(const float*)(ws + WS_GATE + ((qb * 4 + w) * 32 + r) * 4);
    reduce_scatter(lds + K2_A_OFF, o, w, lane, 0, 0, sy, ea);
    float l = o[0][12];
    { float t = other_half(l, hh); l = hh ? l : t; }
    {
      float amax = 0.f;
#pragma unroll
      for (int i = 0; i < 12; i++) amax = fmaxf(amax, fabsf(o[0][i]));
      if (!hh) amax = fmaxf(fmaxf(amax, fabsf(o[0][12])), fmaxf(fabsf(o[0][13]), fmaxf(fabsf(o[0][14]), fabsf(o[0][15]))));
      const bool ok = (l > 1.0e-10f) && (l < 1.0e18f) && (amax < 1.0e37f);
      if (__any(!ok)) {
        u32x4 q0 = q[0][0], q1 = q[0][1];
        q1[2] = hh ? 0u : q1[2];
        float tm = -3.0e38f;
#pragma unroll 1
        for (int kt = 0; kt < 64; kt++) {
          f32x16 s = mfma(kh[kt * 128], q0, zero16());
          s = mfma(kh[kt * 128 + 64], q1, s);
          tm = fmaxf(tm, max16(s));
        }
        tm = -fmaxf(tm, other_half(tm, hh));
        const unsigned h0 = pack2(tm, 0.f) & 0xffffu; const float f0 = __builtin_bit_cast(float, h0 << 16);
        const unsigned h1 = pack2(tm - f0, 0.f) & 0xffffu; const float f1 = __builtin_bit_cast(float, h1 << 16);
        const unsigned h2 = pack2((tm - f0) - f1, 0.f) & 0xffffu;
        q1[2] = hh ? (h0 | (h1 << 16)) : q1[2];
        q1[3] = hh ? h2 : q1[3];
        f32x16 oo = zero16();
#pragma unroll 1
        for (int kt = 0; kt < 64; kt++) {
          f32x16 s = mfma(kh[kt * 128], q0, zero16());
          s = mfma(kh[kt * 128 + 64], q1, s);
          u32x4 p0, p1;
#pragma unroll
          for (int d = 0; d < 4; d++) {
            p0[d] = pack2(__builtin_amdgcn_exp2f(s[2 * d]), __builtin_amdgcn_exp2f(s[2 * d + 1]));
            p1[d] = pack2(__builtin_amdgcn_exp2f(s[8 + 2 * d]), __builtin_amdgcn_exp2f(s[8 + 2 * d + 1]));
          }
          oo = mfma(vh[kt * 128], p0, oo);
          oo = mfma(vh[kt * 128 + 64], p1, oo);
        }
        o[0] = oo;
        l = oo[12];
        { float t = other_half(l, hh); l = hh ? l : t; }
      }
    }
    {
      const __amdgpu_buffer_rsrc_t ro = __builtin_amdgcn_make_buffer_rsrc(ws + WS_O, 0, TBYTES, 0x00020000);
      const float inv = gate_q * __builtin_amdgcn_rcpf(l);
      const unsigned base = (unsigned)(qb * 4 + w) * (56 * 512) + r * 16;
#pragma unroll
      for (int g = 0; g < 4; g++) {
        u32x2 v; v[0] = pack2(o[0][4 * g] * inv, o[0][4 * g + 1] * inv); v[1] = pack2(o[0][4 * g + 2] * inv, o[0][4 * g + 3] * inv);
        const int u = 7 * head + 2 * g + hh;
        if (g < 3 || hh == 0) st8_sc1(v, ro, base + (u >> 1) * 512 + (u & 1) * 8);
      }
    }
    asm volatile("s_waitcnt vmcnt(0)" ::: "memory");
    flag_set(ws + WS_FO + ((qb * 4 + w) * 16 + head) * 4, lane);
    STAMP(3);
    return;
  }

  if (!has_gemm) {
    const int id = (xcd * 4 + bi - 28) * 2 + w;
    if (w < 2 && id < 56) {
      const unsigned reg = K2_B_OFF + w * 28672;
      const unsigned dv = (lane >> 2) * 3584 + (lane & 3) * 16;
      const char* src = w3 + id * 64;
#pragma unroll
      for (int j = 0; j < 28; j++) dma16(dv, src + (size_t)j * (16 * 3584), lds0 + reg + j * 1024);
      asm volatile("s_waitcnt vmcnt(0)" ::: "memory");
      const unsigned to = reg + (8 * hh + ((lane & 15) >> 2)) * 64 + ((lane >> 4) & 1) * 32 + (lane & 3) * 8;
      u32x4 ones; ones[0] = ones[1] = ones[2] = ones[3] = 0x3F803F80u;
      f32x16 cs = zero16();
#pragma unroll
      for (int s = 0; s < 28; s++) cs = mfma(ones, tr_frag(lds + to + s * 1024, 256), cs);
      if (hh == 0) __hip_atomic_store((GAS float*)(ws + WS_CS3) + id * 32 + r, cs[0], __ATOMIC_RELAXED, __HIP_MEMORY_SCOPE_AGENT);
      asm volatile("s_waitcnt vmcnt(0)" ::: "memory");
      flag_set(ws + WS_FCS + id * 4, lane);
    }
    return;
  }
  PRIO_LO;
  unsigned eb = 0;
  LAS unsigned* syb = sy + 4;
  const int nb3 = bi % 14, tb128 = xcd * 2 + bi / 14;
  const unsigned dv64 = w64_dma_voff(lane, 896);
  const unsigned t0 = w64_tr_off(lane, ni), t1 = w64_tr_off(lane, ni ^ 1);
  const unsigned dv3 = (lane >> 4) * 3584 + (((lane & 15) ^ ((lane >> 4) << 2)) * 16);
  const char* w3src = w3 + nb3 * 256;
  unsigned x1p[8];
  {
    const unsigned sl = K2_B_OFF + 57344 + w * 14336;
    const char* wsrc = w2 + (size_t)(112 * w) * 896 + nb * 128;
    const u32x4 rh = mkrsrc(ws + WS_H, TBYTES);
    const int delta = w == 0 ? -1 : w == 1 ? 1 : w == 2 ? -2 : 2;
    u32x4 af[7][2];
    unsigned av[2];
#pragma unroll
    for (int e = 0; e < 2; e++) {
      const int tok = (tb64 * 2 + (tj ^ e)) * 32 + r + delta;
      av[e] = (tok >= 0 && tok < SEQ) ? (unsigned)(((tok >> 5) * 56 + 14 * w + hh) * 512 + (tok & 31) * 16) : 0x80000000u;
    }
#pragma unroll
    for (int s = 0; s < 7; s++) {
      dma16(dv64, wsrc + (2 * s) * (8 * 896), lds0 + sl + (2 * s) * 1024);
      dma16(dv64, wsrc + (2 * s + 1) * (8 * 896), lds0 + sl + (2 * s + 1) * 1024);
      BLOAD(af[s][0], av[0], rh, (s >> 2) * 4096, (s & 3) * 1024);
      BLOAD(af[s][1], av[1], rh, (s >> 2) * 4096, (s & 3) * 1024);
    }
    u32x2 xr[4];
    {
      const char* xp = x + (size_t)((tb64 * 2 + tj) * 32) * 896 + (nb * 64 + ni * 32) * 2;
      const unsigned xo = r * 896 + hh * 8;
#pragma unroll
      for (int g = 0; g < 4; g++) asm volatile("global_load_dwordx2 %0, %1, %2 offset:%3" : "=v"(xr[g]) : "v"(xo), "s"(xp), "i"(g * 16) : "memory");
    }
    STAMP(10);
    PRIO_HI;
    f32x16 t[4];
#pragma unroll
    for (int i = 0; i < 4; i++) t[i] = zero16();
    u32x4 f0[2], f1[2];
    asm volatile("s_waitcnt vmcnt(28)" : "+v"(af[0][0]), "+v"(af[0][1]) :: "memory");
    STAMP(1);
    f0[0] = tr_frag(lds + sl + t0, 512); f1[0] = tr_frag(lds + sl + t1, 512);
#pragma unroll
    for (int s = 0; s < 7; s++) {
      if (s < 6) {
        asm volatile("s_waitcnt vmcnt(%2)" : "+v"(af[s + 1][0]), "+v"(af[s + 1][1]) : "i"(24 - 4 * s) : "memory");
        f0[(s + 1) & 1] = tr_frag(lds + sl + t0 + (s + 1) * 2048, 512); f1[(s + 1) & 1] = tr_frag(lds + sl + t1 + (s + 1) * 2048, 512);
      }
      t[0] = mfma(f0[s & 1], af[s][0], t[0]); t[1] = mfma(f1[s & 1], af[s][0], t[1]);
      t[2] = mfma(f0[s & 1], af[s][1], t[2]); t[3] = mfma(f1[s & 1], af[s][1], t[3]);
      __builtin_amdgcn_sched_barrier(0);
    }
    STAMP(11);
#pragma unroll
    for (int i = 1; i < 4; i++) tile_put(lds + K2_B_OFF + (w ^ i) * 14336 + (i - 1) * 4096, t[i], lane);
    gsync(syb, w, ++eb);
#pragma unroll
    for (int i = 1; i < 4; i++) tile_add(lds + K2_B_OFF + w * 14336 + (i - 1) * 4096, t[0], lane);
    asm volatile("s_waitcnt vmcnt(0)" : "+v"(xr[0]), "+v"(xr[1]), "+v"(xr[2]), "+v"(xr[3]) :: "memory");
    float s1 = 0.f, s2 = 0.f;
#pragma unroll
    for (int g = 0; g < 4; g++) {
      const float a0 = __builtin_bit_cast(float, xr[g][0] << 16) + t[0][4 * g], a1 = __builtin_bit_cast(float, xr[g][0] & 0xffff0000u) + t[0][4 * g + 1];
      const float a2 = __builtin_bit_cast(float, xr[g][1] << 16) + t[0][4 * g + 2], a3 = __builtin_bit_cast(float, xr[g][1] & 0xffff0000u) + t[0][4 * g + 3];
      x1p[2 * g] = pack2(a0, a1); x1p[2 * g + 1] = pack2(a2, a3);
    }
    const __amdgpu_buffer_rsrc_t rx = __builtin_amdgcn_make_buffer_rsrc(ws + WS_X1, 0, TBYTES, 0x00020000);
    const int tile = tb64 * 2 + tj;
#pragma unroll
    for (int gp = 0; gp < 2; gp++) {
      auto a = __builtin_amdgcn_permlane32_swap(x1p[4 * gp], x1p[4 * gp + 2], false, false);
      auto c = __builtin_amdgcn_permlane32_swap(x1p[4 * gp + 1], x1p[4 * gp + 3], false, false);
      u32x4 v; v[0] = a[0]; v[1] = c[0]; v[2] = a[1]; v[3] = c[1];
      st16_sc1(v, rx, (unsigned)(tile * 56 + nb * 8 + ni * 4 + 2 * gp + hh) * 512 + r * 16);
    }
#pragma unroll
    for (int j = 0; j < 8; j++) {
      const float lo = __builtin_bit_cast(float, x1p[j] << 16), hi = __builtin_bit_cast(float, x1p[j] & 0xffff0000u);
      s1 += lo + hi; s2 = __builtin_fmaf(lo, lo, s2); s2 = __builtin_fmaf(hi, hi, s2);
    }
    s1 += other_half(s1, hh); s2 += other_half(s2, hh);
    const __amdgpu_buffer_rsrc_t rs = __builtin_amdgcn_make_buffer_rsrc(ws + WS_STATS, 0, 64 * 14 * 32 * 8, 0x00020000);
    { u32x2 v; v[0] = __builtin_bit_cast(unsigned, s1); v[1] = __builtin_bit_cast(unsigned, s2); st8_sc1(v, rs, hh ? 0x80000000u : (unsigned)((tile * 14 + nb * 2 + ni) * 32 + r) * 8); }
    STAMP(12);
    asm volatile("s_waitcnt vmcnt(0) lgkmcnt(0)" ::: "memory");
    flag_set(ws + WS_FX1 + (tile * 16 + nb * 2 + ni) * 4, lane);
    PRIO_LO;
#pragma unroll
    for (int j = 0; j < 14; j++) { const int row0 = 56 * w + 4 * j; dma16(dv3, w3src + (size_t)row0 * 3584, lds0 + K2_B_OFF + row0 * 256); }
#pragma unroll
    for (int j = 0; j < 14; j++) { const int row0 = 224 + 56 * w + 4 * j; dma16(dv3, w3src + (size_t)row0 * 3584, lds0 + K2_B_OFF + row0 * 256); }
    STAMP(2);
  }
  {
    asm volatile("s_waitcnt vmcnt(0)" ::: "memory");
    gsync(syb, w, ++eb);
    unsigned tb[4];
    {
      const int blk = (lane >> 4) & 1, q = (lane & 15) >> 2, p = lane & 3;
#pragma unroll
      for (int n = 0; n < 4; n++) tb[n] = K2_B_OFF + (8 * hh + q) * 256 + ((n ^ q) * 64) + blk * 32 + p * 8;
    }
    STAMP(3);
    const int tile = tb128 * 4 + w;
    flags_wait(lane < 16 ? ws + WS_FX1 + (tile * 16 + (lane < 14 ? lane : 0)) * 4 : ws + WS_FCS + (nb3 * 4 + (lane & 3)) * 4);
    STAMP(4);
    const u32x4 rx = mkrsrc(ws + WS_X1, TBYTES);
    const u32x4 rsd = mkrsrc(ws + WS_STATS, 64 * 14 * 32 * 8);
    const u32x4 rcd = mkrsrc(ws + WS_CS3, 1792 * 4);
    u32x2 sv[7], cv; float g3;
    {
      const unsigned so = (unsigned)((tile * 14 + hh * 7) * 32 + r) * 8, co = (unsigned)(nb3 * 128 + lane * 2) * 4;
#pragma unroll
      for (int j = 0; j < 7; j++) asm volatile("buffer_load_dwordx2 %0, %1, %2, 0 offen offset:%3 sc1" : "=v"(sv[j]) : "v"(so), "s"(rsd), "i"(j * 256) : "memory");
      asm volatile("buffer_load_dwordx2 %0, %1, %2, 0 offen sc1" : "=v"(cv) : "v"(co), "s"(rcd) : "memory");
      const unsigned go = (unsigned)(tile * 32 + r) * 4;
      asm volatile("global_load_dword %0, %1, %2" : "=v"(g3) : "v"(go), "s"(ws + WS_GATE) : "memory");
    }
    u32x4 af[28];
    const unsigned av = (unsigned)(tile * 56) * 512 + lane * 16;
#pragma unroll
    for (int s = 0; s < 28; s++) BLOAD_SC1(af[s], av, rx, (s >> 2) * 4096, (s & 3) * 1024);
    const __amdgpu_buffer_rsrc_t r3 = __builtin_amdgcn_make_buffer_rsrc(ws + WS_H3, 0, T3BYTES, 0x00020000);
    STAMP(13);
    PRIO_G3;
    constexpr int PD = 6;
    u32x4 wfr[PD];
#pragma unroll
    for (int u = 0; u < PD; u++) wfr[u] = tr_frag(lds + tb[0] + u * 4096, 1024);
    asm volatile("s_waitcnt vmcnt(28)" : "+v"(sv[0]), "+v"(sv[1]), "+v"(sv[2]), "+v"(sv[3]), "+v"(sv[4]), "+v"(sv[5]), "+v"(sv[6]), "+v"(cv), "+v"(g3) :: "memory");
    const unsigned csl = K2_CSP_OFF + w * 512;
    *(LAS u32x2*)(lds + csl + lane * 8) = cv;
    float nmean, ga, gb, irs;
    {
      float s1 = 0.f, s2 = 0.f;
#pragma unroll
      for (int j = 0; j < 7; j++) { const unsigned u0 = sv[j][0], u1 = sv[j][1]; s1 += __builtin_bit_cast(float, u0); s2 += __builtin_bit_cast(float, u1); }
      s1 += other_half(s1, hh); s2 += other_half(s2, hh);
      const float mean = s1 * (1.0f / 448.0f);
      const float var = fmaxf(s2 * (1.0f / 448.0f) - mean * mean, 0.f) + 1e-5f;
      const float rstd = __builtin_amdgcn_rsqf(var);
      nmean = -mean; ga = -2.302208198f * rstd; gb = -0.1029432397f * rstd * rstd * rstd;
      irs = var * rstd * __builtin_amdgcn_rcpf(fmaxf(1.0f - g3, 1.0e-30f));
    }
    f32x16 acc[2];
    unsigned pk[8];
    f32x4 c4;
    float tt[4], ee[4];
#pragma unroll
    for (int n = 0; n < 5; n++) {
#pragma unroll
      for (int s = 0; s < 28; s++) {
        const int u = n * 28 + s;
        if (n < 4) {
          if (n == 0) asm volatile("s_waitcnt vmcnt(%1)" : "+v"(af[s]) : "i"(27 - s) : "memory");
          acc[n & 1] = mfma(wfr[u % PD], af[s], s == 0 ? zero16() : acc[n & 1]);
          if (u + PD < 112) wfr[u % PD] = tr_frag(lds + tb[(u + PD) / 28] + ((u + PD) % 28) * 4096, 1024);
        }
        if (n >= 1 && s < 24) {
          const int pn = n - 1, g = s / 6, ph = s % 6;
          if (ph == 0) c4 = *(const LAS f32x4*)(lds + csl + (pn * 32 + 8 * g + 4 * hh) * 4);
          if (ph == 1) {
#pragma unroll
            for (int i = 0; i < 4; i++) { tt[i] = __builtin_fmaf(nmean, c4[i], acc[pn & 1][4 * g + i]); ee[i] = tt[i] * tt[i]; }
          }
          if (ph == 2) {
#pragma unroll
            for (int i = 0; i < 4; i++) ee[i] = tt[i] * __builtin_fmaf(ee[i], gb, ga);
          }
          if (ph == 3) {
#pragma unroll
            for (int i = 0; i < 4; i++) ee[i] = __builtin_amdgcn_exp2f(ee[i]);
          }
          if (ph == 4) {
#pragma unroll
            for (int i = 0; i < 4; i++) ee[i] = __builtin_amdgcn_rcpf(__builtin_fmaf(ee[i], irs, irs));
          }
          if (ph == 5) {
            pk[2 * g] = pack2(tt[0] * ee[0], tt[1] * ee[1]); pk[2 * g + 1] = pack2(tt[2] * ee[2], tt[3] * ee[3]);
            if (g & 1) {
              const int gp = g >> 1;
              auto a = __builtin_amdgcn_permlane32_swap(pk[4 * gp], pk[4 * gp + 2], false, false);
              auto c = __builtin_amdgcn_permlane32_swap(pk[4 * gp + 1], pk[4 * gp + 3], false, false);
              u32x4 v; v[0] = a[0]; v[1] = c[0]; v[2] = a[1]; v[3] = c[1];
              st16_sc1(v, r3, (unsigned)(tile * 224 + nb3 * 16 + pn * 4 + 2 * gp + hh) * 512 + r * 16);
            }
          }
        }
        if (n < 4) __builtin_amdgcn_sched_barrier(0);
        if (n == 0 && s == 27) STAMP(14);
        if (n == 3 && s == 27) STAMP(15);
      }
    }
    PRIO_HI;
    STAMP(5);
    gsync(syb, w, ++eb);
    asm volatile("s_waitcnt vmcnt(0)" ::: "memory");
    flag_set(ws + WS_FH3 + (tile * 16 + nb3) * 4, lane);
    PRIO_LO;
  }
  {
    const unsigned ring = K2_B_OFF + w * 28672;
    const char* wsrc = w4 + (size_t)(448 * w) * 896 + nb * 128;
#pragma unroll
    for (int j = 0; j < 28; j++) dma16(dv64, wsrc + j * (8 * 896), lds0 + ring + j * 1024);
    const float gate = *(const float*)(ws + WS_GATE + ((tb64 * 2 + tj) * 32 + r) * 4);
    {
      const int blk0 = (7 * w) >> 1;
      const int k = lane & 3;
      flags_wait(ws + WS_FH3 + ((tb64 * 2 + ((lane >> 2) & 1)) * 16 + blk0 + k) * 4);
    }
    asm volatile("s_waitcnt vmcnt(0)" ::: "memory");
    STAMP(6);
    const u32x4 r3 = mkrsrc(ws + WS_H3, T3BYTES);
    unsigned av[2];
#pragma unroll
    for (int e = 0; e < 2; e++) av[e] = (unsigned)((tb64 * 2 + (tj ^ e)) * 224 + 56 * w) * 512 + lane * 16;
    u32x4 af[14][2];
#pragma unroll
    for (int s = 0; s < 14; s++)
#pragma unroll
      for (int e = 0; e < 2; e++) BLOAD_SC1(af[s][e], av[e], r3, (s >> 2) * 4096, (s & 3) * 1024);
    f32x16 t[4];
#pragma unroll
    for (int i = 0; i < 4; i++) t[i] = zero16();
    u32x4 f0[2], f1[2];
    const char* wosrc = wo + (size_t)(112 * w) * 896 + nb * 128;
    f0[0] = tr_frag(lds + ring + t0, 512); f1[0] = tr_frag(lds + ring + t1, 512);
#pragma unroll
    for (int s = 0; s < 28; s++) {
      const int sl = s % 14;
      asm volatile("s_waitcnt lgkmcnt(0)" : "+v"(f0[s & 1]), "+v"(f1[s & 1]) :: "memory");
      if (s + 1 < 28) {
        const int nl = (s + 1) % 14;
        if (s == 13) asm volatile("s_waitcnt vmcnt(50)" ::: "memory");
        else if (s > 13) asm volatile("s_waitcnt vmcnt(%0)" :: "i"(2 + 4 * (13 - nl) + 2 * (s - 14 < 7 ? s - 14 : 7)) : "memory");
        f0[(s + 1) & 1] = tr_frag(lds + ring + t0 + nl * 2048, 512); f1[(s + 1) & 1] = tr_frag(lds + ring + t1 + nl * 2048, 512);
      }
      if (s < 14) {
        dma16(dv64, wsrc + (28 + 2 * s) * (8 * 896), lds0 + ring + (2 * s) * 1024);
        dma16(dv64, wsrc + (29 + 2 * s) * (8 * 896), lds0 + ring + (2 * s + 1) * 1024);
        asm volatile("s_waitcnt vmcnt(%2)" : "+v"(af[s][0]), "+v"(af[s][1]) : "i"(2 * (13 - s) + 4 * s + 2) : "memory");
      } else {
        if (s < 21) {
          dma16(dv64, wosrc + (2 * sl) * (8 * 896), lds0 + ring + (2 * sl) * 1024);
          dma16(dv64, wosrc + (2 * sl + 1) * (8 * 896), lds0 + ring + (2 * sl + 1) * 1024);
        }
        asm volatile("s_waitcnt vmcnt(%2)" : "+v"(af[sl][0]), "+v"(af[sl][1]) : "i"(4 * (13 - sl) + 2 * (s - 13 < 7 ? s - 13 : 7)) : "memory");
      }
      t[0] = mfma(f0[s & 1], af[sl][0], t[0]); t[1] = mfma(f1[s & 1], af[sl][0], t[1]);
      t[2] = mfma(f0[s & 1], af[sl][1], t[2]); t[3] = mfma(f1[s & 1], af[sl][1], t[3]);
      if (s < 14) {
        BLOAD_SC1(af[s][0], av[0], r3, ((s + 14) >> 2) * 4096, ((s + 14) & 3) * 1024);
        BLOAD_SC1(af[s][1], av[1], r3, ((s + 14) >> 2) * 4096, ((s + 14) & 3) * 1024);
      }
      __builtin_amdgcn_sched_barrier(0);
    }
    STAMP(7);
    PRIO_HI;
    flags_wait(ws + WS_FO + ((tb64 * 2 + ((lane >> 2) & 1)) * 16 + 4 * w + (lane & 3)) * 4);
    asm volatile("s_waitcnt vmcnt(0)" ::: "memory");
    STAMP(8);
    {
      const u32x4 ro = mkrsrc(ws + WS_O, TBYTES);
      unsigned ov[2];
#pragma unroll
      for (int e = 0; e < 2; e++) ov[e] = (unsigned)((tb64 * 2 + (tj ^ e)) * 56 + 14 * w) * 512 + lane * 16;
#pragma unroll
      for (int s = 0; s < 7; s++)
#pragma unroll
        for (int e = 0; e < 2; e++) BLOAD_SC1(af[s][e], ov[e], ro, (s >> 2) * 4096, (s & 3) * 1024);
      u32x4 g0[7], g1[7];
#pragma unroll
      for (int s = 0; s < 7; s++) { g0[s] = tr_frag(lds + ring + t0 + s * 2048, 512); g1[s] = tr_frag(lds + ring + t1 + s * 2048, 512); }
#pragma unroll
      for (int s = 0; s < 7; s++) {
        asm volatile("s_waitcnt vmcnt(%2)" : "+v"(af[s][0]), "+v"(af[s][1]) : "i"(2 * (6 - s)) : "memory");
        t[0] = mfma(g0[s], af[s][0], t[0]); t[1] = mfma(g1[s], af[s][0], t[1]);
        t[2] = mfma(g0[s], af[s][1], t[2]); t[3] = mfma(g1[s], af[s][1], t[3]);
      }
    }
    asm volatile("s_waitcnt lgkmcnt(0)" ::: "memory");
#pragma unroll
    for (int i = 1; i < 4; i++) tile_put(lds + ring + (i - 1) * 4096, t[i], lane);
    gsync(syb, w, ++eb);
#pragma unroll
    for (int i = 1; i < 4; i++) tile_add(lds + K2_B_OFF + (w ^ i) * 28672 + (i - 1) * 4096, t[0], lane);
    unsigned pk[8];
    const float og = 1.0f - gate;
#pragma unroll
    for (int j = 0; j < 8; j++)
      pk[j] = pack2(__builtin_fmaf(og, __builtin_bit_cast(float, x1p[j] << 16), t[0][2 * j]), __builtin_fmaf(og, __builtin_bit_cast(float, x1p[j] & 0xffff0000u), t[0][2 * j + 1]));
    char* op = out + (size_t)((tb64 * 2 + tj) * 32 + r) * 896 + (nb * 64 + ni * 32) * 2 + hh * 16;
#pragma unroll
    for (int gp = 0; gp < 2; gp++) {
      auto a2 = __builtin_amdgcn_permlane32_swap(pk[4 * gp], pk[4 * gp + 2], false, false);
      auto c2 = __builtin_amdgcn_permlane32_swap(pk[4 * gp + 1], pk[4 * gp + 3], false, false);
      u32x4 v; v[0] = a2[0]; v[1] = c2[0]; v[2] = a2[1]; v[3] = c2[1];
      *(u32x4*)(op + gp * 32) = v;
    }
    STAMP(9);
  }
}

extern "C" void kernel_launch(void* const* d_in, const int* in_sizes, int n_in,
                              void* d_out, int out_size, void* d_ws, size_t ws_size,
                              hipStream_t stream) {
  const char* x = (const char*)d_in[0]; const char* wqkv = (const char*)d_in[1]; const char* wo = (const char*)d_in[2];
  const char* w1 = (const char*)d_in[4]; const char* w2 = (const char*)d_in[5]; const char* w3 = (const char*)d_in[6];
  const char* w4 = (const char*)d_in[7]; const char* gw = (const char*)d_in[8];
  (void)hipFuncSetAttribute((const void*)k1_kernel, hipFuncAttributeMaxDynamicSharedMemorySize, K1_LDS);
  (void)hipFuncSetAttribute((const void*)k2_kernel, hipFuncAttributeMaxDynamicSharedMemorySize, K2_LDS);
  k1_kernel<<<256, 768, K1_LDS, stream>>>(x, wqkv, w1, gw, (char*)d_ws, wo, w2, w3, w4);
  k2_kernel<<<256, 512, K2_LDS, stream>>>(x, wo, w2, w3, w4, (char*)d_ws, (char*)d_out);
}
```

```cpp
#include <hip/hip_runtime.h>
#include <stdint.h>

#define TOUCH 3
#define PL 2
#define LDPRIO

typedef __attribute__((ext_vector_type(8))) __bf16 bf16x8;
typedef __attribute__((ext_vector_type(2))) __bf16 bf16x2;
typedef __attribute__((ext_vector_type(4))) short s16x4;
typedef __attribute__((ext_vector_type(16))) float f32x16;
typedef __attribute__((ext_vector_type(4))) float f32x4;
typedef __attribute__((ext_vector_type(2))) float f32x2;
typedef __attribute__((ext_vector_type(4))) unsigned u32x4;
typedef __attribute__((ext_vector_type(2))) unsigned u32x2;
#define LAS __attribute__((address_space(3)))
#define GAS __attribute__((address_space(1)))
#define DI __device__ __forceinline__

constexpr int SEQ = 2048, DM = 448, NH = 16, DH = 28, FF = 1792;

constexpr size_t WS_CNT   = 0;
constexpr size_t WS_GATE  = 16384;
constexpr size_t WS_STATS = 32768;
constexpr size_t WS_Q     = 1u << 20;
constexpr size_t WS_K     = WS_Q + (2u << 20);
constexpr size_t WS_V     = WS_K + (2u << 20);
constexpr size_t WS_H     = WS_V + (2u << 20);
constexpr size_t WS_X1    = WS_H + (2u << 20);
constexpr size_t WS_O     = WS_X1 + (2u << 20);
constexpr size_t WS_H3    = WS_O + (2u << 20);
constexpr unsigned TBYTES = 64u * 56u * 512u;
constexpr unsigned T3BYTES = 64u * 224u * 512u;

DI f32x16 mfma(u32x4 a, u32x4 b, f32x16 c) {
  return __builtin_amdgcn_mfma_f32_32x32x16_bf16(__builtin_bit_cast(bf16x8, a), __builtin_bit_cast(bf16x8, b), c, 0, 0, 0);
}
DI unsigned pack2(float a, float b) {
  f32x2 v; v[0] = a; v[1] = b;
  return __builtin_bit_cast(unsigned, __builtin_convertvector(v, bf16x2));
}
DI u32x4 tr_frag(const LAS char* p, int hi_off) {
  s16x4 lo = __builtin_amdgcn_ds_read_tr16_b64_v4i16((LAS s16x4*)p);
  s16x4 hi = __builtin_amdgcn_ds_read_tr16_b64_v4i16((LAS s16x4*)(p + hi_off));
  u32x2 a = __builtin_bit_cast(u32x2, lo), b = __builtin_bit_cast(u32x2, hi);
  u32x4 r; r[0] = a[0]; r[1] = a[1]; r[2] = b[0]; r[3] = b[1];
  return r;
}
DI float gelu_f(float x) {
  const float C0 = -2.302208198f;
  const float C1 = -0.1029432397f;
  float z = x * __builtin_fmaf(x * x, C1, C0);
  float e = __builtin_amdgcn_exp2f(z);
  return x * __builtin_amdgcn_rcpf(1.0f + e);
}
DI float other_half(float v, int hh) {
  unsigned u = __builtin_bit_cast(unsigned, v);
  auto r = __builtin_amdgcn_permlane32_swap(u, u, false, false);
  return __builtin_bit_cast(float, hh ? r[0] : r[1]);
}
DI void sq2(unsigned v, float& a, float& b) {
  const float lo = __builtin_bit_cast(float, v << 16), hi = __builtin_bit_cast(float, v & 0xffff0000u);
  a = __builtin_fmaf(lo, lo, a); b = __builtin_fmaf(hi, hi, b);
}
#define STAMP(i)
#define STAMP_DECL(base)
#define STAMP_FLUSH(base, n)
DI f32x16 zero16() { f32x16 z; for (int i = 0; i < 16; i++) z[i] = 0.f; return z; }

DI void dma16_masked(unsigned voff, const void* sbase, unsigned ldsdst, uint64_t mask) {
  unsigned keep;
  asm volatile("s_mov_b32 %0, m0\n\ts_mov_b32 m0, %3\n\ts_mov_b64 exec, %4\n\tglobal_load_lds_dwordx4 %1, %2\n\ts_mov_b64 exec, -1\n\ts_mov_b32 m0, %0"
               : "=&s"(keep) : "v"(voff), "s"(sbase), "s"(ldsdst), "s"(mask) : "memory");
}
DI void dma16(unsigned voff, const void* sbase, unsigned ldsdst) {
  unsigned keep;
  asm volatile("s_mov_b32 %0, m0\n\ts_mov_b32 m0, %3\n\ts_nop 0\n\tglobal_load_lds_dwordx4 %1, %2\n\ts_mov_b32 m0, %0"
               : "=&s"(keep) : "v"(voff), "s"(sbase), "s"(ldsdst) : "memory");
}

constexpr int K1_SLOT    = 32768;
constexpr int K1_AUX_OFF = 151040;
constexpr int K1_CS_OFF  = K1_AUX_OFF + 3584;
constexpr int K1_SQ_OFF  = K1_CS_OFF + 2048;
constexpr int K1_LDS     = K1_SQ_OFF + 1024;

DI u32x4 tr_frag2(const LAS char* p0, const LAS char* p1) {
  s16x4 lo = __builtin_amdgcn_ds_read_tr16_b64_v4i16((LAS s16x4*)p0);
  s16x4 hi = __builtin_amdgcn_ds_read_tr16_b64_v4i16((LAS s16x4*)p1);
  u32x2 a = __builtin_bit_cast(u32x2, lo), b = __builtin_bit_cast(u32x2, hi);
  u32x4 r; r[0] = a[0]; r[1] = a[1]; r[2] = b[0]; r[3] = b[1];
  return r;
}
DI unsigned k1_tr_off(int lane, int t) {
  const int hh = lane >> 5, blk = (lane >> 4) & 1, q = (lane & 15) >> 2, p = lane & 3;
  int u = 7 * t + 4 * blk + p;
  if (blk == 1 && p == 3) u = 7 * t;
  return (8 * hh + q) * 256 + (((u >> 1) ^ (q << 2)) * 16) + (u & 1) * 8;
}

constexpr int K2_B_OFF   = 0;
constexpr int K2_A_OFF   = 114688;
constexpr int K2_CSP_OFF = K2_A_OFF + 32768;
constexpr int K2_CS_OFF  = K2_CSP_OFF + 2048;
constexpr int K2_M_OFF   = K2_CS_OFF + 512;
constexpr int K2_SY_OFF  = K2_M_OFF + 512;
constexpr int K2_END     = K2_SY_OFF + 64;
static_assert(K2_END <= K1_AUX_OFF, "lds map");
constexpr int K2_LDS     = K1_LDS;
constexpr unsigned SPIN_CAP = 1u << 20;
#define POLL_SLEEP 4
#define PH 2
#define PG3 2
#define PRIO_G3 __builtin_amdgcn_s_setprio(PG3)
#define PRIO_HI __builtin_amdgcn_s_setprio(PH)
#define PRIO_LO __builtin_amdgcn_s_setprio(PL)
constexpr size_t WS_FX1 = WS_CNT, WS_FH3 = WS_CNT + 4096, WS_FO = WS_CNT + 8192, WS_FCS = WS_CNT + 12288;
constexpr size_t WS_EPOCH = WS_CNT + 12288 + 1024;
constexpr size_t WS_F1 = 524288;
constexpr size_t WS_CS3 = 24576;

DI void gsync(LAS unsigned* f, int w, unsigned epoch) {
  asm volatile("s_waitcnt lgkmcnt(0)" ::: "memory");
  __hip_atomic_store(f + w, epoch, __ATOMIC_RELAXED, __HIP_MEMORY_SCOPE_WORKGROUP);
  for (unsigned s = 0; s < SPIN_CAP; s++) {
    u32x4 v;
    asm volatile("ds_read_b128 %0, %1\n\ts_waitcnt lgkmcnt(0)" : "=v"(v) : "v"((unsigned)(uintptr_t)f) : "memory");
    const unsigned a = v[0], b = v[1], c = v[2], d = v[3];
    const unsigned m = min(min(a, b), min(c, d));
    if ((unsigned)__builtin_amdgcn_readfirstlane(m) >= epoch) break;
    __builtin_amdgcn_s_sleep(1);
  }
}
DI void flag_set_t(char* p, int lane, unsigned tag) {
  if (lane == 0) __hip_atomic_store((GAS unsigned*)p, tag, __ATOMIC_RELAXED, __HIP_MEMORY_SCOPE_AGENT);
}
#define flag_set(p, lane) flag_set_t(p, lane, tag)
#define flags_wait(p) flags_wait_t(p, tag)
DI char* f1_flag(char* ws, int type, int sub, int tile) { return ws + WS_F1 + ((type * 4 + sub) * 16 + (tile >> 2)) * 128; }
DI void block_publish(LAS unsigned* cnt, char* p, int lane, unsigned tag) {
  if (lane == 0) {
    const unsigned old = __hip_atomic_fetch_add(cnt, 1u, __ATOMIC_RELAXED, __HIP_MEMORY_SCOPE_WORKGROUP);
    if (old == 7u) __hip_atomic_store((GAS unsigned*)p, tag, __ATOMIC_RELAXED, __HIP_MEMORY_SCOPE_AGENT);
  }
}
DI void flags_wait_t(const char* p, unsigned tag) {
  for (unsigned s = 0; s < SPIN_CAP; s++) {
    const unsigned v = __hip_atomic_load((const GAS unsigned*)p, __ATOMIC_RELAXED, __HIP_MEMORY_SCOPE_AGENT);
    if (__all(v == tag)) break;
    __builtin_amdgcn_s_sleep(POLL_SLEEP);
  }
  asm volatile("" ::: "memory");
}
DI u32x4 mkrsrc(const void* p, unsigned bytes) {
  const uint64_t a = (uint64_t)(uintptr_t)p;
  u32x4 r; r[0] = (unsigned)a; r[1] = (unsigned)(a >> 32) & 0xffffu; r[2] = bytes; r[3] = 0x00020000u;
  return r;
}
#define BLOAD(dst, voff, rs, soff, imm) \
  asm volatile("buffer_load_dwordx4 %0, %1, %2, %3 offen offset:%4" : "=v"(dst) : "v"(voff), "s"(rs), "s"(soff), "i"(imm) : "memory")
#define BLOAD_SC1(dst, voff, rs, soff, imm) \
  asm volatile("buffer_load_dwordx4 %0, %1, %2, %3 offen offset:%4 sc1" : "=v"(dst) : "v"(voff), "s"(rs), "s"(soff), "i"(imm) : "memory")
DI void st16_sc1(u32x4 v, __amdgpu_buffer_rsrc_t rs, unsigned off) { __builtin_amdgcn_raw_buffer_store_b128(v, rs, off, 0, 16); }
DI void st8_sc1(u32x2 v, __amdgpu_buffer_rsrc_t rs, unsigned off) { __builtin_amdgcn_raw_buffer_store_b64(v, rs, off, 0, 16); }

DI void tile_put(LAS char* p, const f32x16& t, int lane) {
#pragma unroll
  for (int g = 0; g < 4; g++) { f32x4 v; v[0] = t[4 * g]; v[1] = t[4 * g + 1]; v[2] = t[4 * g + 2]; v[3] = t[4 * g + 3]; *(LAS f32x4*)(p + g * 1024 + lane * 16) = v; }
}
DI void tile_add(const LAS char* p, f32x16& t, int lane) {
#pragma unroll
  for (int g = 0; g < 4; g++) { f32x4 v = *(const LAS f32x4*)(p + g * 1024 + lane * 16); t[4 * g] += v[0]; t[4 * g + 1] += v[1]; t[4 * g + 2] += v[2]; t[4 * g + 3] += v[3]; }
}
DI void reduce_scatter(LAS char* buf, f32x16 (&t)[4], int w, int lane, int x0, int x1, LAS unsigned* f, unsigned& epoch) {
  tile_put(buf + (((w ^ x0) * 2 + 0) * 4096), t[2], lane);
  tile_put(buf + (((w ^ x1) * 2 + 1) * 4096), t[3], lane);
  gsync(f, w, ++epoch);
  tile_add(buf + (((w ^ 2 ^ x0) * 2 + 0) * 4096), t[0], lane);
  tile_add(buf + (((w ^ 2 ^ x1) * 2 + 1) * 4096), t[1], lane);
  asm volatile("s_waitcnt lgkmcnt(0)" ::: "memory");
  tile_put(buf + (((w ^ 2 ^ x0) * 2 + 0) * 4096), t[1], lane);
  gsync(f, w, ++epoch);
  tile_add(buf + (((w ^ 3 ^ x0) * 2 + 0) * 4096), t[0], lane);
  asm volatile("s_waitcnt lgkmcnt(0)" ::: "memory");
}

DI unsigned w64_dma_voff(int lane, unsigned ldw) { const int rin = lane >> 3, pc = lane & 7; return rin * ldw + ((pc ^ (((rin >> 1) & 1) << 2)) * 16); }
DI unsigned w64_tr_off(int lane, int nt) {
  const int hh = lane >> 5, blk = (lane >> 4) & 1, q = (lane & 15) >> 2, p = lane & 3;
  return (8 * hh + q) * 128 + ((nt ^ ((q >> 1) & 1)) * 64) + blk * 32 + p * 8;
}

#define ATT_EXP4(SC, B, P, D)                                                                                    \
    SC[B] = __builtin_amdgcn_exp2f(SC[B]); SC[B + 1] = __builtin_amdgcn_exp2f(SC[B + 1]);                       \
    SC[B + 2] = __builtin_amdgcn_exp2f(SC[B + 2]); SC[B + 3] = __builtin_amdgcn_exp2f(SC[B + 3]);               \
    P[D] = pack2(SC[B], SC[B + 1]); P[D + 1] = pack2(SC[B + 2], SC[B + 3]);                                     \
    __builtin_amdgcn_sched_barrier(0);
#define ATT_STEP(SC, SN, PC0, PC1, PP0, PP1, KN0, KN1, QN0, QN1, VP0, VP1, OP)                                  \
  {                                                                                                             \
    SN = mfma(KN0, QN0, zero16());                                                                              \
    __builtin_amdgcn_sched_barrier(0);                                                                          \
    ATT_EXP4(SC, 0, PC0, 0)                                                                                     \
    SN = mfma(KN1, QN1, SN);                                                                                    \
    __builtin_amdgcn_sched_barrier(0);                                                                          \
    ATT_EXP4(SC, 4, PC0, 2)                                                                                     \
    OP = mfma(VP0, PP0, OP);                                                                                    \
    __builtin_amdgcn_sched_barrier(0);                                                                          \
    ATT_EXP4(SC, 8, PC1, 0)                                                                                     \
    OP = mfma(VP1, PP1, OP);                                                                                    \
    __builtin_amdgcn_sched_barrier(0);                                                                          \
    ATT_EXP4(SC, 12, PC1, 2)                                                                                    \
  }

DI float max16(const f32x16& s) {
  float a = fmaxf(fmaxf(s[0], s[1]), s[2]), b = fmaxf(fmaxf(s[3], s[4]), s[5]), c = fmaxf(fmaxf(s[6], s[7]), s[8]);
  float d = fmaxf(fmaxf(s[9], s[10]), s[11]), e = fmaxf(fmaxf(s[12], s[13]), s[14]);
  return fmaxf(fmaxf(fmaxf(a, b), c), fmaxf(fmaxf(d, e), s[15]));
}

template <int KIND>
DI void phase1(LAS char* lds, const char* __restrict__ x, const char* __restrict__ wsrc, const int ldw, const char* __restrict__ gate_w,
               char* __restrict__ ws, const int type, const int sub, const int tg, const int wv, const int stbase_, const char* touch) {
  const unsigned lds0 = (unsigned)(uintptr_t)lds;
  const int tid = threadIdx.x, lane = tid & 63, w = wv & 3, np = wv >> 2;
  const int r = lane & 31, hh = lane >> 5;
  const int tile = tg * 4 + w;
  const int drow = lane >> 4, dlc = (lane & 15) ^ (drow << 2);
  const unsigned dvoff = drow * ldw + (dlc < 14 ? dlc : 0) * 16;
  const uint64_t dmask = ~((3ull << 14) | (3ull << (16 + 10)) | (3ull << (32 + 6)) | (3ull << (48 + 2)));
  unsigned xv[2];
  {
    const int rin = lane >> 3, pc = lane & 7;
#pragma unroll
    for (int e = 0; e < 2; e++) xv[e] = rin * 896 + ((pc ^ ((4 * e + (rin >> 1)) & 7)) * 16);
  }
  const char* xbase = x + (size_t)(tg * 128 + w * 32 + np * 16) * 896;
  const char* wbase = wsrc + (size_t)(8 * wv) * ldw;
  auto issue_half = [&](int st, int jj) {
    const unsigned slot = lds0 + (st & 3) * K1_SLOT;
    dma16_masked(dvoff, wbase + (size_t)(64 * st + 4 * jj) * ldw, slot + (8 * wv + 4 * jj) * 256, dmask);
    dma16(xv[jj], xbase + jj * (8 * 896) + st * 128, slot + 16384 + w * 4096 + (2 * np + jj) * 1024);
  };
  unsigned gw0 = 0;
  if (KIND == 2) {
    const unsigned go = (tid < 448 ? tid : 447) * 2;
    asm volatile("global_load_ushort %0, %1, %2" : "=v"(gw0) : "v"(go), "s"(gate_w) : "memory");
  }
  issue_half(0, 0); issue_half(0, 1); issue_half(1, 0); issue_half(1, 1);
  unsigned xb[4];
#pragma unroll
  for (int ks = 0; ks < 4; ks++) xb[ks] = 16384 + w * 4096 + r * 128 + (((2 * ks + hh) ^ ((r >> 1) & 7)) * 16);
  const unsigned wb0 = k1_tr_off(lane, 2 * np), wb1 = k1_tr_off(lane, 2 * np + 1);
  const bool isaux = ((lane >> 4) & 1) == 1 && (lane & 3) == 3;
  const unsigned ab = isaux ? K1_AUX_OFF + (8 * hh + ((lane & 15) >> 2)) * 8 : wb1;
  const unsigned kstep = isaux ? 128u : 4096u, hoff = isaux ? 32u : 1024u;
  const unsigned sstep = isaux ? 512u : (unsigned)K1_SLOT, wrapst = isaux ? 512u : (unsigned)(-3 * K1_SLOT);
  unsigned a3[4];
#pragma unroll
  for (int ks = 0; ks < 4; ks++) a3[ks] = ab + ks * kstep;
  f32x16 acc[2], cs[2]; acc[0] = zero16(); acc[1] = zero16(); cs[0] = zero16(); cs[1] = zero16();
  u32x4 ones; ones[0] = ones[1] = ones[2] = ones[3] = 0x3F803F80u;
  u32x4 wf[2][4][2], xf[2][4], cf[2][2];
  float sqa[4] = {0.f, 0.f, 0.f, 0.f};
  unsigned sink;
  auto read_stage = [&](int st) {
    const int so = (st & 3) * K1_SLOT;
#pragma unroll
    for (int ks = 0; ks < 4; ks++) {
      xf[st & 1][ks] = *(const LAS u32x4*)(lds + xb[ks] + so);
      wf[st & 1][ks][0] = tr_frag(lds + wb0 + so + ks * 4096, 1024);
      if (KIND == 2) { wf[st & 1][ks][1] = tr_frag2(lds + a3[ks], lds + a3[ks] + hoff); a3[ks] += (st == 3) ? wrapst : sstep; }
      else wf[st & 1][ks][1] = tr_frag(lds + wb1 + so + ks * 4096, 1024);
    }
    if (KIND == 2) { cf[st & 1][0] = tr_frag(lds + wb0 + so + w * 4096, 1024); cf[st & 1][1] = tr_frag(lds + wb1 + so + w * 4096, 1024); }
  };
  asm volatile("s_waitcnt vmcnt(4)" : "+v"(gw0) :: "memory");
  if (KIND == 2) {
    u32x2 a; a[0] = 0x3F80u | (gw0 << 16); a[1] = 0u;
    *(LAS u32x2*)(lds + K1_AUX_OFF + (tid < 448 ? tid : 447) * 8) = a;
  }
  asm volatile("s_waitcnt lgkmcnt(0)\n\ts_barrier" ::: "memory");
  read_stage(0);
  issue_half(2, 0); issue_half(2, 1); issue_half(3, 0); issue_half(3, 1);
#pragma unroll
  for (int st = 0; st < 7; st++) {
    __builtin_amdgcn_sched_barrier(0);
    if (st < 4)       asm volatile("s_waitcnt vmcnt(8) lgkmcnt(0)\n\ts_barrier" ::: "memory");
    else if (st == 4) asm volatile("s_waitcnt vmcnt(5) lgkmcnt(0)\n\ts_barrier" ::: "memory");
    else if (st == 5) asm volatile("s_waitcnt vmcnt(1) lgkmcnt(0)\n\ts_barrier" ::: "memory");
    else              asm volatile("s_waitcnt lgkmcnt(0)\n\ts_barrier" ::: "memory");
    if (st < 6) read_stage(st + 1);
    __builtin_amdgcn_sched_barrier(0);
#pragma unroll
    for (int ks = 0; ks < 4; ks++) {
      acc[0] = KIND == 1 ? mfma(xf[st & 1][ks], wf[st & 1][ks][0], acc[0]) : mfma(wf[st & 1][ks][0], xf[st & 1][ks], acc[0]);
      if (KIND == 2) sq2(np ? xf[st & 1][ks][2] : xf[st & 1][ks][0], sqa[0], sqa[1]);
      acc[1] = KIND == 1 ? mfma(xf[st & 1][ks], wf[st & 1][ks][1], acc[1]) : mfma(wf[st & 1][ks][1], xf[st & 1][ks], acc[1]);
      if (KIND == 2) sq2(np ? xf[st & 1][ks][3] : xf[st & 1][ks][1], sqa[2], sqa[3]);
      if (st + 4 < 7 && (ks == 0 || ks == 2)) issue_half(st + 4, ks >> 1);
      if (st == 3 && ks == 0) asm volatile("global_load_dword %0, %1, off" : "=v"(sink) : "v"(touch) : "memory");
    }
    if (KIND == 2) {
      cs[0] = mfma(ones, cf[st & 1][0], cs[0]); cs[1] = mfma(ones, cf[st & 1][1], cs[1]);
      asm volatile("" : "+v"(sqa[0]), "+v"(sqa[1]), "+v"(sqa[2]), "+v"(sqa[3]));
    }
  }
  __builtin_amdgcn_sched_barrier(0);
  asm volatile("s_waitcnt vmcnt(0)" : "+v"(sink) :: "memory");
  if (KIND < 2) {
    const float sc = type == 0 ? 0.27264314f : 1.0f;
    const __amdgpu_buffer_rsrc_t rq = __builtin_amdgcn_make_buffer_rsrc(ws + WS_Q, 0, 6u << 20, 0x00020000);
#pragma unroll
    for (int j = 0; j < 2; j++) {
      const int head = sub * 4 + 2 * np + j;
      u32x4 f0, f1;
#pragma unroll
      for (int d = 0; d < 4; d++) {
        f0[d] = pack2(acc[j][2 * d] * sc, acc[j][2 * d + 1] * sc);
        f1[d] = pack2(acc[j][8 + 2 * d] * sc, acc[j][8 + 2 * d + 1] * sc);
      }
      if (KIND == 1) {
        const unsigned v = r == 28 ? 0x3F803F80u : 0u;
#pragma unroll
        for (int d = 0; d < 4; d++) { f0[d] = r >= 28 ? v : f0[d]; f1[d] = r >= 28 ? v : f1[d]; }
      } else {
        f1[2] = hh ? (type == 1 ? 0x3F803F80u : 0u) : f1[2];
        f1[3] = hh ? (type == 1 ? 0x00003F80u : 0u) : f1[3];
      }
      const unsigned o = (unsigned)type * (2u << 20) + (unsigned)(head * 64 + tile) * 2048 + lane * 16;
      st16_sc1(f0, rq, o); st16_sc1(f1, rq, o + 1024);
    }
  } else {
    float sq = (sqa[0] + sqa[1]) + (sqa[2] + sqa[3]);
    sq += other_half(sq, hh);
    if (hh == 0) {
      *(LAS float*)(lds + K1_SQ_OFF + ((np * 4 + w) * 32 + r) * 4) = sq;
      *(LAS float*)(lds + K1_CS_OFF + (w * 128 + (2 * np) * 32 + r) * 4) = cs[0][0];
      *(LAS float*)(lds + K1_CS_OFF + (w * 128 + (2 * np + 1) * 32 + r) * 4) = cs[1][0];
    }
    if (sub == 0 && np == 0 && hh == 1) {
      const float gt = __builtin_amdgcn_rcpf(1.0f + __builtin_amdgcn_exp2f(-1.442695041f * acc[1][13]));
      __hip_atomic_store((GAS float*)(ws + WS_GATE) + tile * 32 + r, gt, __ATOMIC_RELAXED, __HIP_MEMORY_SCOPE_AGENT);
    }
    float sx = acc[1][12];
    { float o = other_half(sx, hh); sx = hh ? sx : o; }
    const float mean = sx * (1.0f / 448.0f);
    asm volatile("s_waitcnt lgkmcnt(0)\n\ts_barrier" ::: "memory");
    sq += *(LAS float*)(lds + K1_SQ_OFF + (((np ^ 1) * 4 + w) * 32 + r) * 4);
    const float var = fmaxf(sq * (1.0f / 448.0f) - mean * mean, 0.f) + 1e-5f;
    const float rstd = __builtin_amdgcn_rsqf(var);
    const float ga = -2.302208198f * rstd, gb = -0.1029432397f * rstd * rstd * rstd, irs = var * rstd;
    const __amdgpu_buffer_rsrc_t rh = __builtin_amdgcn_make_buffer_rsrc(ws + WS_H, 0, TBYTES, 0x00020000);
    unsigned pa[2][4][2];
#pragma unroll
    for (int j = 0; j < 2; j++) {
      const int t = 2 * np + j;
#pragma unroll
      for (int g = 0; g < 4; g++) {
        const LAS char* cp = lds + K1_CS_OFF + (t * 32 + 8 * g + 4 * hh) * 4;
        const f32x4 c = (*(const LAS f32x4*)cp + *(const LAS f32x4*)(cp + 512)) + (*(const LAS f32x4*)(cp + 1024) + *(const LAS f32x4*)(cp + 1536));
        float y[4];
#pragma unroll
        for (int i = 0; i < 4; i++) {
          const float tt = __builtin_fmaf(-mean, c[i], acc[j][4 * g + i]);
          const float e = __builtin_amdgcn_exp2f(tt * __builtin_fmaf(tt * tt, gb, ga));
          y[i] = tt * __builtin_amdgcn_rcpf(__builtin_fmaf(e, irs, irs));
        }
        pa[j][g][0] = pack2(y[0], y[1]); pa[j][g][1] = pack2(y[2], y[3]);
      }
    }
    const unsigned dst = (unsigned)(tile * 56 + 14 * sub + 7 * np) * 512 + r * 16;
    u32x4 v0, v1, v2, v3;
#pragma unroll
    for (int d = 0; d < 2; d++) {
      auto s0 = __builtin_amdgcn_permlane32_swap(pa[0][0][d], pa[0][1][d], false, false);
      v0[d] = s0[0]; v0[2 + d] = s0[1];
      auto s1 = __builtin_amdgcn_permlane32_swap(pa[0][3][d], pa[0][2][d], false, false);
      v1[d] = s1[0]; v1[2 + d] = hh ? pa[0][2][d] : pa[1][0][d];
      auto s2 = __builtin_amdgcn_permlane32_swap(pa[1][0][d], pa[1][2][d], false, false);
      v2[d] = hh ? pa[1][1][d] : s2[1]; v2[2 + d] = hh ? s2[0] : pa[1][1][d];
      auto s3 = __builtin_amdgcn_permlane32_swap(pa[1][2][d], pa[1][2][d], false, false);
      v3[d] = s3[1]; v3[2 + d] = pa[1][3][d];
    }
    st16_sc1(v0, rh, dst + hh * 512);
    st16_sc1(v1, rh, dst + (hh ? 2 : 3) * 512);
    st16_sc1(v2, rh, dst + (4 + hh) * 512);
    st16_sc1(v3, rh, hh ? 0x80000000u : dst + 6 * 512);
  }
}

__global__ __launch_bounds__(512, 2) void fused_kernel(const char* __restrict__ x, const char* __restrict__ wqkv,
                                                       const char* __restrict__ w1, const char* __restrict__ gate_w,
                                                       const char* __restrict__ wo,
                                                       const char* __restrict__ w2, const char* __restrict__ w3,
                                                       const char* __restrict__ w4, char* __restrict__ ws,
                                                       char* __restrict__ out) {
  extern __shared__ __attribute__((aligned(16))) char smem[];
  LAS char* lds = (LAS char*)smem;
  const unsigned lds0 = (unsigned)(uintptr_t)lds;
  LAS unsigned* sy = (LAS unsigned*)(lds + K2_SY_OFF);
  const int tid = threadIdx.x, lane = tid & 63;
  const int wv = __builtin_amdgcn_readfirstlane(tid >> 6);
  const int w = wv & 3;
  const int b = blockIdx.x, xcd = b & 7, bi = b >> 3;
  const int r = lane & 31, hh = lane >> 5;
  const bool has_gemm = bi < 28;
  const int nb = bi % 7, tb64 = xcd * 4 + bi / 7;
  const int ni = w & 1, tj = w >> 1;
  STAMP_DECL(256 + blockIdx.x) STAMP(0);
  if (tid < 16) sy[tid] = 0u;
  {
    const int type = bi & 3, sub = xcd & 3, tg = (xcd >> 2) * 8 + (bi >> 2);
    const int sb_ = 0;
    int k = (b * 8 + wv) * 16 + (lane & 15); k = k < 31360 ? k : 31359;
    const char* touch = k < 3136 ? w2 + (size_t)k * 128 : k < 15680 ? w3 + (size_t)(k - 3136) * 128 : k < 28224 ? w4 + (size_t)(k - 15680) * 128 : wo + (size_t)(k - 28224) * 128;
    if (type < 2)       phase1<0>(lds, x, wqkv + (type * 448 + sub * 112) * 2, 2688, gate_w, ws, type, sub, tg, wv, sb_, touch);
    else if (type == 2) phase1<1>(lds, x, wqkv + (2 * 448 + sub * 112) * 2, 2688, gate_w, ws, type, sub, tg, wv, sb_, touch);
    else                phase1<2>(lds, x, w1 + sub * 224, 896, gate_w, ws, type, sub, tg, wv, sb_, touch);
  }
#define EPOCH_LOAD unsigned epoch; asm volatile("global_load_dword %0, %1, %2 sc1" : "=v"(epoch) : "v"(0u), "s"(ws + WS_EPOCH) : "memory")
#define EPOCH_WAIT(N) asm volatile("s_waitcnt vmcnt(" #N ")" : "+v"(epoch) :: "memory"); epoch = __builtin_amdgcn_readfirstlane(epoch); \
  const unsigned tag = (epoch * 0x9E3779B1u) ^ 0x5bd1e995u
  char* const my_f1 = f1_flag(ws, bi & 3, xcd & 3, ((xcd >> 2) * 8 + (bi >> 2)) * 4);
  STAMP(15);

  if (wv < 4) {
    const int head = 2 * xcd + (bi >> 4), qb = bi & 15;
    EPOCH_LOAD; EPOCH_WAIT(0);
    block_publish(sy + 12, my_f1, lane, tag);
    STAMP(13);
    const __amdgpu_buffer_rsrc_t rq = __builtin_amdgcn_make_buffer_rsrc(ws + WS_Q, 0, 6u << 20, 0x00020000);
    const unsigned qo = (unsigned)(head * 64 + qb * 4) * 2048 + lane * 16;
    const unsigned kho = (2u << 20) + (unsigned)(head * 64) * 2048 + lane * 16, vho = kho + (2u << 20);
    const unsigned ko = kho + w * 16 * 2048, vo = vho + w * 16 * 2048;
#define LDQ(off) __builtin_amdgcn_raw_buffer_load_b128(rq, (off), 0, 16)
    unsigned ea = 0;
    {
      const int hs = head >> 2;
      flags_wait(lane < 8 ? f1_flag(ws, 1 + (lane >> 2), hs, w * 16 + (lane & 3) * 4) : f1_flag(ws, 0, hs, qb * 4));
    }
    STAMP(14);
    __builtin_amdgcn_s_setprio(3);
    u32x4 q[4][2];
#pragma unroll
    for (int i = 0; i < 4; i++) { q[i][0] = LDQ(qo + (w ^ i) * 2048); q[i][1] = LDQ(qo + (w ^ i) * 2048 + 1024); }
    u32x4 kb[4][2], vb[4][2];
#pragma unroll
    for (int j = 0; j < 3; j++) { kb[j][0] = LDQ(ko + j * 2048); kb[j][1] = LDQ(ko + j * 2048 + 1024); vb[j][0] = LDQ(vo + j * 2048); vb[j][1] = LDQ(vo + j * 2048 + 1024); }
    f32x16 o[4];
    {
      LAS float* mx = (LAS float*)(lds + K2_M_OFF);
      f32x16 s = mfma(kb[0][0], q[0][0], zero16());
      s = mfma(kb[0][1], q[0][1], s);
      const float m = max16(s);
      if (hh) mx[w * 32 + r] = m;
      gsync(sy, w, ++ea);
#pragma unroll
      for (int i = 0; i < 4; i++) {
        const float mm = mx[(w ^ i) * 32 + r];
        q[i][1][2] = hh ? (pack2(-mm, 0.f)) : q[i][1][2];
      }
    }
    STAMP(1);
    __builtin_amdgcn_s_setprio(0);
    {
#pragma unroll
      for (int i = 0; i < 4; i++) o[i] = zero16();
      f32x16 sA, sB;
      u32x4 pA0, pA1, pB0, pB1;
      sA = mfma(kb[0][0], q[0][0], zero16());
      sA = mfma(kb[0][1], q[0][1], sA);
      pB0 = pB1 = (u32x4){0u, 0u, 0u, 0u};
      vb[3][0] = vb[3][1] = (u32x4){0u, 0u, 0u, 0u};
#pragma unroll 1
      for (int kq = 0; kq < 4; kq++) {
#pragma unroll
        for (int j = 0; j < 4; j++) {
          const int kt = kq * 4 + j;
          const int jp = (j + 3) & 3, jn = (j + 1) & 3;
          ATT_STEP(sA, sB, pA0, pA1, pB0, pB1, kb[j][0], kb[j][1], q[1][0], q[1][1], vb[jp][0], vb[jp][1], o[3]);
          {
            const int kn = kt + 3 < 16 ? kt + 3 : 15;
            __builtin_amdgcn_s_setprio(3);
            kb[jp][0] = LDQ(ko + kn * 2048); kb[jp][1] = LDQ(ko + kn * 2048 + 1024); vb[jp][0] = LDQ(vo + kn * 2048); vb[jp][1] = LDQ(vo + kn * 2048 + 1024);
            __builtin_amdgcn_sched_barrier(0);
            __builtin_amdgcn_s_setprio(0);
          }
          ATT_STEP(sB, sA, pB0, pB1, pA0, pA1, kb[j][0], kb[j][1], q[2][0], q[2][1], vb[j][0], vb[j][1], o[0]);
          ATT_STEP(sA, sB, pA0, pA1, pB0, pB1, kb[j][0], kb[j][1], q[3][0], q[3][1], vb[j][0], vb[j][1], o[1]);
          ATT_STEP(sB, sA, pB0, pB1, pA0, pA1, kb[jn][0], kb[jn][1], q[0][0], q[0][1], vb[j][0], vb[j][1], o[2]);
        }
      }
      o[3] = mfma(vb[3][0], pB0, o[3]);
      o[3] = mfma(vb[3][1], pB1, o[3]);
    }
    STAMP(2);
    __builtin_amdgcn_s_setprio(3);
    flags_wait(f1_flag(ws, 3, 0, qb * 4));
    const float gate_q = __hip_atomic_load((const GAS float*)(ws + WS_GATE) + (qb * 4 + w) * 32 + r, __ATOMIC_RELAXED, __HIP_MEMORY_SCOPE_AGENT);
    reduce_scatter(lds + K2_A_OFF, o, w, lane, 0, 0, sy, ea);
    float l = o[0][12];
    { float t = other_half(l, hh); l = hh ? l : t; }
    {
      float amax = 0.f;
#pragma unroll
      for (int i = 0; i < 12; i++) amax = fmaxf(amax, fabsf(o[0][i]));
      if (!hh) amax = fmaxf(fmaxf(amax, fabsf(o[0][12])), fmaxf(fabsf(o[0][13]), fmaxf(fabsf(o[0][14]), fabsf(o[0][15]))));
      const bool ok = (l > 1.0e-10f) && (l < 1.0e18f) && (amax < 1.0e37f);
      if (__any(!ok)) {
        flags_wait(f1_flag(ws, 1 + (lane >> 5), head >> 2, (lane & 15) * 4));
        u32x4 q0 = q[0][0], q1 = q[0][1];
        q1[2] = hh ? 0u : q1[2];
        float tm = -3.0e38f;
#pragma unroll 1
        for (int kt = 0; kt < 64; kt++) {
          f32x16 s = mfma(LDQ(kho + kt * 2048), q0, zero16());
          s = mfma(LDQ(kho + kt * 2048 + 1024), q1, s);
          tm = fmaxf(tm, max16(s));
        }
        tm = -fmaxf(tm, other_half(tm, hh));
        const unsigned h0 = pack2(tm, 0.f) & 0xffffu; const float f0 = __builtin_bit_cast(float, h0 << 16);
        const unsigned h1 = pack2(tm - f0, 0.f) & 0xffffu; const float f1 = __builtin_bit_cast(float, h1 << 16);
        const unsigned h2 = pack2((tm - f0) - f1, 0.f) & 0xffffu;
        q1[2] = hh ? (h0 | (h1 << 16)) : q1[2];
        q1[3] = hh ? h2 : q1[3];
        f32x16 oo = zero16();
#pragma unroll 1
        for (int kt = 0; kt < 64; kt++) {
          f32x16 s = mfma(LDQ(kho + kt * 2048), q0, zero16());
          s = mfma(LDQ(kho + kt * 2048 + 1024), q1, s);
          u32x4 p0, p1;
#pragma unroll
          for (int d = 0; d < 4; d++) {
            p0[d] = pack2(__builtin_amdgcn_exp2f(s[2 * d]), __builtin_amdgcn_exp2f(s[2 * d + 1]));
            p1[d] = pack2(__builtin_amdgcn_exp2f(s[8 + 2 * d]), __builtin_amdgcn_exp2f(s[8 + 2 * d + 1]));
          }
          oo = mfma(LDQ(vho + kt * 2048), p0, oo);
          oo = mfma(LDQ(vho + kt * 2048 + 1024), p1, oo);
        }
        o[0] = oo;
        l = oo[12];
        { float t = other_half(l, hh); l = hh ? l : t; }
      }
    }
    {
      const __amdgpu_buffer_rsrc_t ro = __builtin_amdgcn_make_buffer_rsrc(ws + WS_O, 0, TBYTES, 0x00020000);
      const float inv = gate_q * __builtin_amdgcn_rcpf(l);
      const unsigned base = (unsigned)(qb * 4 + w) * (56 * 512) + r * 16;
#pragma unroll
      for (int g = 0; g < 4; g++) {
        u32x2 v; v[0] = pack2(o[0][4 * g] * inv, o[0][4 * g + 1] * inv); v[1] = pack2(o[0][4 * g + 2] * inv, o[0][4 * g + 3] * inv);
        const int u = 7 * head + 2 * g + hh;
        if (g < 3 || hh == 0) st8_sc1(v, ro, base + (u >> 1) * 512 + (u & 1) * 8);
      }
    }
    asm volatile("s_waitcnt vmcnt(0)" ::: "memory");
    flag_set(ws + WS_FO + ((qb * 4 + w) * 16 + head) * 4, lane);
    STAMP(3);
    return;
  }

  if (!has_gemm) {
    EPOCH_LOAD; EPOCH_WAIT(0);
    block_publish(sy + 12, my_f1, lane, tag);
    if (b == 224 && w == 3) {
#pragma unroll 1
      for (int i = 0; i < 4; i++) flags_wait(ws + WS_F1 + (i * 64 + lane) * 128);
      if (lane == 0) __hip_atomic_store((GAS unsigned*)(ws + WS_EPOCH), epoch + 1u, __ATOMIC_RELAXED, __HIP_MEMORY_SCOPE_AGENT);
    }
    const int id = (xcd * 4 + bi - 28) * 2 + w;
    if (w < 2 && id < 56) {
      const unsigned reg = K2_B_OFF + w * 28672;
      const unsigned dv = (lane >> 2) * 3584 + (lane & 3) * 16;
      const char* src = w3 + id * 64;
#pragma unroll
      for (int j = 0; j < 28; j++) dma16(dv, src + (size_t)j * (16 * 3584), lds0 + reg + j * 1024);
      asm volatile("s_waitcnt vmcnt(0)" ::: "memory");
      const unsigned to = reg + (8 * hh + ((lane & 15) >> 2)) * 64 + ((lane >> 4) & 1) * 32 + (lane & 3) * 8;
      u32x4 ones; ones[0] = ones[1] = ones[2] = ones[3] = 0x3F803F80u;
      f32x16 cs = zero16();
#pragma unroll
      for (int s = 0; s < 28; s++) cs = mfma(ones, tr_frag(lds + to + s * 1024, 256), cs);
      if (hh == 0) __hip_atomic_store((GAS float*)(ws + WS_CS3) + id * 32 + r, cs[0], __ATOMIC_RELAXED, __HIP_MEMORY_SCOPE_AGENT);
      asm volatile("s_waitcnt vmcnt(0)" ::: "memory");
      flag_set(ws + WS_FCS + id * 4, lane);
    }
    return;
  }
  PRIO_LO;
  unsigned eb = 0;
  LAS unsigned* syb = sy + 4;
  const int nb3 = bi % 14, tb128 = xcd * 2 + bi / 14;
  const unsigned dv64 = w64_dma_voff(lane, 896);
  const unsigned t0 = w64_tr_off(lane, ni), t1 = w64_tr_off(lane, ni ^ 1);
  const unsigned dv3 = (lane >> 4) * 3584 + (((lane & 15) ^ ((lane >> 4) << 2)) * 16);
  const char* w3src = w3 + nb3 * 256;
  unsigned x1p[8];
  const unsigned sl = K2_B_OFF + 57344 + w * 14336;
  EPOCH_LOAD;
  {
    const char* wsrc = w2 + (size_t)(112 * w) * 896 + nb * 128;
#pragma unroll
    for (int j = 0; j < 14; j++) dma16(dv64, wsrc + j * (8 * 896), lds0 + sl + j * 1024);
  }
  EPOCH_WAIT(14);
  block_publish(sy + 12, my_f1, lane, tag);
  STAMP(14);
#pragma unroll
  for (int j = 0; j < 14; j++) { const int row0 = 56 * w + 4 * j; dma16(dv3, w3src + (size_t)row0 * 3584, lds0 + K2_B_OFF + row0 * 256); }
  {
    const u32x4 rh = mkrsrc(ws + WS_H, TBYTES);
    const int delta = w == 0 ? -1 : w == 1 ? 1 : w == 2 ? -2 : 2;
    u32x4 af[7][2];
    unsigned av[2];
#pragma unroll
    for (int e = 0; e < 2; e++) {
      const int tok = (tb64 * 2 + (tj ^ e)) * 32 + r + delta;
      av[e] = (tok >= 0 && tok < SEQ) ? (unsigned)(((tok >> 5) * 56 + 14 * w + hh) * 512 + (tok & 31) * 16) : 0x80000000u;
    }
    u32x2 xr[4];
    {
      const char* xp = x + (size_t)((tb64 * 2 + tj) * 32) * 896 + (nb * 64 + ni * 32) * 2;
      const unsigned xo = r * 896 + hh * 8;
#pragma unroll
      for (int g = 0; g < 4; g++) asm volatile("global_load_dwordx2 %0, %1, %2 offset:%3" : "=v"(xr[g]) : "v"(xo), "s"(xp), "i"(g * 16) : "memory");
    }
    {
      int tl = tb64 * 2 - 1 + (lane & 3); tl = tl < 0 ? 0 : tl > 63 ? 63 : tl;
      flags_wait(f1_flag(ws, 3, w, tl));
    }
    asm volatile("s_waitcnt vmcnt(0)" : "+v"(xr[0]), "+v"(xr[1]), "+v"(xr[2]), "+v"(xr[3]) :: "memory");
    STAMP(10);
#pragma unroll
    for (int s = 0; s < 7; s++) {
      BLOAD_SC1(af[s][0], av[0], rh, (s >> 2) * 4096, (s & 3) * 1024);
      BLOAD_SC1(af[s][1], av[1], rh, (s >> 2) * 4096, (s & 3) * 1024);
    }
    PRIO_HI;
    f32x16 t[4];
#pragma unroll
    for (int i = 0; i < 4; i++) t[i] = zero16();
    u32x4 f0[7], f1[7];
#pragma unroll
    for (int s = 0; s < 7; s++) { f0[s] = tr_frag(lds + sl + t0 + s * 2048, 512); f1[s] = tr_frag(lds + sl + t1 + s * 2048, 512); }
    STAMP(1);
#pragma unroll
    for (int s = 0; s < 7; s++) {
      asm volatile("s_waitcnt vmcnt(%2)" : "+v"(af[s][0]), "+v"(af[s][1]) : "i"(12 - 2 * s) : "memory");
      t[0] = mfma(f0[s], af[s][0], t[0]); t[1] = mfma(f1[s], af[s][0], t[1]);
      t[2] = mfma(f0[s], af[s][1], t[2]); t[3] = mfma(f1[s], af[s][1], t[3]);
      __builtin_amdgcn_sched_barrier(0);
    }
    STAMP(11);
    asm volatile("s_waitcnt lgkmcnt(0)" ::: "memory");
#pragma unroll
    for (int i = 1; i < 4; i++) tile_put(lds + sl + (i - 1) * 4096, t[i], lane);
    gsync(syb, w, ++eb);
#pragma unroll
    for (int i = 1; i < 4; i++) tile_add(lds + K2_B_OFF + 57344 + (w ^ i) * 14336 + (i - 1) * 4096, t[0], lane);
    float s1 = 0.f, s2 = 0.f;
#pragma unroll
    for (int g = 0; g < 4; g++) {
      const float a0 = __builtin_bit_cast(float, xr[g][0] << 16) + t[0][4 * g], a1 = __builtin_bit_cast(float, xr[g][0] & 0xffff0000u) + t[0][4 * g + 1];
      const float a2 = __builtin_bit_cast(float, xr[g][1] << 16) + t[0][4 * g + 2], a3 = __builtin_bit_cast(float, xr[g][1] & 0xffff0000u) + t[0][4 * g + 3];
      x1p[2 * g] = pack2(a0, a1); x1p[2 * g + 1] = pack2(a2, a3);
    }
    const __amdgpu_buffer_rsrc_t rx = __builtin_amdgcn_make_buffer_rsrc(ws + WS_X1, 0, TBYTES, 0x00020000);
    const int tile = tb64 * 2 + tj;
#pragma unroll
    for (int gp = 0; gp < 2; gp++) {
      auto a = __builtin_amdgcn_permlane32_swap(x1p[4 * gp], x1p[4 * gp + 2], false, false);
      auto c = __builtin_amdgcn_permlane32_swap(x1p[4 * gp + 1], x1p[4 * gp + 3], false, false);
      u32x4 v; v[0] = a[0]; v[1] = c[0]; v[2] = a[1]; v[3] = c[1];
      st16_sc1(v, rx, (unsigned)(tile * 56 + nb * 8 + ni * 4 + 2 * gp + hh) * 512 + r * 16);
    }
#pragma unroll
    for (int j = 0; j < 8; j++) {
      const float lo = __builtin_bit_cast(float, x1p[j] << 16), hi = __builtin_bit_cast(float, x1p[j] & 0xffff0000u);
      s1 += lo + hi; s2 = __builtin_fmaf(lo, lo, s2); s2 = __builtin_fmaf(hi, hi, s2);
    }
    s1 += other_half(s1, hh); s2 += other_half(s2, hh);
    const __amdgpu_buffer_rsrc_t rs = __builtin_amdgcn_make_buffer_rsrc(ws + WS_STATS, 0, 64 * 14 * 32 * 8, 0x00020000);
    { u32x2 v; v[0] = __builtin_bit_cast(unsigned, s1); v[1] = __builtin_bit_cast(unsigned, s2); st8_sc1(v, rs, hh ? 0x80000000u : (unsigned)((tile * 14 + nb * 2 + ni) * 32 + r) * 8); }
    STAMP(12);
    gsync(syb, w, ++eb);
#pragma unroll
    for (int j = 0; j < 14; j++) { const int row0 = 224 + 56 * w + 4 * j; dma16(dv3, w3src + (size_t)row0 * 3584, lds0 + K2_B_OFF + row0 * 256); }
    asm volatile("s_waitcnt vmcnt(14)" ::: "memory");
    flag_set(ws + WS_FX1 + (tile * 16 + nb * 2 + ni) * 4, lane);
    STAMP(2);
  }
  {
    asm volatile("s_waitcnt vmcnt(0)" ::: "memory");
    gsync(syb, w, ++eb);
    unsigned tb[4];
    {
      const int blk = (lane >> 4) & 1, q = (lane & 15) >> 2, p = lane & 3;
#pragma unroll
      for (int n = 0; n < 4; n++) tb[n] = K2_B_OFF + (8 * hh + q) * 256 + ((n ^ q) * 64) + blk * 32 + p * 8;
    }
    STAMP(3);
    const int tile = tb128 * 4 + w;
    flags_wait(lane < 16 ? ws + WS_FX1 + (tile * 16 + (lane < 14 ? lane : 0)) * 4 : lane < 32 ? ws + WS_FCS + (nb3 * 4 + (lane & 3)) * 4
               : f1_flag(ws, 3, 0, lane < 48 ? tile : tb64 * 2 + tj));
    STAMP(4);
    const u32x4 rx = mkrsrc(ws + WS_X1, TBYTES);
    const u32x4 rsd = mkrsrc(ws + WS_STATS, 64 * 14 * 32 * 8);
    const u32x4 rcd = mkrsrc(ws + WS_CS3, 1792 * 4);
    u32x2 sv[7], cv; float g3;
    {
      const unsigned so = (unsigned)((tile * 14 + hh * 7) * 32 + r) * 8, co = (unsigned)(nb3 * 128 + lane * 2) * 4;
#pragma unroll
      for (int j = 0; j < 7; j++) asm volatile("buffer_load_dwordx2 %0, %1, %2, 0 offen offset:%3 sc1" : "=v"(sv[j]) : "v"(so), "s"(rsd), "i"(j * 256) : "memory");
      asm volatile("buffer_load_dwordx2 %0, %1, %2, 0 offen sc1" : "=v"(cv) : "v"(co), "s"(rcd) : "memory");
      const unsigned go = (unsigned)(tile * 32 + r) * 4;
      asm volatile("global_load_dword %0, %1, %2 sc1" : "=v"(g3) : "v"(go), "s"(ws + WS_GATE) : "memory");
    }
    u32x4 af[28];
    const unsigned av = (unsigned)(tile * 56) * 512 + lane * 16;
#pragma unroll
    for (int s = 0; s < 28; s++) BLOAD_SC1(af[s], av, rx, (s >> 2) * 4096, (s & 3) * 1024);
    const __amdgpu_buffer_rsrc_t r3 = __builtin_amdgcn_make_buffer_rsrc(ws + WS_H3, 0, T3BYTES, 0x00020000);
    STAMP(13);
    PRIO_G3;
    constexpr int PD = 6;
    u32x4 wfr[PD];
#pragma unroll
    for (int u = 0; u < PD; u++) wfr[u] = tr_frag(lds + tb[0] + u * 4096, 1024);
    asm volatile("s_waitcnt vmcnt(28)" : "+v"(sv[0]), "+v"(sv[1]), "+v"(sv[2]), "+v"(sv[3]), "+v"(sv[4]), "+v"(sv[5]), "+v"(sv[6]), "+v"(cv), "+v"(g3) :: "memory");
    const unsigned csl = K2_CSP_OFF + w * 512;
    *(LAS u32x2*)(lds + csl + lane * 8) = cv;
    float nmean, ga, gb, irs;
    {
      float s1 = 0.f, s2 = 0.f;
#pragma unroll
      for (int j = 0; j < 7; j++) { const unsigned u0 = sv[j][0], u1 = sv[j][1]; s1 += __builtin_bit_cast(float, u0); s2 += __builtin_bit_cast(float, u1); }
      s1 += other_half(s1, hh); s2 += other_half(s2, hh);
      const float mean = s1 * (1.0f / 448.0f);
      const float var = fmaxf(s2 * (1.0f / 448.0f) - mean * mean, 0.f) + 1e-5f;
      const float rstd = __builtin_amdgcn_rsqf(var);
      nmean = -mean; ga = -2.302208198f * rstd; gb = -0.1029432397f * rstd * rstd * rstd;
      irs = var * rstd * __builtin_amdgcn_rcpf(fmaxf(1.0f - g3, 1.0e-30f));
    }
    f32x16 acc[2];
    unsigned pk[8];
    f32x4 c4;
    float tt[4], ee[4];
#pragma unroll
    for (int n = 0; n < 5; n++) {
#pragma unroll
      for (int s = 0; s < 28; s++) {
        const int u = n * 28 + s;
        if (n < 4) {
          if (n == 0) asm volatile("s_waitcnt vmcnt(%1)" : "+v"(af[s]) : "i"(27 - s) : "memory");
          acc[n & 1] = mfma(wfr[u % PD], af[s], s == 0 ? zero16() : acc[n & 1]);
          if (u + PD < 112) wfr[u % PD] = tr_frag(lds + tb[(u + PD) / 28] + ((u + PD) % 28) * 4096, 1024);
        }
        if (n >= 1 && s < 24) {
          const int pn = n - 1, g = s / 6, ph = s % 6;
          if (ph == 0) c4 = *(const LAS f32x4*)(lds + csl + (pn * 32 + 8 * g + 4 * hh) * 4);
          if (ph == 1) {
#pragma unroll
            for (int i = 0; i < 4; i++) { tt[i] = __builtin_fmaf(nmean, c4[i], acc[pn & 1][4 * g + i]); ee[i] = tt[i] * tt[i]; }
          }
          if (ph == 2) {
#pragma unroll
            for (int i = 0; i < 4; i++) ee[i] = tt[i] * __builtin_fmaf(ee[i], gb, ga);
          }
          if (ph == 3) {
#pragma unroll
            for (int i = 0; i < 4; i++) ee[i] = __builtin_amdgcn_exp2f(ee[i]);
          }
          if (ph == 4) {
#pragma unroll
            for (int i = 0; i < 4; i++) ee[i] = __builtin_amdgcn_rcpf(__builtin_fmaf(ee[i], irs, irs));
          }
          if (ph == 5) {
            pk[2 * g] = pack2(tt[0] * ee[0], tt[1] * ee[1]); pk[2 * g + 1] = pack2(tt[2] * ee[2], tt[3] * ee[3]);
            if (g & 1) {
              const int gp = g >> 1;
              auto a = __builtin_amdgcn_permlane32_swap(pk[4 * gp], pk[4 * gp + 2], false, false);
              auto c = __builtin_amdgcn_permlane32_swap(pk[4 * gp + 1], pk[4 * gp + 3], false, false);
              u32x4 v; v[0] = a[0]; v[1] = c[0]; v[2] = a[1]; v[3] = c[1];
              st16_sc1(v, r3, (unsigned)(tile * 224 + nb3 * 16 + pn * 4 + 2 * gp + hh) * 512 + r * 16);
            }
          }
        }
        if (n < 4) __builtin_amdgcn_sched_barrier(0);
      }
    }
    PRIO_HI;
    STAMP(5);
    gsync(syb, w, ++eb);
    asm volatile("s_waitcnt vmcnt(0)" ::: "memory");
    flag_set(ws + WS_FH3 + (tile * 16 + nb3) * 4, lane);
    PRIO_LO;
  }
  {
    const unsigned ring = K2_B_OFF + w * 28672;
    const char* wsrc = w4 + (size_t)(448 * w) * 896 + nb * 128;
#pragma unroll
    for (int j = 0; j < 28; j++) dma16(dv64, wsrc + j * (8 * 896), lds0 + ring + j * 1024);
    const float gate = __hip_atomic_load((const GAS float*)(ws + WS_GATE) + (tb64 * 2 + tj) * 32 + r, __ATOMIC_RELAXED, __HIP_MEMORY_SCOPE_AGENT);
    {
      const int blk0 = (7 * w) >> 1;
      const int k = lane & 3;
      flags_wait(ws + WS_FH3 + ((tb64 * 2 + ((lane >> 2) & 1)) * 16 + blk0 + k) * 4);
    }
    asm volatile("s_waitcnt vmcnt(0)" ::: "memory");
    STAMP(6);
    const u32x4 r3 = mkrsrc(ws + WS_H3, T3BYTES);
    unsigned av[2];
#pragma unroll
    for (int e = 0; e < 2; e++) av[e] = (unsigned)((tb64 * 2 + (tj ^ e)) * 224 + 56 * w) * 512 + lane * 16;
    u32x4 af[14][2];
#pragma unroll
    for (int s = 0; s < 14; s++)
#pragma unroll
      for (int e = 0; e < 2; e++) BLOAD_SC1(af[s][e], av[e], r3, (s >> 2) * 4096, (s & 3) * 1024);
    f32x16 t[4];
#pragma unroll
    for (int i = 0; i < 4; i++) t[i] = zero16();
    u32x4 f0[2], f1[2];
    const char* wosrc = wo + (size_t)(112 * w) * 896 + nb * 128;
    f0[0] = tr_frag(lds + ring + t0, 512); f1[0] = tr_frag(lds + ring + t1, 512);
#pragma unroll
    for (int s = 0; s < 28; s++) {
      const int sl = s % 14;
      asm volatile("s_waitcnt lgkmcnt(0)" : "+v"(f0[s & 1]), "+v"(f1[s & 1]) :: "memory");
      if (s + 1 < 28) {
        const int nl = (s + 1) % 14;
        if (s == 13) asm volatile("s_waitcnt vmcnt(50)" ::: "memory");
        else if (s > 13) asm volatile("s_waitcnt vmcnt(%0)" :: "i"(2 + 4 * (13 - nl) + 2 * (s - 14 < 7 ? s - 14 : 7)) : "memory");
        f0[(s + 1) & 1] = tr_frag(lds + ring + t0 + nl * 2048, 512); f1[(s + 1) & 1] = tr_frag(lds + ring + t1 + nl * 2048, 512);
      }
      if (s < 14) {
        dma16(dv64, wsrc + (28 + 2 * s) * (8 * 896), lds0 + ring + (2 * s) * 1024);
        dma16(dv64, wsrc + (29 + 2 * s) * (8 * 896), lds0 + ring + (2 * s + 1) * 1024);
        asm volatile("s_waitcnt vmcnt(%2)" : "+v"(af[s][0]), "+v"(af[s][1]) : "i"(2 * (13 - s) + 4 * s + 2) : "memory");
      } else {
        if (s < 21) {
          dma16(dv64, wosrc + (2 * sl) * (8 * 896), lds0 + ring + (2 * sl) * 1024);
          dma16(dv64, wosrc + (2 * sl + 1) * (8 * 896), lds0 + ring + (2 * sl + 1) * 1024);
        }
        asm volatile("s_waitcnt vmcnt(%2)" : "+v"(af[sl][0]), "+v"(af[sl][1]) : "i"(4 * (13 - sl) + 2 * (s - 13 < 7 ? s - 13 : 7)) : "memory");
      }
      t[0] = mfma(f0[s & 1], af[sl][0], t[0]); t[1] = mfma(f1[s & 1], af[sl][0], t[1]);
      t[2] = mfma(f0[s & 1], af[sl][1], t[2]); t[3] = mfma(f1[s & 1], af[sl][1], t[3]);
      if (s < 14) {
        BLOAD_SC1(af[s][0], av[0], r3, ((s + 14) >> 2) * 4096, ((s + 14) & 3) * 1024);
        BLOAD_SC1(af[s][1], av[1], r3, ((s + 14) >> 2) * 4096, ((s + 14) & 3) * 1024);
      }
      __builtin_amdgcn_sched_barrier(0);
    }
    STAMP(7);
    PRIO_HI;
    flags_wait(ws + WS_FO + ((tb64 * 2 + ((lane >> 2) & 1)) * 16 + 4 * w + (lane & 3)) * 4);
    asm volatile("s_waitcnt vmcnt(0)" ::: "memory");
    STAMP(8);
    {
      const u32x4 ro = mkrsrc(ws + WS_O, TBYTES);
      unsigned ov[2];
#pragma unroll
      for (int e = 0; e < 2; e++) ov[e] = (unsigned)((tb64 * 2 + (tj ^ e)) * 56 + 14 * w) * 512 + lane * 16;
#pragma unroll
      for (int s = 0; s < 7; s++)
#pragma unroll
        for (int e = 0; e < 2; e++) BLOAD_SC1(af[s][e], ov[e], ro, (s >> 2) * 4096, (s & 3) * 1024);
      u32x4 g0[7], g1[7];
#pragma unroll
      for (int s = 0; s < 7; s++) { g0[s] = tr_frag(lds + ring + t0 + s * 2048, 512); g1[s] = tr_frag(lds + ring + t1 + s * 2048, 512); }
#pragma unroll
      for (int s = 0; s < 7; s++) {
        asm volatile("s_waitcnt vmcnt(%2)" : "+v"(af[s][0]), "+v"(af[s][1]) : "i"(2 * (6 - s)) : "memory");
        t[0] = mfma(g0[s], af[s][0], t[0]); t[1] = mfma(g1[s], af[s][0], t[1]);
        t[2] = mfma(g0[s], af[s][1], t[2]); t[3] = mfma(g1[s], af[s][1], t[3]);
      }
    }
    asm volatile("s_waitcnt lgkmcnt(0)" ::: "memory");
#pragma unroll
    for (int i = 1; i < 4; i++) tile_put(lds + ring + (i - 1) * 4096, t[i], lane);
    gsync(syb, w, ++eb);
#pragma unroll
    for (int i = 1; i < 4; i++) tile_add(lds + K2_B_OFF + (w ^ i) * 28672 + (i - 1) * 4096, t[0], lane);
    unsigned pk[8];
    const float og = 1.0f - gate;
#pragma unroll
    for (int j = 0; j < 8; j++)
      pk[j] = pack2(__builtin_fmaf(og, __builtin_bit_cast(float, x1p[j] << 16), t[0][2 * j]), __builtin_fmaf(og, __builtin_bit_cast(float, x1p[j] & 0xffff0000u), t[0][2 * j + 1]));
    char* op = out + (size_t)((tb64 * 2 + tj) * 32 + r) * 896 + (nb * 64 + ni * 32) * 2 + hh * 16;
#pragma unroll
    for (int gp = 0; gp < 2; gp++) {
      auto a2 = __builtin_amdgcn_permlane32_swap(pk[4 * gp], pk[4 * gp + 2], false, false);
      auto c2 = __builtin_amdgcn_permlane32_swap(pk[4 * gp + 1], pk[4 * gp + 3], false, false);
      u32x4 v; v[0] = a2[0]; v[1] = c2[0]; v[2] = a2[1]; v[3] = c2[1];
      *(u32x4*)(op + gp * 32) = v;
    }
    STAMP(9);
  }
}

extern "C" void kernel_launch(void* const* d_in, const int* in_sizes, int n_in,
                              void* d_out, int out_size, void* d_ws, size_t ws_size,
                              hipStream_t stream) {
  const char* x = (const char*)d_in[0]; const char* wqkv = (const char*)d_in[1]; const char* wo = (const char*)d_in[2];
  const char* w1 = (const char*)d_in[4]; const char* w2 = (const char*)d_in[5]; const char* w3 = (const char*)d_in[6];
  const char* w4 = (const char*)d_in[7]; const char* gw = (const char*)d_in[8];
  (void)hipFuncSetAttribute((const void*)fused_kernel, hipFuncAttributeMaxDynamicSharedMemorySize, K2_LDS);
  fused_kernel<<<256, 512, K2_LDS, stream>>>(x, wqkv, w1, gw, wo, w2, w3, w4, (char*)d_ws, (char*)d_out);
}
```

```cpp
#include <hip/hip_runtime.h>
#include <stdint.h>

#define TOUCH 3
#define PL 2
#define LDPRIO

typedef __attribute__((ext_vector_type(8))) __bf16 bf16x8;
typedef __attribute__((ext_vector_type(2))) __bf16 bf16x2;
typedef __attribute__((ext_vector_type(4))) short s16x4;
typedef __attribute__((ext_vector_type(16))) float f32x16;
typedef __attribute__((ext_vector_type(4))) float f32x4;
typedef __attribute__((ext_vector_type(2))) float f32x2;
typedef __attribute__((ext_vector_type(4))) unsigned u32x4;
typedef __attribute__((ext_vector_type(2))) unsigned u32x2;
#define LAS __attribute__((address_space(3)))
#define GAS __attribute__((address_space(1)))
#define DI __device__ __forceinline__

constexpr int SEQ = 2048, DM = 448, NH = 16, DH = 28, FF = 1792;

constexpr size_t WS_CNT   = 0;
constexpr size_t WS_GATE  = 16384;
constexpr size_t WS_STATS = 32768;
constexpr size_t WS_Q     = 1u << 20;
constexpr size_t WS_K     = WS_Q + (2u << 20);
constexpr size_t WS_V     = WS_K + (2u << 20);
constexpr size_t WS_H     = WS_V + (2u << 20);
constexpr size_t WS_X1    = WS_H + (2u << 20);
constexpr size_t WS_O     = WS_X1 + (2u << 20);
constexpr size_t WS_H3    = WS_O + (2u << 20);
constexpr unsigned TBYTES = 64u * 56u * 512u;
constexpr unsigned T3BYTES = 64u * 224u * 512u;

DI f32x16 mfma(u32x4 a, u32x4 b, f32x16 c) {
  return __builtin_amdgcn_mfma_f32_32x32x16_bf16(__builtin_bit_cast(bf16x8, a), __builtin_bit_cast(bf16x8, b), c, 0, 0, 0);
}
DI unsigned pack2(float a, float b) {
  f32x2 v; v[0] = a; v[1] = b;
  return __builtin_bit_cast(unsigned, __builtin_convertvector(v, bf16x2));
}
DI u32x4 tr_frag(const LAS char* p, int hi_off) {
  s16x4 lo = __builtin_amdgcn_ds_read_tr16_b64_v4i16((LAS s16x4*)p);
  s16x4 hi = __builtin_amdgcn_ds_read_tr16_b64_v4i16((LAS s16x4*)(p + hi_off));
  u32x2 a = __builtin_bit_cast(u32x2, lo), b = __builtin_bit_cast(u32x2, hi);
  u32x4 r; r[0] = a[0]; r[1] = a[1]; r[2] = b[0]; r[3] = b[1];
  return r;
}
DI float gelu_f(float x) {
  const float C0 = -2.302208198f;
  const float C1 = -0.1029432397f;
  float z = x * __builtin_fmaf(x * x, C1, C0);
  float e = __builtin_amdgcn_exp2f(z);
  return x * __builtin_amdgcn_rcpf(1.0f + e);
}
DI unsigned gelu2(float a0, float a1, float c0, float c1, float nmean, float ga, float gb, float irs) {
  const f32x2 a = {a0, a1}, c = {c0, c1}, nm = {nmean, nmean}, ga2 = {ga, ga}, gb2 = {gb, gb}, ir = {irs, irs};
  const f32x2 t = __builtin_elementwise_fma(nm, c, a);
  const f32x2 z = t * __builtin_elementwise_fma(t * t, gb2, ga2);
  f32x2 e; e[0] = __builtin_amdgcn_exp2f(z[0]); e[1] = __builtin_amdgcn_exp2f(z[1]);
  const f32x2 d = __builtin_elementwise_fma(e, ir, ir);
  f32x2 rr; rr[0] = __builtin_amdgcn_rcpf(d[0]); rr[1] = __builtin_amdgcn_rcpf(d[1]);
  const f32x2 y = t * rr;
  return __builtin_bit_cast(unsigned, __builtin_convertvector(y, bf16x2));
}
DI float other_half(float v, int hh) {
  unsigned u = __builtin_bit_cast(unsigned, v);
  auto r = __builtin_amdgcn_permlane32_swap(u, u, false, false);
  return __builtin_bit_cast(float, hh ? r[0] : r[1]);
}
DI void sq2(unsigned v, float& a, float& b) {
  const float lo = __builtin_bit_cast(float, v << 16), hi = __builtin_bit_cast(float, v & 0xffff0000u);
  a = __builtin_fmaf(lo, lo, a); b = __builtin_fmaf(hi, hi, b);
}
#define STAMP(i)
#define STAMP_DECL(base)
#define STAMP_FLUSH(base, n)
DI f32x16 zero16() { f32x16 z; for (int i = 0; i < 16; i++) z[i] = 0.f; return z; }

DI void dma16_masked(unsigned voff, const void* sbase, unsigned ldsdst, uint64_t mask) {
  unsigned keep;
  asm volatile("s_mov_b32 %0, m0\n\ts_mov_b32 m0, %3\n\ts_mov_b64 exec, %4\n\tglobal_load_lds_dwordx4 %1, %2\n\ts_mov_b64 exec, -1\n\ts_mov_b32 m0, %0"
               : "=&s"(keep) : "v"(voff), "s"(sbase), "s"(ldsdst), "s"(mask) : "memory");
}
DI void dma16(unsigned voff, const void* sbase, unsigned ldsdst) {
  unsigned keep;
  asm volatile("s_mov_b32 %0, m0\n\ts_mov_b32 m0, %3\n\ts_nop 0\n\tglobal_load_lds_dwordx4 %1, %2\n\ts_mov_b32 m0, %0"
               : "=&s"(keep) : "v"(voff), "s"(sbase), "s"(ldsdst) : "memory");
}

constexpr int K1_SLOT    = 32768;
constexpr int K1_AUX_OFF = 151040;
constexpr int K1_CS_OFF  = K1_AUX_OFF + 3584;
constexpr int K1_SQ_OFF  = K1_CS_OFF + 2048;
constexpr int K1_LDS     = K1_SQ_OFF + 1024;

DI u32x4 tr_frag2(const LAS char* p0, const LAS char* p1) {
  s16x4 lo = __builtin_amdgcn_ds_read_tr16_b64_v4i16((LAS s16x4*)p0);
  s16x4 hi = __builtin_amdgcn_ds_read_tr16_b64_v4i16((LAS s16x4*)p1);
  u32x2 a = __builtin_bit_cast(u32x2, lo), b = __builtin_bit_cast(u32x2, hi);
  u32x4 r; r[0] = a[0]; r[1] = a[1]; r[2] = b[0]; r[3] = b[1];
  return r;
}
DI unsigned k1_tr_off(int lane, int t) {
  const int hh = lane >> 5, blk = (lane >> 4) & 1, q = (lane & 15) >> 2, p = lane & 3;
  int u = 7 * t + 4 * blk + p;
  if (blk == 1 && p == 3) u = 7 * t;
  return (8 * hh + q) * 256 + (((u >> 1) ^ (q << 2)) * 16) + (u & 1) * 8;
}

constexpr int K2_B_OFF   = 0;
constexpr int K2_A_OFF   = 114688;
constexpr int K2_CSP_OFF = K2_A_OFF + 32768;
constexpr int K2_CS_OFF  = K2_CSP_OFF + 2048;
constexpr int K2_M_OFF   = K2_CS_OFF + 512;
constexpr int K2_SY_OFF  = K2_M_OFF + 512;
constexpr int K2_END     = K2_SY_OFF + 64;
static_assert(K2_END <= K1_AUX_OFF, "lds map");
constexpr int K2_LDS     = K1_LDS;
constexpr unsigned SPIN_CAP = 1u << 20;
#define POLL_SLEEP 4
#define PH 2
#define PG3 2
#define PRIO_G3 __builtin_amdgcn_s_setprio(PG3)
#define PRIO_HI __builtin_amdgcn_s_setprio(PH)
#define PRIO_LO __builtin_amdgcn_s_setprio(PL)
constexpr size_t WS_FX1 = WS_CNT, WS_FH3 = WS_CNT + 4096, WS_FO = WS_CNT + 8192, WS_FCS = WS_CNT + 12288;
constexpr size_t WS_EPOCH = WS_CNT + 12288 + 1024;
constexpr size_t WS_F1 = 524288;
constexpr size_t WS_CS3 = 24576;

DI void gsync(LAS unsigned* f, int w, unsigned epoch) {
  asm volatile("s_waitcnt lgkmcnt(0)" ::: "memory");
  __hip_atomic_store(f + w, epoch, __ATOMIC_RELAXED, __HIP_MEMORY_SCOPE_WORKGROUP);
  for (unsigned s = 0; s < SPIN_CAP; s++) {
    u32x4 v;
    asm volatile("ds_read_b128 %0, %1\n\ts_waitcnt lgkmcnt(0)" : "=v"(v) : "v"((unsigned)(uintptr_t)f) : "memory");
    const unsigned a = v[0], b = v[1], c = v[2], d = v[3];
    const unsigned m = min(min(a, b), min(c, d));
    if ((unsigned)__builtin_amdgcn_readfirstlane(m) >= epoch) break;
    __builtin_amdgcn_s_sleep(1);
  }
}
DI void flag_set_t(char* p, int lane, unsigned tag) {
  if (lane == 0) __hip_atomic_store((GAS unsigned*)p, tag, __ATOMIC_RELAXED, __HIP_MEMORY_SCOPE_AGENT);
}
#define flag_set(p, lane) flag_set_t(p, lane, tag)
#define flags_wait(p) flags_wait_t(p, tag)
DI char* f1_flag(char* ws, int type, int sub, int tile) { return ws + WS_F1 + ((type * 4 + sub) * 16 + (tile >> 2)) * 128; }
DI void block_publish(LAS unsigned* cnt, char* p, int lane, unsigned tag) {
  if (lane == 0) {
    const unsigned old = __hip_atomic_fetch_add(cnt, 1u, __ATOMIC_RELAXED, __HIP_MEMORY_SCOPE_WORKGROUP);
    if (old == 7u) __hip_atomic_store((GAS unsigned*)p, tag, __ATOMIC_RELAXED, __HIP_MEMORY_SCOPE_AGENT);
  }
}
DI void flags_wait_t(const char* p, unsigned tag) {
  for (unsigned s = 0; s < SPIN_CAP; s++) {
    const unsigned v = __hip_atomic_load((const GAS unsigned*)p, __ATOMIC_RELAXED, __HIP_MEMORY_SCOPE_AGENT);
    if (__all(v == tag)) break;
    __builtin_amdgcn_s_sleep(POLL_SLEEP);
  }
  asm volatile("" ::: "memory");
}
DI u32x4 mkrsrc(const void* p, unsigned bytes) {
  const uint64_t a = (uint64_t)(uintptr_t)p;
  u32x4 r; r[0] = (unsigned)a; r[1] = (unsigned)(a >> 32) & 0xffffu; r[2] = bytes; r[3] = 0x00020000u;
  return r;
}
#define BLOAD(dst, voff, rs, soff, imm) \
  asm volatile("buffer_load_dwordx4 %0, %1, %2, %3 offen offset:%4" : "=v"(dst) : "v"(voff), "s"(rs), "s"(soff), "i"(imm) : "memory")
#define BLOAD_SC1(dst, voff, rs, soff, imm) \
  asm volatile("buffer_load_dwordx4 %0, %1, %2, %3 offen offset:%4 sc1" : "=v"(dst) : "v"(voff), "s"(rs), "s"(soff), "i"(imm) : "memory")
DI void st16_sc1(u32x4 v, __amdgpu_buffer_rsrc_t rs, unsigned off) { __builtin_amdgcn_raw_buffer_store_b128(v, rs, off, 0, 16); }
DI void st8_sc1(u32x2 v, __amdgpu_buffer_rsrc_t rs, unsigned off) { __builtin_amdgcn_raw_buffer_store_b64(v, rs, off, 0, 16); }

DI void tile_put(LAS char* p, const f32x16& t, int lane) {
#pragma unroll
  for (int g = 0; g < 4; g++) { f32x4 v; v[0] = t[4 * g]; v[1] = t[4 * g + 1]; v[2] = t[4 * g + 2]; v[3] = t[4 * g + 3]; *(LAS f32x4*)(p + g * 1024 + lane * 16) = v; }
}
DI void tile_add(const LAS char* p, f32x16& t, int lane) {
#pragma unroll
  for (int g = 0; g < 4; g++) { f32x4 v = *(const LAS f32x4*)(p + g * 1024 + lane * 16); t[4 * g] += v[0]; t[4 * g + 1] += v[1]; t[4 * g + 2] += v[2]; t[4 * g + 3] += v[3]; }
}
DI void reduce_scatter(LAS char* buf, f32x16 (&t)[4], int w, int lane, int x0, int x1, LAS unsigned* f, unsigned& epoch) {
  tile_put(buf + (((w ^ x0) * 2 + 0) * 4096), t[2], lane);
  tile_put(buf + (((w ^ x1) * 2 + 1) * 4096), t[3], lane);
  gsync(f, w, ++epoch);
  tile_add(buf + (((w ^ 2 ^ x0) * 2 + 0) * 4096), t[0], lane);
  tile_add(buf + (((w ^ 2 ^ x1) * 2 + 1) * 4096), t[1], lane);
  asm volatile("s_waitcnt lgkmcnt(0)" ::: "memory");
  tile_put(buf + (((w ^ 2 ^ x0) * 2 + 0) * 4096), t[1], lane);
  gsync(f, w, ++epoch);
  tile_add(buf + (((w ^ 3 ^ x0) * 2 + 0) * 4096), t[0], lane);
  asm volatile("s_waitcnt lgkmcnt(0)" ::: "memory");
}

DI unsigned w64_dma_voff(int lane, unsigned ldw) { const int rin = lane >> 3, pc = lane & 7; return rin * ldw + ((pc ^ (((rin >> 1) & 1) << 2)) * 16); }
DI unsigned w64_tr_off(int lane, int nt) {
  const int hh = lane >> 5, blk = (lane >> 4) & 1, q = (lane & 15) >> 2, p = lane & 3;
  return (8 * hh + q) * 128 + ((nt ^ ((q >> 1) & 1)) * 64) + blk * 32 + p * 8;
}

#define ATT_EXP4(SC, B, P, D)                                                                                    \
    SC[B] = __builtin_amdgcn_exp2f(SC[B]); SC[B + 1] = __builtin_amdgcn_exp2f(SC[B + 1]);                       \
    SC[B + 2] = __builtin_amdgcn_exp2f(SC[B + 2]); SC[B + 3] = __builtin_amdgcn_exp2f(SC[B + 3]);               \
    P[D] = pack2(SC[B], SC[B + 1]); P[D + 1] = pack2(SC[B + 2], SC[B + 3]);                                     \
    __builtin_amdgcn_sched_barrier(0);
#define ATT_STEP(SC, SN, PC0, PC1, PP0, PP1, KN0, KN1, QN0, QN1, VP0, VP1, OP)                                  \
  {                                                                                                             \
    SN = mfma(KN0, QN0, zero16());                                                                              \
    __builtin_amdgcn_sched_barrier(0);                                                                          \
    ATT_EXP4(SC, 0, PC0, 0)                                                                                     \
    SN = mfma(KN1, QN1, SN);                                                                                    \
    __builtin_amdgcn_sched_barrier(0);                                                                          \
    ATT_EXP4(SC, 4, PC0, 2)                                                                                     \
    OP = mfma(VP0, PP0, OP);                                                                                    \
    __builtin_amdgcn_sched_barrier(0);                                                                          \
    ATT_EXP4(SC, 8, PC1, 0)                                                                                     \
    OP = mfma(VP1, PP1, OP);                                                                                    \
    __builtin_amdgcn_sched_barrier(0);                                                                          \
    ATT_EXP4(SC, 12, PC1, 2)                                                                                    \
  }

DI float max16(const f32x16& s) {
  float a = fmaxf(fmaxf(s[0], s[1]), s[2]), b = fmaxf(fmaxf(s[3], s[4]), s[5]), c = fmaxf(fmaxf(s[6], s[7]), s[8]);
  float d = fmaxf(fmaxf(s[9], s[10]), s[11]), e = fmaxf(fmaxf(s[12], s[13]), s[14]);
  return fmaxf(fmaxf(fmaxf(a, b), c), fmaxf(fmaxf(d, e), s[15]));
}

template <int KIND>
DI void phase1(LAS char* lds, const char* __restrict__ x, const char* __restrict__ wsrc, const int ldw, const char* __restrict__ gate_w,
               char* __restrict__ ws, const int type, const int sub, const int tg, const int wv, const int stbase_, const char* touch) {
  const unsigned lds0 = (unsigned)(uintptr_t)lds;
  const int tid = threadIdx.x, lane = tid & 63, w = wv & 3, np = wv >> 2;
  const int r = lane & 31, hh = lane >> 5;
  const int tile = tg * 4 + w;
  const int drow = lane >> 4, dlc = (lane & 15) ^ (drow << 2);
  const unsigned dvoff = drow * ldw + (dlc < 14 ? dlc : 0) * 16;
  const uint64_t dmask = ~((3ull << 14) | (3ull << (16 + 10)) | (3ull << (32 + 6)) | (3ull << (48 + 2)));
  unsigned xv[2];
  {
    const int rin = lane >> 3, pc = lane & 7;
#pragma unroll
    for (int e = 0; e < 2; e++) xv[e] = rin * 896 + ((pc ^ ((4 * e + (rin >> 1)) & 7)) * 16);
  }
  const char* xbase = x + (size_t)(tg * 128 + w * 32 + np * 16) * 896;
  const char* wbase = wsrc + (size_t)(8 * wv) * ldw;
  auto issue_half = [&](int st, int jj) {
    const unsigned slot = lds0 + (st & 3) * K1_SLOT;
    dma16_masked(dvoff, wbase + (size_t)(64 * st + 4 * jj) * ldw, slot + (8 * wv + 4 * jj) * 256, dmask);
    dma16(xv[jj], xbase + jj * (8 * 896) + st * 128, slot + 16384 + w * 4096 + (2 * np + jj) * 1024);
  };
  unsigned gw0 = 0;
  if (KIND == 2) {
    const unsigned go = (tid < 448 ? tid : 447) * 2;
    asm volatile("global_load_ushort %0, %1, %2" : "=v"(gw0) : "v"(go), "s"(gate_w) : "memory");
  }
  issue_half(0, 0); issue_half(0, 1); issue_half(1, 0); issue_half(1, 1);
  unsigned xb[4];
#pragma unroll
  for (int ks = 0; ks < 4; ks++) xb[ks] = 16384 + w * 4096 + r * 128 + (((2 * ks + hh) ^ ((r >> 1) & 7)) * 16);
  const unsigned wb0 = k1_tr_off(lane, 2 * np), wb1 = k1_tr_off(lane, 2 * np + 1);
  const bool isaux = ((lane >> 4) & 1) == 1 && (lane & 3) == 3;
  const unsigned ab = isaux ? K1_AUX_OFF + (8 * hh + ((lane & 15) >> 2)) * 8 : wb1;
  const unsigned kstep = isaux ? 128u : 4096u, hoff = isaux ? 32u : 1024u;
  const unsigned sstep = isaux ? 512u : (unsigned)K1_SLOT, wrapst = isaux ? 512u : (unsigned)(-3 * K1_SLOT);
  unsigned a3[4];
#pragma unroll
  for (int ks = 0; ks < 4; ks++) a3[ks] = ab + ks * kstep;
  f32x16 acc[2], cs[2]; acc[0] = zero16(); acc[1] = zero16(); cs[0] = zero16(); cs[1] = zero16();
  u32x4 ones; ones[0] = ones[1] = ones[2] = ones[3] = 0x3F803F80u;
  u32x4 wf[2][4][2], xf[2][4], cf[2][2];
  float sqa[4] = {0.f, 0.f, 0.f, 0.f};
  unsigned sink;
  auto read_stage = [&](int st) {
    const int so = (st & 3) * K1_SLOT;
#pragma unroll
    for (int ks = 0; ks < 4; ks++) {
      xf[st & 1][ks] = *(const LAS u32x4*)(lds + xb[ks] + so);
      wf[st & 1][ks][0] = tr_frag(lds + wb0 + so + ks * 4096, 1024);
      if (KIND == 2) { wf[st & 1][ks][1] = tr_frag2(lds + a3[ks], lds + a3[ks] + hoff); a3[ks] += (st == 3) ? wrapst : sstep; }
      else wf[st & 1][ks][1] = tr_frag(lds + wb1 + so + ks * 4096, 1024);
    }
    if (KIND == 2) { cf[st & 1][0] = tr_frag(lds + wb0 + so + w * 4096, 1024); cf[st & 1][1] = tr_frag(lds + wb1 + so + w * 4096, 1024); }
  };
  asm volatile("s_waitcnt vmcnt(4)" : "+v"(gw0) :: "memory");
  if (KIND == 2) {
    u32x2 a; a[0] = 0x3F80u | (gw0 << 16); a[1] = 0u;
    *(LAS u32x2*)(lds + K1_AUX_OFF + (tid < 448 ? tid : 447) * 8) = a;
  }
  asm volatile("s_waitcnt lgkmcnt(0)\n\ts_barrier" ::: "memory");
  read_stage(0);
  issue_half(2, 0); issue_half(2, 1); issue_half(3, 0); issue_half(3, 1);
#pragma unroll
  for (int st = 0; st < 7; st++) {
    __builtin_amdgcn_sched_barrier(0);
    if (st < 4)       asm volatile("s_waitcnt vmcnt(8) lgkmcnt(0)\n\ts_barrier" ::: "memory");
    else if (st == 4) asm volatile("s_waitcnt vmcnt(5) lgkmcnt(0)\n\ts_barrier" ::: "memory");
    else if (st == 5) asm volatile("s_waitcnt vmcnt(1) lgkmcnt(0)\n\ts_barrier" ::: "memory");
    else              asm volatile("s_waitcnt lgkmcnt(0)\n\ts_barrier" ::: "memory");
    if (st < 6) read_stage(st + 1);
    __builtin_amdgcn_sched_barrier(0);
#pragma unroll
    for (int ks = 0; ks < 4; ks++) {
      acc[0] = KIND == 1 ? mfma(xf[st & 1][ks], wf[st & 1][ks][0], acc[0]) : mfma(wf[st & 1][ks][0], xf[st & 1][ks], acc[0]);
      if (KIND == 2) sq2(np ? xf[st & 1][ks][2] : xf[st & 1][ks][0], sqa[0], sqa[1]);
      acc[1] = KIND == 1 ? mfma(xf[st & 1][ks], wf[st & 1][ks][1], acc[1]) : mfma(wf[st & 1][ks][1], xf[st & 1][ks], acc[1]);
      if (KIND == 2) sq2(np ? xf[st & 1][ks][3] : xf[st & 1][ks][1], sqa[2], sqa[3]);
      if (st + 4 < 7 && (ks == 0 || ks == 2)) issue_half(st + 4, ks >> 1);
      if (st == 3 && ks == 0) asm volatile("global_load_dword %0, %1, off" : "=v"(sink) : "v"(touch) : "memory");
    }
    if (KIND == 2) {
      cs[0] = mfma(ones, cf[st & 1][0], cs[0]); cs[1] = mfma(ones, cf[st & 1][1], cs[1]);
      asm volatile("" : "+v"(sqa[0]), "+v"(sqa[1]), "+v"(sqa[2]), "+v"(sqa[3]));
    }
  }
  __builtin_amdgcn_sched_barrier(0);
  asm volatile("s_waitcnt vmcnt(0)" : "+v"(sink) :: "memory");
  if (KIND < 2) {
    const float sc = type == 0 ? 0.27264314f : 1.0f;
    const __amdgpu_buffer_rsrc_t rq = __builtin_amdgcn_make_buffer_rsrc(ws + WS_Q, 0, 6u << 20, 0x00020000);
#pragma unroll
    for (int j = 0; j < 2; j++) {
      const int head = sub * 4 + 2 * np + j;
      u32x4 f0, f1;
#pragma unroll
      for (int d = 0; d < 4; d++) {
        f0[d] = pack2(acc[j][2 * d] * sc, acc[j][2 * d + 1] * sc);
        f1[d] = pack2(acc[j][8 + 2 * d] * sc, acc[j][8 + 2 * d + 1] * sc);
      }
      if (KIND == 1) {
        const unsigned v = r == 28 ? 0x3F803F80u : 0u;
#pragma unroll
        for (int d = 0; d < 4; d++) { f0[d] = r >= 28 ? v : f0[d]; f1[d] = r >= 28 ? v : f1[d]; }
      } else {
        f1[2] = hh ? (type == 1 ? 0x3F803F80u : 0u) : f1[2];
        f1[3] = hh ? (type == 1 ? 0x00003F80u : 0u) : f1[3];
      }
      const unsigned o = (unsigned)type * (2u << 20) + (unsigned)(head * 64 + tile) * 2048 + lane * 16;
      st16_sc1(f0, rq, o); st16_sc1(f1, rq, o + 1024);
    }
  } else {
    float sq = (sqa[0] + sqa[1]) + (sqa[2] + sqa[3]);
    sq += other_half(sq, hh);
    if (hh == 0) {
      *(LAS float*)(lds + K1_SQ_OFF + ((np * 4 + w) * 32 + r) * 4) = sq;
      *(LAS float*)(lds + K1_CS_OFF + (w * 128 + (2 * np) * 32 + r) * 4) = cs[0][0];
      *(LAS float*)(lds + K1_CS_OFF + (w * 128 + (2 * np + 1) * 32 + r) * 4) = cs[1][0];
    }
    if (sub == 0 && np == 0 && hh == 1) {
      const float gt = __builtin_amdgcn_rcpf(1.0f + __builtin_amdgcn_exp2f(-1.442695041f * acc[1][13]));
      __hip_atomic_store((GAS float*)(ws + WS_GATE) + tile * 32 + r, gt, __ATOMIC_RELAXED, __HIP_MEMORY_SCOPE_AGENT);
    }
    float sx = acc[1][12];
    { float o = other_half(sx, hh); sx = hh ? sx : o; }
    const float mean = sx * (1.0f / 448.0f);
    asm volatile("s_waitcnt lgkmcnt(0)\n\ts_barrier" ::: "memory");
    sq += *(LAS float*)(lds + K1_SQ_OFF + (((np ^ 1) * 4 + w) * 32 + r) * 4);
    const float var = fmaxf(sq * (1.0f / 448.0f) - mean * mean, 0.f) + 1e-5f;
    const float rstd = __builtin_amdgcn_rsqf(var);
    const float ga = -2.302208198f * rstd, gb = -0.1029432397f * rstd * rstd * rstd, irs = var * rstd;
    const __amdgpu_buffer_rsrc_t rh = __builtin_amdgcn_make_buffer_rsrc(ws + WS_H, 0, TBYTES, 0x00020000);
    unsigned pa[2][4][2];
#pragma unroll
    for (int j = 0; j < 2; j++) {
      const int t = 2 * np + j;
#pragma unroll
      for (int g = 0; g < 4; g++) {
        const LAS char* cp = lds + K1_CS_OFF + (t * 32 + 8 * g + 4 * hh) * 4;
        const f32x4 c = (*(const LAS f32x4*)cp + *(const LAS f32x4*)(cp + 512)) + (*(const LAS f32x4*)(cp + 1024) + *(const LAS f32x4*)(cp + 1536));
        pa[j][g][0] = gelu2(acc[j][4 * g], acc[j][4 * g + 1], c[0], c[1], -mean, ga, gb, irs);
        pa[j][g][1] = gelu2(acc[j][4 * g + 2], acc[j][4 * g + 3], c[2], c[3], -mean, ga, gb, irs);
      }
    }
    const unsigned dst = (unsigned)(tile * 56 + 14 * sub + 7 * np) * 512 + r * 16;
    u32x4 v0, v1, v2, v3;
#pragma unroll
    for (int d = 0; d < 2; d++) {
      auto s0 = __builtin_amdgcn_permlane32_swap(pa[0][0][d], pa[0][1][d], false, false);
      v0[d] = s0[0]; v0[2 + d] = s0[1];
      auto s1 = __builtin_amdgcn_permlane32_swap(pa[0][3][d], pa[0][2][d], false, false);
      v1[d] = s1[0]; v1[2 + d] = hh ? pa[0][2][d] : pa[1][0][d];
      auto s2 = __builtin_amdgcn_permlane32_swap(pa[1][0][d], pa[1][2][d], false, false);
      v2[d] = hh ? pa[1][1][d] : s2[1]; v2[2 + d] = hh ? s2[0] : pa[1][1][d];
      auto s3 = __builtin_amdgcn_permlane32_swap(pa[1][2][d], pa[1][2][d], false, false);
      v3[d] = s3[1]; v3[2 + d] = pa[1][3][d];
    }
    st16_sc1(v0, rh, dst + hh * 512);
    st16_sc1(v1, rh, dst + (hh ? 2 : 3) * 512);
    st16_sc1(v2, rh, dst + (4 + hh) * 512);
    st16_sc1(v3, rh, hh ? 0x80000000u : dst + 6 * 512);
  }
}

__global__ __launch_bounds__(512, 2) void fused_kernel(const char* __restrict__ x, const char* __restrict__ wqkv,
                                                       const char* __restrict__ w1, const char* __restrict__ gate_w,
                                                       const char* __restrict__ wo,
                                                       const char* __restrict__ w2, const char* __restrict__ w3,
                                                       const char* __restrict__ w4, char* __restrict__ ws,
                                                       char* __restrict__ out) {
  extern __shared__ __attribute__((aligned(16))) char smem[];
  LAS char* lds = (LAS char*)smem;
  const unsigned lds0 = (unsigned)(uintptr_t)lds;
  LAS unsigned* sy = (LAS unsigned*)(lds + K2_SY_OFF);
  const int tid = threadIdx.x, lane = tid & 63;
  const int wv = __builtin_amdgcn_readfirstlane(tid >> 6);
  const int w = wv & 3;
  const int b = blockIdx.x, xcd = b & 7, bi = b >> 3;
  const int r = lane & 31, hh = lane >> 5;
  const bool has_gemm = bi < 28;
  const int nb = bi % 7, tb64 = xcd * 4 + bi / 7;
  const int ni = w & 1, tj = w >> 1;
  const int p1type = bi < 24 ? bi % 3 : 3, p1tg = (xcd >> 2) * 8 + (bi < 24 ? bi / 3 : bi - 24);
  STAMP_DECL(256 + blockIdx.x) STAMP(0);
  if (tid < 16) sy[tid] = 0u;
  {
    const int type = p1type, sub = xcd & 3, tg = p1tg;
    const int sb_ = 0;
    int k = (b * 8 + wv) * 16 + (lane & 15); k = k < 31360 ? k : 31359;
    const char* touch = k < 3136 ? w2 + (size_t)k * 128 : k < 15680 ? w3 + (size_t)(k - 3136) * 128 : k < 28224 ? w4 + (size_t)(k - 15680) * 128 : wo + (size_t)(k - 28224) * 128;
    if (type < 2)       phase1<0>(lds, x, wqkv + (type * 448 + sub * 112) * 2, 2688, gate_w, ws, type, sub, tg, wv, sb_, touch);
    else if (type == 2) phase1<1>(lds, x, wqkv + (2 * 448 + sub * 112) * 2, 2688, gate_w, ws, type, sub, tg, wv, sb_, touch);
    else                phase1<2>(lds, x, w1 + sub * 224, 896, gate_w, ws, type, sub, tg, wv, sb_, touch);
  }
#define EPOCH_LOAD unsigned epoch; asm volatile("global_load_dword %0, %1, %2 sc1" : "=v"(epoch) : "v"(0u), "s"(ws + WS_EPOCH) : "memory")
#define EPOCH_WAIT(N) asm volatile("s_waitcnt vmcnt(" #N ")" : "+v"(epoch) :: "memory"); epoch = __builtin_amdgcn_readfirstlane(epoch); \
  const unsigned tag = (epoch * 0x9E3779B1u) ^ 0x5bd1e995u
  char* const my_f1 = f1_flag(ws, p1type, xcd & 3, p1tg * 4);
  STAMP(15);

  if (wv < 4) {
    const int head = 2 * xcd + (bi >> 4), qb = bi & 15;
    EPOCH_LOAD; EPOCH_WAIT(0);
    block_publish(sy + 12, my_f1, lane, tag);
    STAMP(13);
    const __amdgpu_buffer_rsrc_t rq = __builtin_amdgcn_make_buffer_rsrc(ws + WS_Q, 0, 6u << 20, 0x00020000);
    const unsigned qo = (unsigned)(head * 64 + qb * 4) * 2048 + lane * 16;
    const unsigned kho = (2u << 20) + (unsigned)(head * 64) * 2048 + lane * 16, vho = kho + (2u << 20);
    const unsigned ko = kho + w * 16 * 2048, vo = vho + w * 16 * 2048;
#define LDQ(off) __builtin_amdgcn_raw_buffer_load_b128(rq, (off), 0, 16)
    unsigned ea = 0;
    {
      const int hs = head >> 2;
      flags_wait(lane < 8 ? f1_flag(ws, 1 + (lane >> 2), hs, w * 16 + (lane & 3) * 4) : f1_flag(ws, 0, hs, qb * 4));
    }
    STAMP(14);
    __builtin_amdgcn_s_setprio(3);
    u32x4 q[4][2];
#pragma unroll
    for (int i = 0; i < 4; i++) { q[i][0] = LDQ(qo + (w ^ i) * 2048); q[i][1] = LDQ(qo + (w ^ i) * 2048 + 1024); }
    u32x4 kb[4][2], vb[4][2];
#pragma unroll
    for (int j = 0; j < 3; j++) { kb[j][0] = LDQ(ko + j * 2048); kb[j][1] = LDQ(ko + j * 2048 + 1024); vb[j][0] = LDQ(vo + j * 2048); vb[j][1] = LDQ(vo + j * 2048 + 1024); }
    f32x16 o[4];
    {
      LAS float* mx = (LAS float*)(lds + K2_M_OFF);
      f32x16 s = mfma(kb[0][0], q[0][0], zero16());
      s = mfma(kb[0][1], q[0][1], s);
      const float m = max16(s);
      if (hh) mx[w * 32 + r] = m;
      gsync(sy, w, ++ea);
#pragma unroll
      for (int i = 0; i < 4; i++) {
        const float mm = mx[(w ^ i) * 32 + r];
        q[i][1][2] = hh ? (pack2(-mm, 0.f)) : q[i][1][2];
      }
    }
    STAMP(1);
    __builtin_amdgcn_s_setprio(0);
    {
#pragma unroll
      for (int i = 0; i < 4; i++) o[i] = zero16();
      f32x16 sA, sB;
      u32x4 pA0, pA1, pB0, pB1;
      sA = mfma(kb[0][0], q[0][0], zero16());
      sA = mfma(kb[0][1], q[0][1], sA);
      pB0 = pB1 = (u32x4){0u, 0u, 0u, 0u};
      vb[3][0] = vb[3][1] = (u32x4){0u, 0u, 0u, 0u};
#pragma unroll 1
      for (int kq = 0; kq < 4; kq++) {
#pragma unroll
        for (int j = 0; j < 4; j++) {
          const int kt = kq * 4 + j;
          const int jp = (j + 3) & 3, jn = (j + 1) & 3;
          ATT_STEP(sA, sB, pA0, pA1, pB0, pB1, kb[j][0], kb[j][1], q[1][0], q[1][1], vb[jp][0], vb[jp][1], o[3]);
          {
            const int kn = kt + 3 < 16 ? kt + 3 : 15;
            __builtin_amdgcn_s_setprio(3);
            kb[jp][0] = LDQ(ko + kn * 2048); kb[jp][1] = LDQ(ko + kn * 2048 + 1024); vb[jp][0] = LDQ(vo + kn * 2048); vb[jp][1] = LDQ(vo + kn * 2048 + 1024);
            __builtin_amdgcn_sched_barrier(0);
            __builtin_amdgcn_s_setprio(0);
          }
          ATT_STEP(sB, sA, pB0, pB1, pA0, pA1, kb[j][0], kb[j][1], q[2][0], q[2][1], vb[j][0], vb[j][1], o[0]);
          ATT_STEP(sA, sB, pA0, pA1, pB0, pB1, kb[j][0], kb[j][1], q[3][0], q[3][1], vb[j][0], vb[j][1], o[1]);
          ATT_STEP(sB, sA, pB0, pB1, pA0, pA1, kb[jn][0], kb[jn][1], q[0][0], q[0][1], vb[j][0], vb[j][1], o[2]);
        }
      }
      o[3] = mfma(vb[3][0], pB0, o[3]);
      o[3] = mfma(vb[3][1], pB1, o[3]);
    }
    STAMP(2);
    __builtin_amdgcn_s_setprio(3);
    flags_wait(f1_flag(ws, 3, 0, qb * 4));
    const float gate_q = __hip_atomic_load((const GAS float*)(ws + WS_GATE) + (qb * 4 + w) * 32 + r, __ATOMIC_RELAXED, __HIP_MEMORY_SCOPE_AGENT);
    reduce_scatter(lds + K2_A_OFF, o, w, lane, 0, 0, sy, ea);
    float l = o[0][12];
    { float t = other_half(l, hh); l = hh ? l : t; }
    {
      float amax = 0.f;
#pragma unroll
      for (int i = 0; i < 12; i++) amax = fmaxf(amax, fabsf(o[0][i]));
      if (!hh) amax = fmaxf(fmaxf(amax, fabsf(o[0][12])), fmaxf(fabsf(o[0][13]), fmaxf(fabsf(o[0][14]), fabsf(o[0][15]))));
      const bool ok = (l > 1.0e-10f) && (l < 1.0e18f) && (amax < 1.0e37f);
      if (__any(!ok)) {
        flags_wait(f1_flag(ws, 1 + (lane >> 5), head >> 2, (lane & 15) * 4));
        u32x4 q0 = q[0][0], q1 = q[0][1];
        q1[2] = hh ? 0u : q1[2];
        float tm = -3.0e38f;
#pragma unroll 1
        for (int kt = 0; kt < 64; kt++) {
          f32x16 s = mfma(LDQ(kho + kt * 2048), q0, zero16());
          s = mfma(LDQ(kho + kt * 2048 + 1024), q1, s);
          tm = fmaxf(tm, max16(s));
        }
        tm = -fmaxf(tm, other_half(tm, hh));
        const unsigned h0 = pack2(tm, 0.f) & 0xffffu; const float f0 = __builtin_bit_cast(float, h0 << 16);
        const unsigned h1 = pack2(tm - f0, 0.f) & 0xffffu; const float f1 = __builtin_bit_cast(float, h1 << 16);
        const unsigned h2 = pack2((tm - f0) - f1, 0.f) & 0xffffu;
        q1[2] = hh ? (h0 | (h1 << 16)) : q1[2];
        q1[3] = hh ? h2 : q1[3];
        f32x16 oo = zero16();
#pragma unroll 1
        for (int kt = 0; kt < 64; kt++) {
          f32x16 s = mfma(LDQ(kho + kt * 2048), q0, zero16());
          s = mfma(LDQ(kho + kt * 2048 + 1024), q1, s);
          u32x4 p0, p1;
#pragma unroll
          for (int d = 0; d < 4; d++) {
            p0[d] = pack2(__builtin_amdgcn_exp2f(s[2 * d]), __builtin_amdgcn_exp2f(s[2 * d + 1]));
            p1[d] = pack2(__builtin_amdgcn_exp2f(s[8 + 2 * d]), __builtin_amdgcn_exp2f(s[8 + 2 * d + 1]));
          }
          oo = mfma(LDQ(vho + kt * 2048), p0, oo);
          oo = mfma(LDQ(vho + kt * 2048 + 1024), p1, oo);
        }
        o[0] = oo;
        l = oo[12];
        { float t = other_half(l, hh); l = hh ? l : t; }
      }
    }
    {
      const __amdgpu_buffer_rsrc_t ro = __builtin_amdgcn_make_buffer_rsrc(ws + WS_O, 0, TBYTES, 0x00020000);
      const float inv = gate_q * __builtin_amdgcn_rcpf(l);
      const unsigned base = (unsigned)(qb * 4 + w) * (56 * 512) + r * 16;
#pragma unroll
      for (int g = 0; g < 4; g++) {
        u32x2 v; v[0] = pack2(o[0][4 * g] * inv, o[0][4 * g + 1] * inv); v[1] = pack2(o[0][4 * g + 2] * inv, o[0][4 * g + 3] * inv);
        const int u = 7 * head + 2 * g + hh;
        if (g < 3 || hh == 0) st8_sc1(v, ro, base + (u >> 1) * 512 + (u & 1) * 8);
      }
    }
    asm volatile("s_waitcnt vmcnt(0)" ::: "memory");
    flag_set(ws + WS_FO + ((qb * 4 + w) * 16 + head) * 4, lane);
    STAMP(3);
    return;
  }

  if (!has_gemm) {
    EPOCH_LOAD; EPOCH_WAIT(0);
    block_publish(sy + 12, my_f1, lane, tag);
    if (b == 224 && w == 3) {
#pragma unroll 1
      for (int i = 0; i < 4; i++) flags_wait(ws + WS_F1 + (i * 64 + lane) * 128);
      if (lane == 0) __hip_atomic_store((GAS unsigned*)(ws + WS_EPOCH), epoch + 1u, __ATOMIC_RELAXED, __HIP_MEMORY_SCOPE_AGENT);
    }
    const int id = (xcd * 4 + bi - 28) * 2 + w;
    if (w < 2 && id < 56) {
      const unsigned reg = K2_B_OFF + w * 28672;
      const unsigned dv = (lane >> 2) * 3584 + (lane & 3) * 16;
      const char* src = w3 + id * 64;
#pragma unroll
      for (int j = 0; j < 28; j++) dma16(dv, src + (size_t)j * (16 * 3584), lds0 + reg + j * 1024);
      asm volatile("s_waitcnt vmcnt(0)" ::: "memory");
      const unsigned to = reg + (8 * hh + ((lane & 15) >> 2)) * 64 + ((lane >> 4) & 1) * 32 + (lane & 3) * 8;
      u32x4 ones; ones[0] = ones[1] = ones[2] = ones[3] = 0x3F803F80u;
      f32x16 cs = zero16();
#pragma unroll
      for (int s = 0; s < 28; s++) cs = mfma(ones, tr_frag(lds + to + s * 1024, 256), cs);
      if (hh == 0) __hip_atomic_store((GAS float*)(ws + WS_CS3) + id * 32 + r, cs[0], __ATOMIC_RELAXED, __HIP_MEMORY_SCOPE_AGENT);
      asm volatile("s_waitcnt vmcnt(0)" ::: "memory");
      flag_set(ws + WS_FCS + id * 4, lane);
    }
    return;
  }
  PRIO_LO;
  unsigned eb = 0;
  LAS unsigned* syb = sy + 4;
  const int nb3 = bi % 14, tb128 = xcd * 2 + bi / 14;
  const unsigned dv64 = w64_dma_voff(lane, 896);
  const unsigned t0 = w64_tr_off(lane, ni), t1 = w64_tr_off(lane, ni ^ 1);
  const unsigned dv3 = (lane >> 4) * 3584 + (((lane & 15) ^ ((lane >> 4) << 2)) * 16);
  const char* w3src = w3 + nb3 * 256;
  const unsigned ring = K2_B_OFF + w * 28672;
  const char* w4src = w4 + (size_t)(448 * w) * 896 + nb * 128;
  unsigned x1p[8];
  const unsigned sl = K2_B_OFF + 57344 + w * 14336;
  EPOCH_LOAD;
  EPOCH_WAIT(0);
  block_publish(sy + 12, my_f1, lane, tag);
  STAMP(14);
  {
    const char* wsrc = w2 + (size_t)(112 * w) * 896 + nb * 128;
#pragma unroll
    for (int j = 0; j < 14; j++) dma16(dv64, wsrc + j * (8 * 896), lds0 + sl + j * 1024);
  }
#pragma unroll
  for (int j = 0; j < 14; j++) { const int row0 = 56 * w + 4 * j; dma16(dv3, w3src + (size_t)row0 * 3584, lds0 + K2_B_OFF + row0 * 256); }
  {
    const u32x4 rh = mkrsrc(ws + WS_H, TBYTES);
    const int delta = w == 0 ? -1 : w == 1 ? 1 : w == 2 ? -2 : 2;
    u32x4 af[7][2];
    unsigned av[2];
#pragma unroll
    for (int e = 0; e < 2; e++) {
      const int tok = (tb64 * 2 + (tj ^ e)) * 32 + r + delta;
      av[e] = (tok >= 0 && tok < SEQ) ? (unsigned)(((tok >> 5) * 56 + 14 * w + hh) * 512 + (tok & 31) * 16) : 0x80000000u;
    }
    u32x2 xr[4];
    {
      const char* xp = x + (size_t)((tb64 * 2 + tj) * 32) * 896 + (nb * 64 + ni * 32) * 2;
      const unsigned xo = r * 896 + hh * 8;
#pragma unroll
      for (int g = 0; g < 4; g++) asm volatile("global_load_dwordx2 %0, %1, %2 offset:%3" : "=v"(xr[g]) : "v"(xo), "s"(xp), "i"(g * 16) : "memory");
    }
    {
      int tl = tb64 * 2 - 1 + (lane & 3); tl = tl < 0 ? 0 : tl > 63 ? 63 : tl;
      flags_wait(f1_flag(ws, 3, w, tl));
    }
    asm volatile("s_waitcnt vmcnt(0)" : "+v"(xr[0]), "+v"(xr[1]), "+v"(xr[2]), "+v"(xr[3]) :: "memory");
    STAMP(10);
#pragma unroll
    for (int s = 0; s < 7; s++) {
      BLOAD_SC1(af[s][0], av[0], rh, (s >> 2) * 4096, (s & 3) * 1024);
      BLOAD_SC1(af[s][1], av[1], rh, (s >> 2) * 4096, (s & 3) * 1024);
    }
    PRIO_HI;
    f32x16 t[4];
#pragma unroll
    for (int i = 0; i < 4; i++) t[i] = zero16();
    u32x4 f0[7], f1[7];
#pragma unroll
    for (int s = 0; s < 7; s++) { f0[s] = tr_frag(lds + sl + t0 + s * 2048, 512); f1[s] = tr_frag(lds + sl + t1 + s * 2048, 512); }
    STAMP(1);
#pragma unroll
    for (int s = 0; s < 7; s++) {
      asm volatile("s_waitcnt vmcnt(%2)" : "+v"(af[s][0]), "+v"(af[s][1]) : "i"(12 - 2 * s) : "memory");
      t[0] = mfma(f0[s], af[s][0], t[0]); t[1] = mfma(f1[s], af[s][0], t[1]);
      t[2] = mfma(f0[s], af[s][1], t[2]); t[3] = mfma(f1[s], af[s][1], t[3]);
      __builtin_amdgcn_sched_barrier(0);
    }
    STAMP(11);
    asm volatile("s_waitcnt lgkmcnt(0)" ::: "memory");
#pragma unroll
    for (int i = 1; i < 4; i++) tile_put(lds + sl + (i - 1) * 4096, t[i], lane);
    gsync(syb, w, ++eb);
#pragma unroll
    for (int i = 1; i < 4; i++) tile_add(lds + K2_B_OFF + 57344 + (w ^ i) * 14336 + (i - 1) * 4096, t[0], lane);
    float s1 = 0.f, s2 = 0.f;
#pragma unroll
    for (int g = 0; g < 4; g++) {
      const float a0 = __builtin_bit_cast(float, xr[g][0] << 16) + t[0][4 * g], a1 = __builtin_bit_cast(float, xr[g][0] & 0xffff0000u) + t[0][4 * g + 1];
      const float a2 = __builtin_bit_cast(float, xr[g][1] << 16) + t[0][4 * g + 2], a3 = __builtin_bit_cast(float, xr[g][1] & 0xffff0000u) + t[0][4 * g + 3];
      x1p[2 * g] = pack2(a0, a1); x1p[2 * g + 1] = pack2(a2, a3);
    }
    const __amdgpu_buffer_rsrc_t rx = __builtin_amdgcn_make_buffer_rsrc(ws + WS_X1, 0, TBYTES, 0x00020000);
    const int tile = tb64 * 2 + tj;
#pragma unroll
    for (int gp = 0; gp < 2; gp++) {
      auto a = __builtin_amdgcn_permlane32_swap(x1p[4 * gp], x1p[4 * gp + 2], false, false);
      auto c = __builtin_amdgcn_permlane32_swap(x1p[4 * gp + 1], x1p[4 * gp + 3], false, false);
      u32x4 v; v[0] = a[0]; v[1] = c[0]; v[2] = a[1]; v[3] = c[1];
      st16_sc1(v, rx, (unsigned)(tile * 56 + nb * 8 + ni * 4 + 2 * gp + hh) * 512 + r * 16);
    }
#pragma unroll
    for (int j = 0; j < 8; j++) {
      const float lo = __builtin_bit_cast(float, x1p[j] << 16), hi = __builtin_bit_cast(float, x1p[j] & 0xffff0000u);
      s1 += lo + hi; s2 = __builtin_fmaf(lo, lo, s2); s2 = __builtin_fmaf(hi, hi, s2);
    }
    s1 += other_half(s1, hh); s2 += other_half(s2, hh);
    const __amdgpu_buffer_rsrc_t rs = __builtin_amdgcn_make_buffer_rsrc(ws + WS_STATS, 0, 64 * 14 * 32 * 8, 0x00020000);
    { u32x2 v; v[0] = __builtin_bit_cast(unsigned, s1); v[1] = __builtin_bit_cast(unsigned, s2); st8_sc1(v, rs, hh ? 0x80000000u : (unsigned)((tile * 14 + nb * 2 + ni) * 32 + r) * 8); }
    STAMP(12);
    gsync(syb, w, ++eb);
#pragma unroll
    for (int j = 0; j < 14; j++) { const int row0 = 224 + 56 * w + 4 * j; dma16(dv3, w3src + (size_t)row0 * 3584, lds0 + K2_B_OFF + row0 * 256); }
    asm volatile("s_waitcnt vmcnt(14)" ::: "memory");
    flag_set(ws + WS_FX1 + (tile * 16 + nb * 2 + ni) * 4, lane);
    STAMP(2);
  }
  {
    asm volatile("s_waitcnt vmcnt(0)" ::: "memory");
    gsync(syb, w, ++eb);
    unsigned tb[4];
    {
      const int blk = (lane >> 4) & 1, q = (lane & 15) >> 2, p = lane & 3;
#pragma unroll
      for (int n = 0; n < 4; n++) tb[n] = K2_B_OFF + (8 * hh + q) * 256 + ((n ^ q) * 64) + blk * 32 + p * 8;
    }
    STAMP(3);
    const int tile = tb128 * 4 + w;
    flags_wait(lane < 16 ? ws + WS_FX1 + (tile * 16 + (lane < 14 ? lane : 0)) * 4 : lane < 32 ? ws + WS_FCS + (nb3 * 4 + (lane & 3)) * 4
               : f1_flag(ws, 3, 0, lane < 48 ? tile : tb64 * 2 + tj));
    STAMP(4);
    const u32x4 rx = mkrsrc(ws + WS_X1, TBYTES);
    const u32x4 rsd = mkrsrc(ws + WS_STATS, 64 * 14 * 32 * 8);
    const u32x4 rcd = mkrsrc(ws + WS_CS3, 1792 * 4);
    u32x2 sv[7], cv; float g3;
    {
      const unsigned so = (unsigned)((tile * 14 + hh * 7) * 32 + r) * 8, co = (unsigned)(nb3 * 128 + lane * 2) * 4;
#pragma unroll
      for (int j = 0; j < 7; j++) asm volatile("buffer_load_dwordx2 %0, %1, %2, 0 offen offset:%3 sc1" : "=v"(sv[j]) : "v"(so), "s"(rsd), "i"(j * 256) : "memory");
      asm volatile("buffer_load_dwordx2 %0, %1, %2, 0 offen sc1" : "=v"(cv) : "v"(co), "s"(rcd) : "memory");
      const unsigned go = (unsigned)(tile * 32 + r) * 4;
      asm volatile("global_load_dword %0, %1, %2 sc1" : "=v"(g3) : "v"(go), "s"(ws + WS_GATE) : "memory");
    }
    constexpr int AW = 16;
    u32x4 af[AW];
    const unsigned av = (unsigned)(tile * 56) * 512 + lane * 16;
#pragma unroll
    for (int s = 0; s < AW; s++) BLOAD_SC1(af[s], av, rx, (s >> 2) * 4096, (s & 3) * 1024);
    const __amdgpu_buffer_rsrc_t r3 = __builtin_amdgcn_make_buffer_rsrc(ws + WS_H3, 0, T3BYTES, 0x00020000);
    STAMP(13);
    PRIO_G3;
    u32x4 wfr[3][4];
#pragma unroll
    for (int k = 0; k < 2; k++)
#pragma unroll
      for (int n = 0; n < 4; n++) wfr[k][n] = tr_frag(lds + tb[n] + k * 4096, 1024);
    f32x16 acc[4];
#pragma unroll
    for (int n = 0; n < 4; n++) acc[n] = zero16();
#pragma unroll
    for (int s = 0; s < 28; s++) {
      asm volatile("s_waitcnt vmcnt(%1)" : "+v"(af[s % AW]) : "i"(s + AW <= 28 ? AW - 1 : 27 - s) : "memory");
#pragma unroll
      for (int n = 0; n < 4; n++) {
        acc[n] = mfma(wfr[s % 3][n], af[s % AW], acc[n]);
        if (s + 2 < 28) wfr[(s + 2) % 3][n] = tr_frag(lds + tb[n] + (s + 2) * 4096, 1024);
        __builtin_amdgcn_sched_barrier(0);
      }
      if (s + AW < 28) BLOAD_SC1(af[s % AW], av, rx, ((s + AW) >> 2) * 4096, ((s + AW) & 3) * 1024);
      __builtin_amdgcn_sched_barrier(0);
    }
    STAMP(8);
    PRIO_HI;
    asm volatile("" : "+v"(sv[0]), "+v"(sv[1]), "+v"(sv[2]), "+v"(sv[3]), "+v"(sv[4]), "+v"(sv[5]), "+v"(sv[6]), "+v"(cv), "+v"(g3));
    const unsigned csl = K2_CSP_OFF + w * 512;
    *(LAS u32x2*)(lds + csl + lane * 8) = cv;
    float nmean, ga, gb, irs;
    {
      float s1 = 0.f, s2 = 0.f;
#pragma unroll
      for (int j = 0; j < 7; j++) { const unsigned u0 = sv[j][0], u1 = sv[j][1]; s1 += __builtin_bit_cast(float, u0); s2 += __builtin_bit_cast(float, u1); }
      s1 += other_half(s1, hh); s2 += other_half(s2, hh);
      const float mean = s1 * (1.0f / 448.0f);
      const float var = fmaxf(s2 * (1.0f / 448.0f) - mean * mean, 0.f) + 1e-5f;
      const float rstd = __builtin_amdgcn_rsqf(var);
      nmean = -mean; ga = -2.302208198f * rstd; gb = -0.1029432397f * rstd * rstd * rstd;
      irs = var * rstd * __builtin_amdgcn_rcpf(fmaxf(1.0f - g3, 1.0e-30f));
    }
#pragma unroll
    for (int n = 0; n < 4; n++) {
#pragma unroll
      for (int gp = 0; gp < 2; gp++) {
        unsigned pk[4];
#pragma unroll
        for (int g2 = 0; g2 < 2; g2++) {
          const int g = 2 * gp + g2;
          const f32x4 c4 = *(const LAS f32x4*)(lds + csl + (n * 32 + 8 * g + 4 * hh) * 4);
          pk[2 * g2] = gelu2(acc[n][4 * g], acc[n][4 * g + 1], c4[0], c4[1], nmean, ga, gb, irs);
          pk[2 * g2 + 1] = gelu2(acc[n][4 * g + 2], acc[n][4 * g + 3], c4[2], c4[3], nmean, ga, gb, irs);
        }
        auto a = __builtin_amdgcn_permlane32_swap(pk[0], pk[2], false, false);
        auto c = __builtin_amdgcn_permlane32_swap(pk[1], pk[3], false, false);
        u32x4 v; v[0] = a[0]; v[1] = c[0]; v[2] = a[1]; v[3] = c[1];
        st16_sc1(v, r3, (unsigned)(tile * 224 + nb3 * 16 + n * 4 + 2 * gp + hh) * 512 + r * 16);
      }
    }
    PRIO_HI;
    STAMP(5);
    gsync(syb, w, ++eb);
    asm volatile("s_waitcnt vmcnt(0)" ::: "memory");
    flag_set(ws + WS_FH3 + (tile * 16 + nb3) * 4, lane);
    PRIO_LO;
  }
  {
    const char* wsrc = w4src;
#pragma unroll
    for (int j = 0; j < 28; j++) dma16(dv64, wsrc + j * (8 * 896), lds0 + ring + j * 1024);
    const float gate = __hip_atomic_load((const GAS float*)(ws + WS_GATE) + (tb64 * 2 + tj) * 32 + r, __ATOMIC_RELAXED, __HIP_MEMORY_SCOPE_AGENT);
    {
      const int blk0 = (7 * w) >> 1;
      const int k = lane & 3;
      flags_wait(ws + WS_FH3 + ((tb64 * 2 + ((lane >> 2) & 1)) * 16 + blk0 + k) * 4);
    }
    asm volatile("s_waitcnt vmcnt(0)" ::: "memory");
    STAMP(6);
    const u32x4 r3 = mkrsrc(ws + WS_H3, T3BYTES);
    unsigned av[2];
#pragma unroll
    for (int e = 0; e < 2; e++) av[e] = (unsigned)((tb64 * 2 + (tj ^ e)) * 224 + 56 * w) * 512 + lane * 16;
    u32x4 af[14][2];
#pragma unroll
    for (int s = 0; s < 14; s++)
#pragma unroll
      for (int e = 0; e < 2; e++) BLOAD_SC1(af[s][e], av[e], r3, (s >> 2) * 4096, (s & 3) * 1024);
    f32x16 t[4];
#pragma unroll
    for (int i = 0; i < 4; i++) t[i] = zero16();
    u32x4 f0[2], f1[2];
    const char* wosrc = wo + (size_t)(112 * w) * 896 + nb * 128;
    f0[0] = tr_frag(lds + ring + t0, 512); f1[0] = tr_frag(lds + ring + t1, 512);
#pragma unroll
    for (int s = 0; s < 28; s++) {
      const int sl = s % 14;
      asm volatile("s_waitcnt lgkmcnt(0)" : "+v"(f0[s & 1]), "+v"(f1[s & 1]) :: "memory");
      if (s + 1 < 28) {
        const int nl = (s + 1) % 14;
        if (s == 13) asm volatile("s_waitcnt vmcnt(50)" ::: "memory");
        else if (s > 13) asm volatile("s_waitcnt vmcnt(%0)" :: "i"(2 + 4 * (13 - nl) + 2 * (s - 14 < 7 ? s - 14 : 7)) : "memory");
        f0[(s + 1) & 1] = tr_frag(lds + ring + t0 + nl * 2048, 512); f1[(s + 1) & 1] = tr_frag(lds + ring + t1 + nl * 2048, 512);
      }
      if (s < 14) {
        dma16(dv64, wsrc + (28 + 2 * s) * (8 * 896), lds0 + ring + (2 * s) * 1024);
        dma16(dv64, wsrc + (29 + 2 * s) * (8 * 896), lds0 + ring + (2 * s + 1) * 1024);
        asm volatile("s_waitcnt vmcnt(%2)" : "+v"(af[s][0]), "+v"(af[s][1]) : "i"(2 * (13 - s) + 4 * s + 2) : "memory");
      } else {
        if (s < 21) {
          dma16(dv64, wosrc + (2 * sl) * (8 * 896), lds0 + ring + (2 * sl) * 1024);
          dma16(dv64, wosrc + (2 * sl + 1) * (8 * 896), lds0 + ring + (2 * sl + 1) * 1024);
        }
        asm volatile("s_waitcnt vmcnt(%2)" : "+v"(af[sl][0]), "+v"(af[sl][1]) : "i"(4 * (13 - sl) + 2 * (s - 13 < 7 ? s - 13 : 7)) : "memory");
      }
      t[0] = mfma(f0[s & 1], af[sl][0], t[0]); t[1] = mfma(f1[s & 1], af[sl][0], t[1]);
      t[2] = mfma(f0[s & 1], af[sl][1], t[2]); t[3] = mfma(f1[s & 1], af[sl][1], t[3]);
      if (s < 14) {
        BLOAD_SC1(af[s][0], av[0], r3, ((s + 14) >> 2) * 4096, ((s + 14) & 3) * 1024);
        BLOAD_SC1(af[s][1], av[1], r3, ((s + 14) >> 2) * 4096, ((s + 14) & 3) * 1024);
      }
      __builtin_amdgcn_sched_barrier(0);
    }
    STAMP(7);
    PRIO_HI;
    flags_wait(ws + WS_FO + ((tb64 * 2 + ((lane >> 2) & 1)) * 16 + 4 * w + (lane & 3)) * 4);
    asm volatile("s_waitcnt vmcnt(0)" ::: "memory");
    {
      const u32x4 ro = mkrsrc(ws + WS_O, TBYTES);
      unsigned ov[2];
#pragma unroll
      for (int e = 0; e < 2; e++) ov[e] = (unsigned)((tb64 * 2 + (tj ^ e)) * 56 + 14 * w) * 512 + lane * 16;
#pragma unroll
      for (int s = 0; s < 7; s++)
#pragma unroll
        for (int e = 0; e < 2; e++) BLOAD_SC1(af[s][e], ov[e], ro, (s >> 2) * 4096, (s & 3) * 1024);
      u32x4 g0[7], g1[7];
#pragma unroll
      for (int s = 0; s < 7; s++) { g0[s] = tr_frag(lds + ring + t0 + s * 2048, 512); g1[s] = tr_frag(lds + ring + t1 + s * 2048, 512); }
#pragma unroll
      for (int s = 0; s < 7; s++) {
        asm volatile("s_waitcnt vmcnt(%2)" : "+v"(af[s][0]), "+v"(af[s][1]) : "i"(2 * (6 - s)) : "memory");
        t[0] = mfma(g0[s], af[s][0], t[0]); t[1] = mfma(g1[s], af[s][0], t[1]);
        t[2] = mfma(g0[s], af[s][1], t[2]); t[3] = mfma(g1[s], af[s][1], t[3]);
      }
    }
    asm volatile("s_waitcnt lgkmcnt(0)" ::: "memory");
#pragma unroll
    for (int i = 1; i < 4; i++) tile_put(lds + ring + (i - 1) * 4096, t[i], lane);
    gsync(syb, w, ++eb);
#pragma unroll
    for (int i = 1; i < 4; i++) tile_add(lds + K2_B_OFF + (w ^ i) * 28672 + (i - 1) * 4096, t[0], lane);
    unsigned pk[8];
    const float og = 1.0f - gate;
#pragma unroll
    for (int j = 0; j < 8; j++)
      pk[j] = pack2(__builtin_fmaf(og, __builtin_bit_cast(float, x1p[j] << 16), t[0][2 * j]), __builtin_fmaf(og, __builtin_bit_cast(float, x1p[j] & 0xffff0000u), t[0][2 * j + 1]));
    char* op = out + (size_t)((tb64 * 2 + tj) * 32 + r) * 896 + (nb * 64 + ni * 32) * 2 + hh * 16;
#pragma unroll
    for (int gp = 0; gp < 2; gp++) {
      auto a2 = __builtin_amdgcn_permlane32_swap(pk[4 * gp], pk[4 * gp + 2], false, false);
      auto c2 = __builtin_amdgcn_permlane32_swap(pk[4 * gp + 1], pk[4 * gp + 3], false, false);
      u32x4 v; v[0] = a2[0]; v[1] = c2[0]; v[2] = a2[1]; v[3] = c2[1];
      *(u32x4*)(op + gp * 32) = v;
    }
    STAMP(9);
  }
}

extern "C" void kernel_launch(void* const* d_in, const int* in_sizes, int n_in,
                              void* d_out, int out_size, void* d_ws, size_t ws_size,
                              hipStream_t stream) {
  const char* x = (const char*)d_in[0]; const char* wqkv = (const char*)d_in[1]; const char* wo = (const char*)d_in[2];
  const char* w1 = (const char*)d_in[4]; const char* w2 = (const char*)d_in[5]; const char* w3 = (const char*)d_in[6];
  const char* w4 = (const char*)d_in[7]; const char* gw = (const char*)d_in[8];
  (void)hipFuncSetAttribute((const void*)fused_kernel, hipFuncAttributeMaxDynamicSharedMemorySize, K2_LDS);
  fused_kernel<<<256, 512, K2_LDS, stream>>>(x, wqkv, w1, gw, wo, w2, w3, w4, (char*)d_ws, (char*)d_out);
}
```

```cpp
#include <hip/hip_runtime.h>
#include <stdint.h>

#define TOUCH_ST 2
#define TOUCH 3
#define PL 2
#define LDPRIO

typedef __attribute__((ext_vector_type(8))) __bf16 bf16x8;
typedef __attribute__((ext_vector_type(2))) __bf16 bf16x2;
typedef __attribute__((ext_vector_type(4))) short s16x4;
typedef __attribute__((ext_vector_type(16))) float f32x16;
typedef __attribute__((ext_vector_type(4))) float f32x4;
typedef __attribute__((ext_vector_type(2))) float f32x2;
typedef __attribute__((ext_vector_type(4))) unsigned u32x4;
typedef __attribute__((ext_vector_type(2))) unsigned u32x2;
#define LAS __attribute__((address_space(3)))
#define GAS __attribute__((address_space(1)))
#define DI __device__ __forceinline__

constexpr int SEQ = 2048, DM = 448, NH = 16, DH = 28, FF = 1792;

constexpr size_t WS_CNT   = 0;
constexpr size_t WS_GATE  = 16384;
constexpr size_t WS_STATS = 32768;
constexpr size_t WS_Q     = 1u << 20;
constexpr size_t WS_K     = WS_Q + (2u << 20);
constexpr size_t WS_V     = WS_K + (2u << 20);
constexpr size_t WS_H     = WS_V + (2u << 20);
constexpr size_t WS_X1    = WS_H + (2u << 20);
constexpr size_t WS_O     = WS_X1 + (2u << 20);
constexpr size_t WS_H3    = WS_O + (2u << 20);
constexpr unsigned TBYTES = 64u * 56u * 512u;
constexpr unsigned T3BYTES = 64u * 224u * 512u;

DI f32x16 mfma(u32x4 a, u32x4 b, f32x16 c) {
  return __builtin_amdgcn_mfma_f32_32x32x16_bf16(__builtin_bit_cast(bf16x8, a), __builtin_bit_cast(bf16x8, b), c, 0, 0, 0);
}
DI unsigned pack2(float a, float b) {
  f32x2 v; v[0] = a; v[1] = b;
  return __builtin_bit_cast(unsigned, __builtin_convertvector(v, bf16x2));
}
DI u32x4 tr_frag(const LAS char* p, int hi_off) {
  s16x4 lo = __builtin_amdgcn_ds_read_tr16_b64_v4i16((LAS s16x4*)p);
  s16x4 hi = __builtin_amdgcn_ds_read_tr16_b64_v4i16((LAS s16x4*)(p + hi_off));
  u32x2 a = __builtin_bit_cast(u32x2, lo), b = __builtin_bit_cast(u32x2, hi);
  u32x4 r; r[0] = a[0]; r[1] = a[1]; r[2] = b[0]; r[3] = b[1];
  return r;
}
DI float gelu_f(float x) {
  const float C0 = -2.302208198f;
  const float C1 = -0.1029432397f;
  float z = x * __builtin_fmaf(x * x, C1, C0);
  float e = __builtin_amdgcn_exp2f(z);
  return x * __builtin_amdgcn_rcpf(1.0f + e);
}
DI unsigned gelu2(float a0, float a1, float c0, float c1, float nmean, float ga, float gb, float irs) {
  const f32x2 a = {a0, a1}, c = {c0, c1}, nm = {nmean, nmean}, ga2 = {ga, ga}, gb2 = {gb, gb}, ir = {irs, irs};
  const f32x2 t = __builtin_elementwise_fma(nm, c, a);
  const f32x2 z = t * __builtin_elementwise_fma(t * t, gb2, ga2);
  f32x2 e; e[0] = __builtin_amdgcn_exp2f(z[0]); e[1] = __builtin_amdgcn_exp2f(z[1]);
  const f32x2 d = __builtin_elementwise_fma(e, ir, ir);
  f32x2 rr; rr[0] = __builtin_amdgcn_rcpf(d[0]); rr[1] = __builtin_amdgcn_rcpf(d[1]);
  const f32x2 y = t * rr;
  return __builtin_bit_cast(unsigned, __builtin_convertvector(y, bf16x2));
}
DI float other_half(float v, int hh) {
  unsigned u = __builtin_bit_cast(unsigned, v);
  auto r = __builtin_amdgcn_permlane32_swap(u, u, false, false);
  return __builtin_bit_cast(float, hh ? r[0] : r[1]);
}
DI void sq2(unsigned v, float& a, float& b) {
  const float lo = __builtin_bit_cast(float, v << 16), hi = __builtin_bit_cast(float, v & 0xffff0000u);
  a = __builtin_fmaf(lo, lo, a); b = __builtin_fmaf(hi, hi, b);
}
#define STAMP(i)
#define STAMP_DECL(base)
#define STAMP_FLUSH(base, n)
DI f32x16 zero16() { f32x16 z; for (int i = 0; i < 16; i++) z[i] = 0.f; return z; }

DI void dma16_masked(unsigned voff, const void* sbase, unsigned ldsdst, uint64_t mask) {
  unsigned keep;
  asm volatile("s_mov_b32 %0, m0\n\ts_mov_b32 m0, %3\n\ts_mov_b64 exec, %4\n\tglobal_load_lds_dwordx4 %1, %2\n\ts_mov_b64 exec, -1\n\ts_mov_b32 m0, %0"
               : "=&s"(keep) : "v"(voff), "s"(sbase), "s"(ldsdst), "s"(mask) : "memory");
}
DI void dma16(unsigned voff, const void* sbase, unsigned ldsdst) {
  unsigned keep;
  asm volatile("s_mov_b32 %0, m0\n\ts_mov_b32 m0, %3\n\ts_nop 0\n\tglobal_load_lds_dwordx4 %1, %2\n\ts_mov_b32 m0, %0"
               : "=&s"(keep) : "v"(voff), "s"(sbase), "s"(ldsdst) : "memory");
}

constexpr int K1_SLOT    = 32768;
constexpr int K1_AUX_OFF = 151040;
constexpr int K1_CS_OFF  = K1_AUX_OFF + 3584;
constexpr int K1_SQ_OFF  = K1_CS_OFF + 2048;
constexpr int K1_LDS     = K1_SQ_OFF + 1024;

DI u32x4 tr_frag2(const LAS char* p0, const LAS char* p1) {
  s16x4 lo = __builtin_amdgcn_ds_read_tr16_b64_v4i16((LAS s16x4*)p0);
  s16x4 hi = __builtin_amdgcn_ds_read_tr16_b64_v4i16((LAS s16x4*)p1);
  u32x2 a = __builtin_bit_cast(u32x2, lo), b = __builtin_bit_cast(u32x2, hi);
  u32x4 r; r[0] = a[0]; r[1] = a[1]; r[2] = b[0]; r[3] = b[1];
  return r;
}
DI unsigned k1_tr_off(int lane, int t) {
  const int hh = lane >> 5, blk = (lane >> 4) & 1, q = (lane & 15) >> 2, p = lane & 3;
  int u = 7 * t + 4 * blk + p;
  if (blk == 1 && p == 3) u = 7 * t;
  return (8 * hh + q) * 256 + (((u >> 1) ^ (q << 2)) * 16) + (u & 1) * 8;
}

constexpr int K2_B_OFF   = 0;
constexpr int K2_A_OFF   = 114688;
constexpr int K2_CSP_OFF = K2_A_OFF + 32768;
constexpr int K2_CS_OFF  = K2_CSP_OFF + 2048;
constexpr int K2_M_OFF   = K2_CS_OFF + 512;
constexpr int K2_SY_OFF  = K2_M_OFF + 512;
constexpr int K2_END     = K2_SY_OFF + 64;
static_assert(K2_END <= K1_AUX_OFF, "lds map");
constexpr int K2_LDS     = K1_LDS;
constexpr unsigned SPIN_CAP = 1u << 20;
#define POLL_SLEEP 4
#define PH 2
#define PG3 2
#define PRIO_G3 __builtin_amdgcn_s_setprio(PG3)
#define PRIO_HI __builtin_amdgcn_s_setprio(PH)
#define PRIO_LO __builtin_amdgcn_s_setprio(PL)
constexpr size_t WS_FX1 = WS_CNT, WS_FH3 = WS_CNT + 4096, WS_FO = WS_CNT + 8192, WS_FCS = WS_CNT + 12288;
constexpr size_t WS_EPOCH = WS_CNT + 12288 + 1024;
constexpr size_t WS_F1 = 524288;
constexpr size_t WS_CS3 = 24576;

DI void gsync(LAS unsigned* f, int w, unsigned epoch) {
  asm volatile("s_waitcnt lgkmcnt(0)" ::: "memory");
  __hip_atomic_store(f + w, epoch, __ATOMIC_RELAXED, __HIP_MEMORY_SCOPE_WORKGROUP);
  for (unsigned s = 0; s < SPIN_CAP; s++) {
    u32x4 v;
    asm volatile("ds_read_b128 %0, %1\n\ts_waitcnt lgkmcnt(0)" : "=v"(v) : "v"((unsigned)(uintptr_t)f) : "memory");
    const unsigned a = v[0], b = v[1], c = v[2], d = v[3];
    const unsigned m = min(min(a, b), min(c, d));
    if ((unsigned)__builtin_amdgcn_readfirstlane(m) >= epoch) break;
    __builtin_amdgcn_s_sleep(1);
  }
}
DI void flag_set_t(char* p, int lane, unsigned tag) {
  if (lane == 0) __hip_atomic_store((GAS unsigned*)p, tag, __ATOMIC_RELAXED, __HIP_MEMORY_SCOPE_AGENT);
}
#define flag_set(p, lane) flag_set_t(p, lane, tag)
#define flags_wait(p) flags_wait_t(p, tag)
DI char* f1_flag(char* ws, int type, int sub, int tile) { return ws + WS_F1 + ((type * 4 + sub) * 16 + (tile >> 2)) * 128; }
DI void block_publish(LAS unsigned* cnt, char* p, int lane, unsigned tag) {
  if (lane == 0) {
    const unsigned old = __hip_atomic_fetch_add(cnt, 1u, __ATOMIC_RELAXED, __HIP_MEMORY_SCOPE_WORKGROUP);
    if (old == 7u) __hip_atomic_store((GAS unsigned*)p, tag, __ATOMIC_RELAXED, __HIP_MEMORY_SCOPE_AGENT);
  }
}
DI void flags_wait_t(const char* p, unsigned tag) {
  for (unsigned s = 0; s < SPIN_CAP; s++) {
    const unsigned v = __hip_atomic_load((const GAS unsigned*)p, __ATOMIC_RELAXED, __HIP_MEMORY_SCOPE_AGENT);
    if (__all(v == tag)) break;
    __builtin_amdgcn_s_sleep(POLL_SLEEP);
  }
  asm volatile("" ::: "memory");
}
DI u32x4 mkrsrc(const void* p, unsigned bytes) {
  const uint64_t a = (uint64_t)(uintptr_t)p;
  u32x4 r; r[0] = (unsigned)a; r[1] = (unsigned)(a >> 32) & 0xffffu; r[2] = bytes; r[3] = 0x00020000u;
  return r;
}
#define BLOAD(dst, voff, rs, soff, imm) \
  asm volatile("buffer_load_dwordx4 %0, %1, %2, %3 offen offset:%4" : "=v"(dst) : "v"(voff), "s"(rs), "s"(soff), "i"(imm) : "memory")
#define BLOAD_SC1(dst, voff, rs, soff, imm) \
  asm volatile("buffer_load_dwordx4 %0, %1, %2, %3 offen offset:%4 sc1" : "=v"(dst) : "v"(voff), "s"(rs), "s"(soff), "i"(imm) : "memory")
DI void st16_sc1(u32x4 v, __amdgpu_buffer_rsrc_t rs, unsigned off) { __builtin_amdgcn_raw_buffer_store_b128(v, rs, off, 0, 16); }
DI void st8_sc1(u32x2 v, __amdgpu_buffer_rsrc_t rs, unsigned off) { __builtin_amdgcn_raw_buffer_store_b64(v, rs, off, 0, 16); }

DI void tile_put(LAS char* p, const f32x16& t, int lane) {
#pragma unroll
  for (int g = 0; g < 4; g++) { f32x4 v; v[0] = t[4 * g]; v[1] = t[4 * g + 1]; v[2] = t[4 * g + 2]; v[3] = t[4 * g + 3]; *(LAS f32x4*)(p + g * 1024 + lane * 16) = v; }
}
DI void tile_add(const LAS char* p, f32x16& t, int lane) {
#pragma unroll
  for (int g = 0; g < 4; g++) { f32x4 v = *(const LAS f32x4*)(p + g * 1024 + lane * 16); t[4 * g] += v[0]; t[4 * g + 1] += v[1]; t[4 * g + 2] += v[2]; t[4 * g + 3] += v[3]; }
}
DI void reduce_scatter(LAS char* buf, f32x16 (&t)[4], int w, int lane, int x0, int x1, LAS unsigned* f, unsigned& epoch) {
  tile_put(buf + (((w ^ x0) * 2 + 0) * 4096), t[2], lane);
  tile_put(buf + (((w ^ x1) * 2 + 1) * 4096), t[3], lane);
  gsync(f, w, ++epoch);
  tile_add(buf + (((w ^ 2 ^ x0) * 2 + 0) * 4096), t[0], lane);
  tile_add(buf + (((w ^ 2 ^ x1) * 2 + 1) * 4096), t[1], lane);
  asm volatile("s_waitcnt lgkmcnt(0)" ::: "memory");
  tile_put(buf + (((w ^ 2 ^ x0) * 2 + 0) * 4096), t[1], lane);
  gsync(f, w, ++epoch);
  tile_add(buf + (((w ^ 3 ^ x0) * 2 + 0) * 4096), t[0], lane);
  asm volatile("s_waitcnt lgkmcnt(0)" ::: "memory");
}

DI unsigned w64_dma_voff(int lane, unsigned ldw) { const int rin = lane >> 3, pc = lane & 7; return rin * ldw + ((pc ^ (((rin >> 1) & 1) << 2)) * 16); }
DI unsigned w64_tr_off(int lane, int nt) {
  const int hh = lane >> 5, blk = (lane >> 4) & 1, q = (lane & 15) >> 2, p = lane & 3;
  return (8 * hh + q) * 128 + ((nt ^ ((q >> 1) & 1)) * 64) + blk * 32 + p * 8;
}

#define ATT_EXP4(SC, B, P, D)                                                                                    \
    SC[B] = __builtin_amdgcn_exp2f(SC[B]); SC[B + 1] = __builtin_amdgcn_exp2f(SC[B + 1]);                       \
    SC[B + 2] = __builtin_amdgcn_exp2f(SC[B + 2]); SC[B + 3] = __builtin_amdgcn_exp2f(SC[B + 3]);               \
    P[D] = pack2(SC[B], SC[B + 1]); P[D + 1] = pack2(SC[B + 2], SC[B + 3]);                                     \
    __builtin_amdgcn_sched_barrier(0);
#define ATT_STEP(SC, SN, PC0, PC1, PP0, PP1, KN0, KN1, QN0, QN1, VP0, VP1, OP)                                  \
  {                                                                                                             \
    SN = mfma(KN0, QN0, zero16());                                                                              \
    __builtin_amdgcn_sched_barrier(0);                                                                          \
    ATT_EXP4(SC, 0, PC0, 0)                                                                                     \
    SN = mfma(KN1, QN1, SN);                                                                                    \
    __builtin_amdgcn_sched_barrier(0);                                                                          \
    ATT_EXP4(SC, 4, PC0, 2)                                                                                     \
    OP = mfma(VP0, PP0, OP);                                                                                    \
    __builtin_amdgcn_sched_barrier(0);                                                                          \
    ATT_EXP4(SC, 8, PC1, 0)                                                                                     \
    OP = mfma(VP1, PP1, OP);                                                                                    \
    __builtin_amdgcn_sched_barrier(0);                                                                          \
    ATT_EXP4(SC, 12, PC1, 2)                                                                                    \
  }

DI float max16(const f32x16& s) {
  float a = fmaxf(fmaxf(s[0], s[1]), s[2]), b = fmaxf(fmaxf(s[3], s[4]), s[5]), c = fmaxf(fmaxf(s[6], s[7]), s[8]);
  float d = fmaxf(fmaxf(s[9], s[10]), s[11]), e = fmaxf(fmaxf(s[12], s[13]), s[14]);
  return fmaxf(fmaxf(fmaxf(a, b), c), fmaxf(fmaxf(d, e), s[15]));
}

template <int KIND>
DI void phase1(LAS char* lds, const char* __restrict__ x, const char* __restrict__ wsrc, const int ldw, const char* __restrict__ gate_w,
               char* __restrict__ ws, const int type, const int sub, const int tg, const int wv, const int stbase_, const char* touch) {
  const unsigned lds0 = (unsigned)(uintptr_t)lds;
  const int tid = threadIdx.x, lane = tid & 63, w = wv & 3, np = wv >> 2;
  const int r = lane & 31, hh = lane >> 5;
  const int tile = tg * 4 + w;
  const int drow = lane >> 4, dlc = (lane & 15) ^ (drow << 2);
  const unsigned dvoff = drow * ldw + (dlc < 14 ? dlc : 0) * 16;
  const uint64_t dmask = ~((3ull << 14) | (3ull << (16 + 10)) | (3ull << (32 + 6)) | (3ull << (48 + 2)));
  unsigned xv[2];
  {
    const int rin = lane >> 3, pc = lane & 7;
#pragma unroll
    for (int e = 0; e < 2; e++) xv[e] = rin * 896 + ((pc ^ ((4 * e + (rin >> 1)) & 7)) * 16);
  }
  const char* xbase = x + (size_t)(tg * 128 + w * 32 + np * 16) * 896;
  const char* wbase = wsrc + (size_t)(8 * wv) * ldw;
  auto issue_half = [&](int st, int jj) {
    const unsigned slot = lds0 + (st & 3) * K1_SLOT;
    dma16_masked(dvoff, wbase + (size_t)(64 * st + 4 * jj) * ldw, slot + (8 * wv + 4 * jj) * 256, dmask);
    dma16(xv[jj], xbase + jj * (8 * 896) + st * 128, slot + 16384 + w * 4096 + (2 * np + jj) * 1024);
  };
  unsigned gw0 = 0;
  if (KIND == 2) {
    const unsigned go = (tid < 448 ? tid : 447) * 2;
    asm volatile("global_load_ushort %0, %1, %2" : "=v"(gw0) : "v"(go), "s"(gate_w) : "memory");
  }
  issue_half(0, 0); issue_half(0, 1); issue_half(1, 0); issue_half(1, 1);
  unsigned xb[4];
#pragma unroll
  for (int ks = 0; ks < 4; ks++) xb[ks] = 16384 + w * 4096 + r * 128 + (((2 * ks + hh) ^ ((r >> 1) & 7)) * 16);
  const unsigned wb0 = k1_tr_off(lane, 2 * np), wb1 = k1_tr_off(lane, 2 * np + 1);
  const bool isaux = ((lane >> 4) & 1) == 1 && (lane & 3) == 3;
  const unsigned ab = isaux ? K1_AUX_OFF + (8 * hh + ((lane & 15) >> 2)) * 8 : wb1;
  const unsigned kstep = isaux ? 128u : 4096u, hoff = isaux ? 32u : 1024u;
  const unsigned sstep = isaux ? 512u : (unsigned)K1_SLOT, wrapst = isaux ? 512u : (unsigned)(-3 * K1_SLOT);
  unsigned a3[4];
#pragma unroll
  for (int ks = 0; ks < 4; ks++) a3[ks] = ab + ks * kstep;
  f32x16 acc[2], cs[2]; acc[0] = zero16(); acc[1] = zero16(); cs[0] = zero16(); cs[1] = zero16();
  u32x4 ones; ones[0] = ones[1] = ones[2] = ones[3] = 0x3F803F80u;
  u32x4 wf[2][4][2], xf[2][4], cf[2][2];
  float sqa[4] = {0.f, 0.f, 0.f, 0.f};
  unsigned sink;
  auto read_stage = [&](int st) {
    const int so = (st & 3) * K1_SLOT;
#pragma unroll
    for (int ks = 0; ks < 4; ks++) {
      xf[st & 1][ks] = *(const LAS u32x4*)(lds + xb[ks] + so);
      wf[st & 1][ks][0] = tr_frag(lds + wb0 + so + ks * 4096, 1024);
      if (KIND == 2) { wf[st & 1][ks][1] = tr_frag2(lds + a3[ks], lds + a3[ks] + hoff); a3[ks] += (st == 3) ? wrapst : sstep; }
      else wf[st & 1][ks][1] = tr_frag(lds + wb1 + so + ks * 4096, 1024);
    }
    if (KIND == 2) { cf[st & 1][0] = tr_frag(lds + wb0 + so + w * 4096, 1024); cf[st & 1][1] = tr_frag(lds + wb1 + so + w * 4096, 1024); }
  };
  asm volatile("s_waitcnt vmcnt(4)" : "+v"(gw0) :: "memory");
  if (KIND == 2) {
    u32x2 a; a[0] = 0x3F80u | (gw0 << 16); a[1] = 0u;
    *(LAS u32x2*)(lds + K1_AUX_OFF + (tid < 448 ? tid : 447) * 8) = a;
  }
  asm volatile("s_waitcnt lgkmcnt(0)\n\ts_barrier" ::: "memory");
  read_stage(0);
  issue_half(2, 0); issue_half(2, 1); issue_half(3, 0); issue_half(3, 1);
#pragma unroll
  for (int st = 0; st < 7; st++) {
    __builtin_amdgcn_sched_barrier(0);
    if (st < 3)       asm volatile("s_waitcnt vmcnt(8) lgkmcnt(0)\n\ts_barrier" ::: "memory");
    else if (st == 3) asm volatile("s_waitcnt vmcnt(9) lgkmcnt(0)\n\ts_barrier" ::: "memory");
    else if (st == 4) asm volatile("s_waitcnt vmcnt(5) lgkmcnt(0)\n\ts_barrier" ::: "memory");
    else if (st == 5) asm volatile("s_waitcnt vmcnt(1) lgkmcnt(0)\n\ts_barrier" ::: "memory");
    else              asm volatile("s_waitcnt lgkmcnt(0)\n\ts_barrier" ::: "memory");
    if (st < 6) read_stage(st + 1);
    __builtin_amdgcn_sched_barrier(0);
#pragma unroll
    for (int ks = 0; ks < 4; ks++) {
      acc[0] = KIND == 1 ? mfma(xf[st & 1][ks], wf[st & 1][ks][0], acc[0]) : mfma(wf[st & 1][ks][0], xf[st & 1][ks], acc[0]);
      if (KIND == 2) sq2(np ? xf[st & 1][ks][2] : xf[st & 1][ks][0], sqa[0], sqa[1]);
      acc[1] = KIND == 1 ? mfma(xf[st & 1][ks], wf[st & 1][ks][1], acc[1]) : mfma(wf[st & 1][ks][1], xf[st & 1][ks], acc[1]);
      if (KIND == 2) sq2(np ? xf[st & 1][ks][3] : xf[st & 1][ks][1], sqa[2], sqa[3]);
      if (st + 4 < 7 && (ks == 0 || ks == 2)) issue_half(st + 4, ks >> 1);
      if (st == TOUCH_ST && ks == 3) asm volatile("global_load_dword %0, %1, off" : "=v"(sink) : "v"(touch) : "memory");
    }
    if (KIND == 2) {
      cs[0] = mfma(ones, cf[st & 1][0], cs[0]); cs[1] = mfma(ones, cf[st & 1][1], cs[1]);
      asm volatile("" : "+v"(sqa[0]), "+v"(sqa[1]), "+v"(sqa[2]), "+v"(sqa[3]));
    }
  }
  __builtin_amdgcn_sched_barrier(0);
  asm volatile("s_waitcnt vmcnt(0)" : "+v"(sink) :: "memory");
  if (KIND < 2) {
    const float sc = type == 0 ? 0.27264314f : 1.0f;
    const __amdgpu_buffer_rsrc_t rq = __builtin_amdgcn_make_buffer_rsrc(ws + WS_Q, 0, 6u << 20, 0x00020000);
#pragma unroll
    for (int j = 0; j < 2; j++) {
      const int head = sub * 4 + 2 * np + j;
      u32x4 f0, f1;
#pragma unroll
      for (int d = 0; d < 4; d++) {
        f0[d] = pack2(acc[j][2 * d] * sc, acc[j][2 * d + 1] * sc);
        f1[d] = pack2(acc[j][8 + 2 * d] * sc, acc[j][8 + 2 * d + 1] * sc);
      }
      if (KIND == 1) {
        const unsigned v = r == 28 ? 0x3F803F80u : 0u;
#pragma unroll
        for (int d = 0; d < 4; d++) { f0[d] = r >= 28 ? v : f0[d]; f1[d] = r >= 28 ? v : f1[d]; }
      } else {
        f1[2] = hh ? (type == 1 ? 0x3F803F80u : 0u) : f1[2];
        f1[3] = hh ? (type == 1 ? 0x00003F80u : 0u) : f1[3];
      }
      const unsigned o = (unsigned)type * (2u << 20) + (unsigned)(head * 64 + tile) * 2048 + lane * 16;
      st16_sc1(f0, rq, o); st16_sc1(f1, rq, o + 1024);
    }
  } else {
    float sq = (sqa[0] + sqa[1]) + (sqa[2] + sqa[3]);
    sq += other_half(sq, hh);
    if (hh == 0) {
      *(LAS float*)(lds + K1_SQ_OFF + ((np * 4 + w) * 32 + r) * 4) = sq;
      *(LAS float*)(lds + K1_CS_OFF + (w * 128 + (2 * np) * 32 + r) * 4) = cs[0][0];
      *(LAS float*)(lds + K1_CS_OFF + (w * 128 + (2 * np + 1) * 32 + r) * 4) = cs[1][0];
    }
    if (sub == 0 && np == 0 && hh == 1) {
      const float gt = __builtin_amdgcn_rcpf(1.0f + __builtin_amdgcn_exp2f(-1.442695041f * acc[1][13]));
      __hip_atomic_store((GAS float*)(ws + WS_GATE) + tile * 32 + r, gt, __ATOMIC_RELAXED, __HIP_MEMORY_SCOPE_AGENT);
    }
    float sx = acc[1][12];
    { float o = other_half(sx, hh); sx = hh ? sx : o; }
    const float mean = sx * (1.0f / 448.0f);
    asm volatile("s_waitcnt lgkmcnt(0)\n\ts_barrier" ::: "memory");
    sq += *(LAS float*)(lds + K1_SQ_OFF + (((np ^ 1) * 4 + w) * 32 + r) * 4);
    const float var = fmaxf(sq * (1.0f / 448.0f) - mean * mean, 0.f) + 1e-5f;
    const float rstd = __builtin_amdgcn_rsqf(var);
    const float ga = -2.302208198f * rstd, gb = -0.1029432397f * rstd * rstd * rstd, irs = var * rstd;
    const __amdgpu_buffer_rsrc_t rh = __builtin_amdgcn_make_buffer_rsrc(ws + WS_H, 0, TBYTES, 0x00020000);
    unsigned pa[2][4][2];
#pragma unroll
    for (int j = 0; j < 2; j++) {
      const int t = 2 * np + j;
#pragma unroll
      for (int g = 0; g < 4; g++) {
        const LAS char* cp = lds + K1_CS_OFF + (t * 32 + 8 * g + 4 * hh) * 4;
        const f32x4 c = (*(const LAS f32x4*)cp + *(const LAS f32x4*)(cp + 512)) + (*(const LAS f32x4*)(cp + 1024) + *(const LAS f32x4*)(cp + 1536));
        pa[j][g][0] = gelu2(acc[j][4 * g], acc[j][4 * g + 1], c[0], c[1], -mean, ga, gb, irs);
        pa[j][g][1] = gelu2(acc[j][4 * g + 2], acc[j][4 * g + 3], c[2], c[3], -mean, ga, gb, irs);
      }
    }
    const unsigned dst = (unsigned)(tile * 56 + 14 * sub + 7 * np) * 512 + r * 16;
    u32x4 v0, v1, v2, v3;
#pragma unroll
    for (int d = 0; d < 2; d++) {
      auto s0 = __builtin_amdgcn_permlane32_swap(pa[0][0][d], pa[0][1][d], false, false);
      v0[d] = s0[0]; v0[2 + d] = s0[1];
      auto s1 = __builtin_amdgcn_permlane32_swap(pa[0][3][d], pa[0][2][d], false, false);
      v1[d] = s1[0]; v1[2 + d] = hh ? pa[0][2][d] : pa[1][0][d];
      auto s2 = __builtin_amdgcn_permlane32_swap(pa[1][0][d], pa[1][2][d], false, false);
      v2[d] = hh ? pa[1][1][d] : s2[1]; v2[2 + d] = hh ? s2[0] : pa[1][1][d];
      auto s3 = __builtin_amdgcn_permlane32_swap(pa[1][2][d], pa[1][2][d], false, false);
      v3[d] = s3[1]; v3[2 + d] = pa[1][3][d];
    }
    st16_sc1(v0, rh, dst + hh * 512);
    st16_sc1(v1, rh, dst + (hh ? 2 : 3) * 512);
    st16_sc1(v2, rh, dst + (4 + hh) * 512);
    st16_sc1(v3, rh, hh ? 0x80000000u : dst + 6 * 512);
  }
}

__global__ __launch_bounds__(512, 2) void fused_kernel(const char* __restrict__ x, const char* __restrict__ wqkv,
                                                       const char* __restrict__ w1, const char* __restrict__ gate_w,
                                                       const char* __restrict__ wo,
                                                       const char* __restrict__ w2, const char* __restrict__ w3,
                                                       const char* __restrict__ w4, char* __restrict__ ws,
                                                       char* __restrict__ out) {
  extern __shared__ __attribute__((aligned(16))) char smem[];
  LAS char* lds = (LAS char*)smem;
  const unsigned lds0 = (unsigned)(uintptr_t)lds;
  LAS unsigned* sy = (LAS unsigned*)(lds + K2_SY_OFF);
  const int tid = threadIdx.x, lane = tid & 63;
  const int wv = __builtin_amdgcn_readfirstlane(tid >> 6);
  const int w = wv & 3;
  const int b = blockIdx.x, xcd = b & 7, bi = b >> 3;
  const int r = lane & 31, hh = lane >> 5;
  const bool has_gemm = bi < 28;
  const int nb = bi % 7, tb64 = xcd * 4 + bi / 7;
  const int ni = w & 1, tj = w >> 1;
  const int p1type = bi < 24 ? bi % 3 : 3, p1tg = (xcd >> 2) * 8 + (bi < 24 ? bi / 3 : bi - 24);
  STAMP_DECL(256 + blockIdx.x) STAMP(0);
  if (tid < 16) sy[tid] = 0u;
  {
    const int type = p1type, sub = xcd & 3, tg = p1tg;
    const int sb_ = 0;
    int k = (b * 8 + wv) * 16 + (lane & 15); k = k < 31360 ? k : 31359;
    const char* touch = k < 3136 ? w2 + (size_t)k * 128 : k < 15680 ? w3 + (size_t)(k - 3136) * 128 : k < 28224 ? w4 + (size_t)(k - 15680) * 128 : wo + (size_t)(k - 28224) * 128;
    {
      uint64_t pc;
      asm volatile("s_getpc_b64 %0" : "=s"(pc));
      const int i = (bi * 8 + wv) * 2 + (lane & 1);
      if (lane >= 32) touch = (const char*)(uintptr_t)((pc & ~127ull) + (uint64_t)i * 128);
    }
    if (type < 2)       phase1<0>(lds, x, wqkv + (type * 448 + sub * 112) * 2, 2688, gate_w, ws, type, sub, tg, wv, sb_, touch);
    else if (type == 2) phase1<1>(lds, x, wqkv + (2 * 448 + sub * 112) * 2, 2688, gate_w, ws, type, sub, tg, wv, sb_, touch);
    else                phase1<2>(lds, x, w1 + sub * 224, 896, gate_w, ws, type, sub, tg, wv, sb_, touch);
  }
#define EPOCH_LOAD unsigned epoch; asm volatile("global_load_dword %0, %1, %2 sc1" : "=v"(epoch) : "v"(0u), "s"(ws + WS_EPOCH) : "memory")
#define EPOCH_WAIT(N) asm volatile("s_waitcnt vmcnt(" #N ")" : "+v"(epoch) :: "memory"); epoch = __builtin_amdgcn_readfirstlane(epoch); \
  const unsigned tag = (epoch * 0x9E3779B1u) ^ 0x5bd1e995u
  char* const my_f1 = f1_flag(ws, p1type, xcd & 3, p1tg * 4);
  STAMP(15);

  if (wv < 4) {
    const int head = 2 * xcd + (bi >> 4), qb = bi & 15;
    EPOCH_LOAD; EPOCH_WAIT(0);
    block_publish(sy + 12, my_f1, lane, tag);
    STAMP(13);
    const __amdgpu_buffer_rsrc_t rq = __builtin_amdgcn_make_buffer_rsrc(ws + WS_Q, 0, 6u << 20, 0x00020000);
    const unsigned qo = (unsigned)(head * 64 + qb * 4) * 2048 + lane * 16;
    const unsigned kho = (2u << 20) + (unsigned)(head * 64) * 2048 + lane * 16, vho = kho + (2u << 20);
    const unsigned ko = kho + w * 16 * 2048, vo = vho + w * 16 * 2048;
#define LDQ(off) __builtin_amdgcn_raw_buffer_load_b128(rq, (off), 0, 16)
    unsigned ea = 0;
    {
      const int hs = head >> 2;
      flags_wait(lane < 8 ? f1_flag(ws, 1 + (lane >> 2), hs, w * 16 + (lane & 3) * 4) : f1_flag(ws, 0, hs, qb * 4));
    }
    STAMP(14);
    __builtin_amdgcn_s_setprio(3);
    u32x4 q[4][2];
#pragma unroll
    for (int i = 0; i < 4; i++) { q[i][0] = LDQ(qo + (w ^ i) * 2048); q[i][1] = LDQ(qo + (w ^ i) * 2048 + 1024); }
    u32x4 kb[4][2], vb[4][2];
#pragma unroll
    for (int j = 0; j < 3; j++) { kb[j][0] = LDQ(ko + j * 2048); kb[j][1] = LDQ(ko + j * 2048 + 1024); vb[j][0] = LDQ(vo + j * 2048); vb[j][1] = LDQ(vo + j * 2048 + 1024); }
    f32x16 o[4];
    {
      LAS float* mx = (LAS float*)(lds + K2_M_OFF);
      f32x16 s = mfma(kb[0][0], q[0][0], zero16());
      s = mfma(kb[0][1], q[0][1], s);
      const float m = max16(s);
      if (hh) mx[w * 32 + r] = m;
      gsync(sy, w, ++ea);
#pragma unroll
      for (int i = 0; i < 4; i++) {
        const float mm = mx[(w ^ i) * 32 + r];
        q[i][1][2] = hh ? (pack2(-mm, 0.f)) : q[i][1][2];
      }
    }
    STAMP(1);
    __builtin_amdgcn_s_setprio(0);
    {
#pragma unroll
      for (int i = 0; i < 4; i++) o[i] = zero16();
      f32x16 sA, sB;
      u32x4 pA0, pA1, pB0, pB1;
      sA = mfma(kb[0][0], q[0][0], zero16());
      sA = mfma(kb[0][1], q[0][1], sA);
      pB0 = pB1 = (u32x4){0u, 0u, 0u, 0u};
      vb[3][0] = vb[3][1] = (u32x4){0u, 0u, 0u, 0u};
#pragma unroll 1
      for (int kq = 0; kq < 4; kq++) {
#pragma unroll
        for (int j = 0; j < 4; j++) {
          const int kt = kq * 4 + j;
          const int jp = (j + 3) & 3, jn = (j + 1) & 3;
          ATT_STEP(sA, sB, pA0, pA1, pB0, pB1, kb[j][0], kb[j][1], q[1][0], q[1][1], vb[jp][0], vb[jp][1], o[3]);
          {
            const int kn = kt + 3 < 16 ? kt + 3 : 15;
            __builtin_amdgcn_s_setprio(3);
            kb[jp][0] = LDQ(ko + kn * 2048); kb[jp][1] = LDQ(ko + kn * 2048 + 1024); vb[jp][0] = LDQ(vo + kn * 2048); vb[jp][1] = LDQ(vo + kn * 2048 + 1024);
            __builtin_amdgcn_sched_barrier(0);
            __builtin_amdgcn_s_setprio(0);
          }
          ATT_STEP(sB, sA, pB0, pB1, pA0, pA1, kb[j][0], kb[j][1], q[2][0], q[2][1], vb[j][0], vb[j][1], o[0]);
          ATT_STEP(sA, sB, pA0, pA1, pB0, pB1, kb[j][0], kb[j][1], q[3][0], q[3][1], vb[j][0], vb[j][1], o[1]);
          ATT_STEP(sB, sA, pB0, pB1, pA0, pA1, kb[jn][0], kb[jn][1], q[0][0], q[0][1], vb[j][0], vb[j][1], o[2]);
        }
      }
      o[3] = mfma(vb[3][0], pB0, o[3]);
      o[3] = mfma(vb[3][1], pB1, o[3]);
    }
    STAMP(2);
    __builtin_amdgcn_s_setprio(3);
    flags_wait(f1_flag(ws, 3, 0, qb * 4));
    const float gate_q = __hip_atomic_load((const GAS float*)(ws + WS_GATE) + (qb * 4 + w) * 32 + r, __ATOMIC_RELAXED, __HIP_MEMORY_SCOPE_AGENT);
    reduce_scatter(lds + K2_A_OFF, o, w, lane, 0, 0, sy, ea);
    float l = o[0][12];
    { float t = other_half(l, hh); l = hh ? l : t; }
    {
      float amax = 0.f;
#pragma unroll
      for (int i = 0; i < 12; i++) amax = fmaxf(amax, fabsf(o[0][i]));
      if (!hh) amax = fmaxf(fmaxf(amax, fabsf(o[0][12])), fmaxf(fabsf(o[0][13]), fmaxf(fabsf(o[0][14]), fabsf(o[0][15]))));
      const bool ok = (l > 1.0e-10f) && (l < 1.0e18f) && (amax < 1.0e37f);
      if (__any(!ok)) {
        flags_wait(f1_flag(ws, 1 + (lane >> 5), head >> 2, (lane & 15) * 4));
        u32x4 q0 = q[0][0], q1 = q[0][1];
        q1[2] = hh ? 0u : q1[2];
        float tm = -3.0e38f;
#pragma unroll 1
        for (int kt = 0; kt < 64; kt++) {
          f32x16 s = mfma(LDQ(kho + kt * 2048), q0, zero16());
          s = mfma(LDQ(kho + kt * 2048 + 1024), q1, s);
          tm = fmaxf(tm, max16(s));
        }
        tm = -fmaxf(tm, other_half(tm, hh));
        const unsigned h0 = pack2(tm, 0.f) & 0xffffu; const float f0 = __builtin_bit_cast(float, h0 << 16);
        const unsigned h1 = pack2(tm - f0, 0.f) & 0xffffu; const float f1 = __builtin_bit_cast(float, h1 << 16);
        const unsigned h2 = pack2((tm - f0) - f1, 0.f) & 0xffffu;
        q1[2] = hh ? (h0 | (h1 << 16)) : q1[2];
        q1[3] = hh ? h2 : q1[3];
        f32x16 oo = zero16();
#pragma unroll 1
        for (int kt = 0; kt < 64; kt++) {
          f32x16 s = mfma(LDQ(kho + kt * 2048), q0, zero16());
          s = mfma(LDQ(kho + kt * 2048 + 1024), q1, s);
          u32x4 p0, p1;
#pragma unroll
          for (int d = 0; d < 4; d++) {
            p0[d] = pack2(__builtin_amdgcn_exp2f(s[2 * d]), __builtin_amdgcn_exp2f(s[2 * d + 1]));
            p1[d] = pack2(__builtin_amdgcn_exp2f(s[8 + 2 * d]), __builtin_amdgcn_exp2f(s[8 + 2 * d + 1]));
          }
          oo = mfma(LDQ(vho + kt * 2048), p0, oo);
          oo = mfma(LDQ(vho + kt * 2048 + 1024), p1, oo);
        }
        o[0] = oo;
        l = oo[12];
        { float t = other_half(l, hh); l = hh ? l : t; }
      }
    }
    {
      const __amdgpu_buffer_rsrc_t ro = __builtin_amdgcn_make_buffer_rsrc(ws + WS_O, 0, TBYTES, 0x00020000);
      const float inv = gate_q * __builtin_amdgcn_rcpf(l);
      const unsigned base = (unsigned)(qb * 4 + w) * (56 * 512) + r * 16;
#pragma unroll
      for (int g = 0; g < 4; g++) {
        u32x2 v; v[0] = pack2(o[0][4 * g] * inv, o[0][4 * g + 1] * inv); v[1] = pack2(o[0][4 * g + 2] * inv, o[0][4 * g + 3] * inv);
        const int u = 7 * head + 2 * g + hh;
        if (g < 3 || hh == 0) st8_sc1(v, ro, base + (u >> 1) * 512 + (u & 1) * 8);
      }
    }
    asm volatile("s_waitcnt vmcnt(0)" ::: "memory");
    flag_set(ws + WS_FO + ((qb * 4 + w) * 16 + head) * 4, lane);
    STAMP(3);
    return;
  }

  if (!has_gemm) {
    EPOCH_LOAD; EPOCH_WAIT(0);
    block_publish(sy + 12, my_f1, lane, tag);
    if (b == 224 && w == 3) {
#pragma unroll 1
      for (int i = 0; i < 4; i++) flags_wait(ws + WS_F1 + (i * 64 + lane) * 128);
      if (lane == 0) __hip_atomic_store((GAS unsigned*)(ws + WS_EPOCH), epoch + 1u, __ATOMIC_RELAXED, __HIP_MEMORY_SCOPE_AGENT);
    }
    const int id = (xcd * 4 + bi - 28) * 2 + w;
    if (w < 2 && id < 56) {
      const unsigned reg = K2_B_OFF + w * 28672;
      const unsigned dv = (lane >> 2) * 3584 + (lane & 3) * 16;
      const char* src = w3 + id * 64;
#pragma unroll
      for (int j = 0; j < 28; j++) dma16(dv, src + (size_t)j * (16 * 3584), lds0 + reg + j * 1024);
      asm volatile("s_waitcnt vmcnt(0)" ::: "memory");
      const unsigned to = reg + (8 * hh + ((lane & 15) >> 2)) * 64 + ((lane >> 4) & 1) * 32 + (lane & 3) * 8;
      u32x4 ones; ones[0] = ones[1] = ones[2] = ones[3] = 0x3F803F80u;
      f32x16 cs = zero16();
#pragma unroll
      for (int s = 0; s < 28; s++) cs = mfma(ones, tr_frag(lds + to + s * 1024, 256), cs);
      if (hh == 0) __hip_atomic_store((GAS float*)(ws + WS_CS3) + id * 32 + r, cs[0], __ATOMIC_RELAXED, __HIP_MEMORY_SCOPE_AGENT);
      asm volatile("s_waitcnt vmcnt(0)" ::: "memory");
      flag_set(ws + WS_FCS + id * 4, lane);
    }
    return;
  }
  PRIO_LO;
  unsigned eb = 0;
  LAS unsigned* syb = sy + 4;
  const int nb3 = bi % 14, tb128 = xcd * 2 + bi / 14;
  const unsigned dv64 = w64_dma_voff(lane, 896);
  const unsigned t0 = w64_tr_off(lane, ni), t1 = w64_tr_off(lane, ni ^ 1);
  const unsigned dv3 = (lane >> 4) * 3584 + (((lane & 15) ^ ((lane >> 4) << 2)) * 16);
  const char* w3src = w3 + nb3 * 256;
  const unsigned ring = K2_B_OFF + w * 28672;
  const char* w4src = w4 + (size_t)(448 * w) * 896 + nb * 128;
  unsigned x1p[8];
  const unsigned sl = K2_B_OFF + 57344 + w * 14336;
  EPOCH_LOAD;
  EPOCH_WAIT(0);
  block_publish(sy + 12, my_f1, lane, tag);
  STAMP(14);
  {
    const char* wsrc = w2 + (size_t)(112 * w) * 896 + nb * 128;
#pragma unroll
    for (int j = 0; j < 14; j++) dma16(dv64, wsrc + j * (8 * 896), lds0 + sl + j * 1024);
  }
#pragma unroll
  for (int j = 0; j < 14; j++) { const int row0 = 56 * w + 4 * j; dma16(dv3, w3src + (size_t)row0 * 3584, lds0 + K2_B_OFF + row0 * 256); }
  {
    const u32x4 rh = mkrsrc(ws + WS_H, TBYTES);
    const int delta = w == 0 ? -1 : w == 1 ? 1 : w == 2 ? -2 : 2;
    u32x4 af[7][2];
    unsigned av[2];
#pragma unroll
    for (int e = 0; e < 2; e++) {
      const int tok = (tb64 * 2 + (tj ^ e)) * 32 + r + delta;
      av[e] = (tok >= 0 && tok < SEQ) ? (unsigned)(((tok >> 5) * 56 + 14 * w + hh) * 512 + (tok & 31) * 16) : 0x80000000u;
    }
    u32x2 xr[4];
    {
      const char* xp = x + (size_t)((tb64 * 2 + tj) * 32) * 896 + (nb * 64 + ni * 32) * 2;
      const unsigned xo = r * 896 + hh * 8;
#pragma unroll
      for (int g = 0; g < 4; g++) asm volatile("global_load_dwordx2 %0, %1, %2 offset:%3" : "=v"(xr[g]) : "v"(xo), "s"(xp), "i"(g * 16) : "memory");
    }
    {
      int tl = tb64 * 2 - 1 + (lane & 3); tl = tl < 0 ? 0 : tl > 63 ? 63 : tl;
      flags_wait(f1_flag(ws, 3, w, tl));
    }
    asm volatile("s_waitcnt vmcnt(0)" : "+v"(xr[0]), "+v"(xr[1]), "+v"(xr[2]), "+v"(xr[3]) :: "memory");
    STAMP(10);
#pragma unroll
    for (int s = 0; s < 7; s++) {
      BLOAD_SC1(af[s][0], av[0], rh, (s >> 2) * 4096, (s & 3) * 1024);
      BLOAD_SC1(af[s][1], av[1], rh, (s >> 2) * 4096, (s & 3) * 1024);
    }
    PRIO_HI;
    f32x16 t[4];
#pragma unroll
    for (int i = 0; i < 4; i++) t[i] = zero16();
    u32x4 f0[7], f1[7];
#pragma unroll
    for (int s = 0; s < 7; s++) { f0[s] = tr_frag(lds + sl + t0 + s * 2048, 512); f1[s] = tr_frag(lds + sl + t1 + s * 2048, 512); }
    STAMP(1);
#pragma unroll
    for (int s = 0; s < 7; s++) {
      asm volatile("s_waitcnt vmcnt(%2)" : "+v"(af[s][0]), "+v"(af[s][1]) : "i"(12 - 2 * s) : "memory");
      t[0] = mfma(f0[s], af[s][0], t[0]); t[1] = mfma(f1[s], af[s][0], t[1]);
      t[2] = mfma(f0[s], af[s][1], t[2]); t[3] = mfma(f1[s], af[s][1], t[3]);
      __builtin_amdgcn_sched_barrier(0);
    }
    STAMP(11);
    asm volatile("s_waitcnt lgkmcnt(0)" ::: "memory");
#pragma unroll
    for (int i = 1; i < 4; i++) tile_put(lds + sl + (i - 1) * 4096, t[i], lane);
    gsync(syb, w, ++eb);
#pragma unroll
    for (int i = 1; i < 4; i++) tile_add(lds + K2_B_OFF + 57344 + (w ^ i) * 14336 + (i - 1) * 4096, t[0], lane);
    float s1 = 0.f, s2 = 0.f;
#pragma unroll
    for (int g = 0; g < 4; g++) {
      const float a0 = __builtin_bit_cast(float, xr[g][0] << 16) + t[0][4 * g], a1 = __builtin_bit_cast(float, xr[g][0] & 0xffff0000u) + t[0][4 * g + 1];
      const float a2 = __builtin_bit_cast(float, xr[g][1] << 16) + t[0][4 * g + 2], a3 = __builtin_bit_cast(float, xr[g][1] & 0xffff0000u) + t[0][4 * g + 3];
      x1p[2 * g] = pack2(a0, a1); x1p[2 * g + 1] = pack2(a2, a3);
    }
    const __amdgpu_buffer_rsrc_t rx = __builtin_amdgcn_make_buffer_rsrc(ws + WS_X1, 0, TBYTES, 0x00020000);
    const int tile = tb64 * 2 + tj;
#pragma unroll
    for (int gp = 0; gp < 2; gp++) {
      auto a = __builtin_amdgcn_permlane32_swap(x1p[4 * gp], x1p[4 * gp + 2], false, false);
      auto c = __builtin_amdgcn_permlane32_swap(x1p[4 * gp + 1], x1p[4 * gp + 3], false, false);
      u32x4 v; v[0] = a[0]; v[1] = c[0]; v[2] = a[1]; v[3] = c[1];
      st16_sc1(v, rx, (unsigned)(tile * 56 + nb * 8 + ni * 4 + 2 * gp + hh) * 512 + r * 16);
    }
#pragma unroll
    for (int j = 0; j < 8; j++) {
      const float lo = __builtin_bit_cast(float, x1p[j] << 16), hi = __builtin_bit_cast(float, x1p[j] & 0xffff0000u);
      s1 += lo + hi; s2 = __builtin_fmaf(lo, lo, s2); s2 = __builtin_fmaf(hi, hi, s2);
    }
    s1 += other_half(s1, hh); s2 += other_half(s2, hh);
    const __amdgpu_buffer_rsrc_t rs = __builtin_amdgcn_make_buffer_rsrc(ws + WS_STATS, 0, 64 * 14 * 32 * 8, 0x00020000);
    { u32x2 v; v[0] = __builtin_bit_cast(unsigned, s1); v[1] = __builtin_bit_cast(unsigned, s2); st8_sc1(v, rs, hh ? 0x80000000u : (unsigned)((tile * 14 + nb * 2 + ni) * 32 + r) * 8); }
    STAMP(12);
    gsync(syb, w, ++eb);
#pragma unroll
    for (int j = 0; j < 14; j++) { const int row0 = 224 + 56 * w + 4 * j; dma16(dv3, w3src + (size_t)row0 * 3584, lds0 + K2_B_OFF + row0 * 256); }
    asm volatile("s_waitcnt vmcnt(14)" ::: "memory");
    flag_set(ws + WS_FX1 + (tile * 16 + nb * 2 + ni) * 4, lane);
    STAMP(2);
  }
  {
    asm volatile("s_waitcnt vmcnt(0)" ::: "memory");
    gsync(syb, w, ++eb);
    unsigned tb[4];
    {
      const int blk = (lane >> 4) & 1, q = (lane & 15) >> 2, p = lane & 3;
#pragma unroll
      for (int n = 0; n < 4; n++) tb[n] = K2_B_OFF + (8 * hh + q) * 256 + ((n ^ q) * 64) + blk * 32 + p * 8;
    }
    STAMP(3);
    const int tile = tb128 * 4 + w;
    flags_wait(lane < 16 ? ws + WS_FX1 + (tile * 16 + (lane < 14 ? lane : 0)) * 4 : lane < 32 ? ws + WS_FCS + (nb3 * 4 + (lane & 3)) * 4
               : f1_flag(ws, 3, 0, lane < 48 ? tile : tb64 * 2 + tj));
    STAMP(4);
    const u32x4 rx = mkrsrc(ws + WS_X1, TBYTES);
    const u32x4 rsd = mkrsrc(ws + WS_STATS, 64 * 14 * 32 * 8);
    const u32x4 rcd = mkrsrc(ws + WS_CS3, 1792 * 4);
    u32x2 sv[7], cv; float g3;
    {
      const unsigned so = (unsigned)((tile * 14 + hh * 7) * 32 + r) * 8, co = (unsigned)(nb3 * 128 + lane * 2) * 4;
#pragma unroll
      for (int j = 0; j < 7; j++) asm volatile("buffer_load_dwordx2 %0, %1, %2, 0 offen offset:%3 sc1" : "=v"(sv[j]) : "v"(so), "s"(rsd), "i"(j * 256) : "memory");
      asm volatile("buffer_load_dwordx2 %0, %1, %2, 0 offen sc1" : "=v"(cv) : "v"(co), "s"(rcd) : "memory");
      const unsigned go = (unsigned)(tile * 32 + r) * 4;
      asm volatile("global_load_dword %0, %1, %2 sc1" : "=v"(g3) : "v"(go), "s"(ws + WS_GATE) : "memory");
    }
    constexpr int AW = 16;
    u32x4 af[AW];
    const unsigned av = (unsigned)(tile * 56) * 512 + lane * 16;
#pragma unroll
    for (int s = 0; s < AW; s++) BLOAD_SC1(af[s], av, rx, (s >> 2) * 4096, (s & 3) * 1024);
    const __amdgpu_buffer_rsrc_t r3 = __builtin_amdgcn_make_buffer_rsrc(ws + WS_H3, 0, T3BYTES, 0x00020000);
    STAMP(13);
    PRIO_G3;
    u32x4 wfr[3][4];
#pragma unroll
    for (int k = 0; k < 2; k++)
#pragma unroll
      for (int n = 0; n < 4; n++) wfr[k][n] = tr_frag(lds + tb[n] + k * 4096, 1024);
    f32x16 acc[4];
#pragma unroll
    for (int n = 0; n < 4; n++) acc[n] = zero16();
#pragma unroll
    for (int s = 0; s < 28; s++) {
      asm volatile("s_waitcnt vmcnt(%1)" : "+v"(af[s % AW]) : "i"(s + AW <= 28 ? AW - 1 : 27 - s) : "memory");
#pragma unroll
      for (int n = 0; n < 4; n++) {
        acc[n] = mfma(wfr[s % 3][n], af[s % AW], acc[n]);
        if (s + 2 < 28) wfr[(s + 2) % 3][n] = tr_frag(lds + tb[n] + (s + 2) * 4096, 1024);
        __builtin_amdgcn_sched_barrier(0);
      }
      if (s + AW < 28) BLOAD_SC1(af[s % AW], av, rx, ((s + AW) >> 2) * 4096, ((s + AW) & 3) * 1024);
      __builtin_amdgcn_sched_barrier(0);
    }
    STAMP(8);
    PRIO_HI;
    asm volatile("" : "+v"(sv[0]), "+v"(sv[1]), "+v"(sv[2]), "+v"(sv[3]), "+v"(sv[4]), "+v"(sv[5]), "+v"(sv[6]), "+v"(cv), "+v"(g3));
    const unsigned csl = K2_CSP_OFF + w * 512;
    *(LAS u32x2*)(lds + csl + lane * 8) = cv;
    float nmean, ga, gb, irs;
    {
      float s1 = 0.f, s2 = 0.f;
#pragma unroll
      for (int j = 0; j < 7; j++) { const unsigned u0 = sv[j][0], u1 = sv[j][1]; s1 += __builtin_bit_cast(float, u0); s2 += __builtin_bit_cast(float, u1); }
      s1 += other_half(s1, hh); s2 += other_half(s2, hh);
      const float mean = s1 * (1.0f / 448.0f);
      const float var = fmaxf(s2 * (1.0f / 448.0f) - mean * mean, 0.f) + 1e-5f;
      const float rstd = __builtin_amdgcn_rsqf(var);
      nmean = -mean; ga = -2.302208198f * rstd; gb = -0.1029432397f * rstd * rstd * rstd;
      irs = var * rstd * __builtin_amdgcn_rcpf(fmaxf(1.0f - g3, 1.0e-30f));
    }
#pragma unroll
    for (int n = 0; n < 4; n++) {
#pragma unroll
      for (int gp = 0; gp < 2; gp++) {
        unsigned pk[4];
#pragma unroll
        for (int g2 = 0; g2 < 2; g2++) {
          const int g = 2 * gp + g2;
          const f32x4 c4 = *(const LAS f32x4*)(lds + csl + (n * 32 + 8 * g + 4 * hh) * 4);
          pk[2 * g2] = gelu2(acc[n][4 * g], acc[n][4 * g + 1], c4[0], c4[1], nmean, ga, gb, irs);
          pk[2 * g2 + 1] = gelu2(acc[n][4 * g + 2], acc[n][4 * g + 3], c4[2], c4[3], nmean, ga, gb, irs);
        }
        auto a = __builtin_amdgcn_permlane32_swap(pk[0], pk[2], false, false);
        auto c = __builtin_amdgcn_permlane32_swap(pk[1], pk[3], false, false);
        u32x4 v; v[0] = a[0]; v[1] = c[0]; v[2] = a[1]; v[3] = c[1];
        st16_sc1(v, r3, (unsigned)(tile * 224 + nb3 * 16 + n * 4 + 2 * gp + hh) * 512 + r * 16);
      }
    }
    PRIO_HI;
    STAMP(5);
    gsync(syb, w, ++eb);
    asm volatile("s_waitcnt vmcnt(0)" ::: "memory");
    flag_set(ws + WS_FH3 + (tile * 16 + nb3) * 4, lane);
    PRIO_LO;
  }
  {
    const char* wsrc = w4src;
#pragma unroll
    for (int j = 0; j < 28; j++) dma16(dv64, wsrc + j * (8 * 896), lds0 + ring + j * 1024);
    const float gate = __hip_atomic_load((const GAS float*)(ws + WS_GATE) + (tb64 * 2 + tj) * 32 + r, __ATOMIC_RELAXED, __HIP_MEMORY_SCOPE_AGENT);
    {
      const int blk0 = (7 * w) >> 1;
      const int k = lane & 3;
      flags_wait(ws + WS_FH3 + ((tb64 * 2 + ((lane >> 2) & 1)) * 16 + blk0 + k) * 4);
    }
    asm volatile("s_waitcnt vmcnt(0)" ::: "memory");
    STAMP(6);
    const u32x4 r3 = mkrsrc(ws + WS_H3, T3BYTES);
    unsigned av[2];
#pragma unroll
    for (int e = 0; e < 2; e++) av[e] = (unsigned)((tb64 * 2 + (tj ^ e)) * 224 + 56 * w) * 512 + lane * 16;
    u32x4 af[14][2];
#pragma unroll
    for (int s = 0; s < 14; s++)
#pragma unroll
      for (int e = 0; e < 2; e++) BLOAD_SC1(af[s][e], av[e], r3, (s >> 2) * 4096, (s & 3) * 1024);
    f32x16 t[4];
#pragma unroll
    for (int i = 0; i < 4; i++) t[i] = zero16();
    u32x4 f0[2], f1[2];
    const char* wosrc = wo + (size_t)(112 * w) * 896 + nb * 128;
    f0[0] = tr_frag(lds + ring + t0, 512); f1[0] = tr_frag(lds + ring + t1, 512);
#pragma unroll
    for (int s = 0; s < 28; s++) {
      const int sl = s % 14;
      asm volatile("s_waitcnt lgkmcnt(0)" : "+v"(f0[s & 1]), "+v"(f1[s & 1]) :: "memory");
      if (s + 1 < 28) {
        const int nl = (s + 1) % 14;
        if (s == 13) asm volatile("s_waitcnt vmcnt(50)" ::: "memory");
        else if (s > 13) asm volatile("s_waitcnt vmcnt(%0)" :: "i"(2 + 4 * (13 - nl) + 2 * (s - 14 < 7 ? s - 14 : 7)) : "memory");
        f0[(s + 1) & 1] = tr_frag(lds + ring + t0 + nl * 2048, 512); f1[(s + 1) & 1] = tr_frag(lds + ring + t1 + nl * 2048, 512);
      }
      if (s < 14) {
        dma16(dv64, wsrc + (28 + 2 * s) * (8 * 896), lds0 + ring + (2 * s) * 1024);
        dma16(dv64, wsrc + (29 + 2 * s) * (8 * 896), lds0 + ring + (2 * s + 1) * 1024);
        asm volatile("s_waitcnt vmcnt(%2)" : "+v"(af[s][0]), "+v"(af[s][1]) : "i"(2 * (13 - s) + 4 * s + 2) : "memory");
      } else {
        if (s < 21) {
          dma16(dv64, wosrc + (2 * sl) * (8 * 896), lds0 + ring + (2 * sl) * 1024);
          dma16(dv64, wosrc + (2 * sl + 1) * (8 * 896), lds0 + ring + (2 * sl + 1) * 1024);
        }
        asm volatile("s_waitcnt vmcnt(%2)" : "+v"(af[sl][0]), "+v"(af[sl][1]) : "i"(4 * (13 - sl) + 2 * (s - 13 < 7 ? s - 13 : 7)) : "memory");
      }
      t[0] = mfma(f0[s & 1], af[sl][0], t[0]); t[1] = mfma(f1[s & 1], af[sl][0], t[1]);
      t[2] = mfma(f0[s & 1], af[sl][1], t[2]); t[3] = mfma(f1[s & 1], af[sl][1], t[3]);
      if (s < 14) {
        BLOAD_SC1(af[s][0], av[0], r3, ((s + 14) >> 2) * 4096, ((s + 14) & 3) * 1024);
        BLOAD_SC1(af[s][1], av[1], r3, ((s + 14) >> 2) * 4096, ((s + 14) & 3) * 1024);
      }
      __builtin_amdgcn_sched_barrier(0);
    }
    STAMP(7);
    PRIO_HI;
    flags_wait(ws + WS_FO + ((tb64 * 2 + ((lane >> 2) & 1)) * 16 + 4 * w + (lane & 3)) * 4);
    asm volatile("s_waitcnt vmcnt(0)" ::: "memory");
    {
      const u32x4 ro = mkrsrc(ws + WS_O, TBYTES);
      unsigned ov[2];
#pragma unroll
      for (int e = 0; e < 2; e++) ov[e] = (unsigned)((tb64 * 2 + (tj ^ e)) * 56 + 14 * w) * 512 + lane * 16;
#pragma unroll
      for (int s = 0; s < 7; s++)
#pragma unroll
        for (int e = 0; e < 2; e++) BLOAD_SC1(af[s][e], ov[e], ro, (s >> 2) * 4096, (s & 3) * 1024);
      u32x4 g0[7], g1[7];
#pragma unroll
      for (int s = 0; s < 7; s++) { g0[s] = tr_frag(lds + ring + t0 + s * 2048, 512); g1[s] = tr_frag(lds + ring + t1 + s * 2048, 512); }
#pragma unroll
      for (int s = 0; s < 7; s++) {
        asm volatile("s_waitcnt vmcnt(%2)" : "+v"(af[s][0]), "+v"(af[s][1]) : "i"(2 * (6 - s)) : "memory");
        t[0] = mfma(g0[s], af[s][0], t[0]); t[1] = mfma(g1[s], af[s][0], t[1]);
        t[2] = mfma(g0[s], af[s][1], t[2]); t[3] = mfma(g1[s], af[s][1], t[3]);
      }
    }
    asm volatile("s_waitcnt lgkmcnt(0)" ::: "memory");
#pragma unroll
    for (int i = 1; i < 4; i++) tile_put(lds + ring + (i - 1) * 4096, t[i], lane);
    gsync(syb, w, ++eb);
#pragma unroll
    for (int i = 1; i < 4; i++) tile_add(lds + K2_B_OFF + (w ^ i) * 28672 + (i - 1) * 4096, t[0], lane);
    unsigned pk[8];
    const float og = 1.0f - gate;
#pragma unroll
    for (int j = 0; j < 8; j++)
      pk[j] = pack2(__builtin_fmaf(og, __builtin_bit_cast(float, x1p[j] << 16), t[0][2 * j]), __builtin_fmaf(og, __builtin_bit_cast(float, x1p[j] & 0xffff0000u), t[0][2 * j + 1]));
    char* op = out + (size_t)((tb64 * 2 + tj) * 32 + r) * 896 + (nb * 64 + ni * 32) * 2 + hh * 16;
#pragma unroll
    for (int gp = 0; gp < 2; gp++) {
      auto a2 = __builtin_amdgcn_permlane32_swap(pk[4 * gp], pk[4 * gp + 2], false, false);
      auto c2 = __builtin_amdgcn_permlane32_swap(pk[4 * gp + 1], pk[4 * gp + 3], false, false);
      u32x4 v; v[0] = a2[0]; v[1] = c2[0]; v[2] = a2[1]; v[3] = c2[1];
      *(u32x4*)(op + gp * 32) = v;
    }
    STAMP(9);
  }
}

extern "C" void kernel_launch(void* const* d_in, const int* in_sizes, int n_in,
                              void* d_out, int out_size, void* d_ws, size_t ws_size,
                              hipStream_t stream) {
  const char* x = (const char*)d_in[0]; const char* wqkv = (const char*)d_in[1]; const char* wo = (const char*)d_in[2];
  const char* w1 = (const char*)d_in[4]; const char* w2 = (const char*)d_in[5]; const char* w3 = (const char*)d_in[6];
  const char* w4 = (const char*)d_in[7]; const char* gw = (const char*)d_in[8];
  (void)hipFuncSetAttribute((const void*)fused_kernel, hipFuncAttributeMaxDynamicSharedMemorySize, K2_LDS);
  fused_kernel<<<256, 512, K2_LDS, stream>>>(x, wqkv, w1, gw, wo, w2, w3, w4, (char*)d_ws, (char*)d_out);
}
```

```cpp
#include <hip/hip_runtime.h>
#include <stdint.h>

#define TOUCH_ST 2
#define TOUCH 3
#define PL 2
#define LDPRIO

typedef __attribute__((ext_vector_type(8))) __bf16 bf16x8;
typedef __attribute__((ext_vector_type(2))) __bf16 bf16x2;
typedef __attribute__((ext_vector_type(4))) short s16x4;
typedef __attribute__((ext_vector_type(16))) float f32x16;
typedef __attribute__((ext_vector_type(4))) float f32x4;
typedef __attribute__((ext_vector_type(2))) float f32x2;
typedef __attribute__((ext_vector_type(4))) unsigned u32x4;
typedef __attribute__((ext_vector_type(2))) unsigned u32x2;
#define LAS __attribute__((address_space(3)))
#define GAS __attribute__((address_space(1)))
#define DI __device__ __forceinline__

constexpr int SEQ = 2048, DM = 448, NH = 16, DH = 28, FF = 1792;

constexpr size_t WS_CNT   = 0;
constexpr size_t WS_GATE  = 16384;
constexpr size_t WS_STATS = 32768;
constexpr size_t WS_Q     = 1u << 20;
constexpr size_t WS_K     = WS_Q + (2u << 20);
constexpr size_t WS_V     = WS_K + (2u << 20);
constexpr size_t WS_H     = WS_V + (2u << 20);
constexpr size_t WS_X1    = WS_H + (2u << 20);
constexpr size_t WS_O     = WS_X1 + (2u << 20);
constexpr size_t WS_H3    = WS_O + (2u << 20);
constexpr unsigned TBYTES = 64u * 56u * 512u;
constexpr unsigned T3BYTES = 64u * 224u * 512u;

DI f32x16 mfma(u32x4 a, u32x4 b, f32x16 c) {
  return __builtin_amdgcn_mfma_f32_32x32x16_bf16(__builtin_bit_cast(bf16x8, a), __builtin_bit_cast(bf16x8, b), c, 0, 0, 0);
}
DI unsigned pack2(float a, float b) {
  f32x2 v; v[0] = a; v[1] = b;
  return __builtin_bit_cast(unsigned, __builtin_convertvector(v, bf16x2));
}
DI u32x4 tr_frag(const LAS char* p, int hi_off) {
  s16x4 lo = __builtin_amdgcn_ds_read_tr16_b64_v4i16((LAS s16x4*)p);
  s16x4 hi = __builtin_amdgcn_ds_read_tr16_b64_v4i16((LAS s16x4*)(p + hi_off));
  u32x2 a = __builtin_bit_cast(u32x2, lo), b = __builtin_bit_cast(u32x2, hi);
  u32x4 r; r[0] = a[0]; r[1] = a[1]; r[2] = b[0]; r[3] = b[1];
  return r;
}
DI float gelu_f(float x) {
  const float C0 = -2.302208198f;
  const float C1 = -0.1029432397f;
  float z = x * __builtin_fmaf(x * x, C1, C0);
  float e = __builtin_amdgcn_exp2f(z);
  return x * __builtin_amdgcn_rcpf(1.0f + e);
}
DI unsigned gelu2(float a0, float a1, float c0, float c1, float nmean, float ga, float gb, float irs) {
  const f32x2 a = {a0, a1}, c = {c0, c1}, nm = {nmean, nmean}, ga2 = {ga, ga}, gb2 = {gb, gb}, ir = {irs, irs};
  const f32x2 t = __builtin_elementwise_fma(nm, c, a);
  const f32x2 z = t * __builtin_elementwise_fma(t * t, gb2, ga2);
  f32x2 e; e[0] = __builtin_amdgcn_exp2f(z[0]); e[1] = __builtin_amdgcn_exp2f(z[1]);
  const f32x2 d = __builtin_elementwise_fma(e, ir, ir);
  f32x2 rr; rr[0] = __builtin_amdgcn_rcpf(d[0]); rr[1] = __builtin_amdgcn_rcpf(d[1]);
  const f32x2 y = t * rr;
  return __builtin_bit_cast(unsigned, __builtin_convertvector(y, bf16x2));
}
DI float other_half(float v, int hh) {
  unsigned u = __builtin_bit_cast(unsigned, v);
  auto r = __builtin_amdgcn_permlane32_swap(u, u, false, false);
  return __builtin_bit_cast(float, hh ? r[0] : r[1]);
}
DI void sq2(unsigned v, float& a, float& b) {
  const float lo = __builtin_bit_cast(float, v << 16), hi = __builtin_bit_cast(float, v & 0xffff0000u);
  a = __builtin_fmaf(lo, lo, a); b = __builtin_fmaf(hi, hi, b);
}
#define STAMP(i)
#define STAMP_DECL(base)
#define STAMP_FLUSH(base, n)
DI f32x16 zero16() { f32x16 z; for (int i = 0; i < 16; i++) z[i] = 0.f; return z; }

DI void dma16_masked(unsigned voff, const void* sbase, unsigned ldsdst, uint64_t mask) {
  unsigned keep;
  asm volatile("s_mov_b32 %0, m0\n\ts_mov_b32 m0, %3\n\ts_mov_b64 exec, %4\n\tglobal_load_lds_dwordx4 %1, %2\n\ts_mov_b64 exec, -1\n\ts_mov_b32 m0, %0"
               : "=&s"(keep) : "v"(voff), "s"(sbase), "s"(ldsdst), "s"(mask) : "memory");
}
DI void dma16(unsigned voff, const void* sbase, unsigned ldsdst) {
  unsigned keep;
  asm volatile("s_mov_b32 %0, m0\n\ts_mov_b32 m0, %3\n\ts_nop 0\n\tglobal_load_lds_dwordx4 %1, %2\n\ts_mov_b32 m0, %0"
               : "=&s"(keep) : "v"(voff), "s"(sbase), "s"(ldsdst) : "memory");
}

constexpr int K1_SLOT    = 32768;
constexpr int K1_AUX_OFF = 151040;
constexpr int K1_CS_OFF  = K1_AUX_OFF + 3584;
constexpr int K1_SQ_OFF  = K1_CS_OFF + 2048;
constexpr int K1_LDS     = K1_SQ_OFF + 1024;

DI u32x4 tr_frag2(const LAS char* p0, const LAS char* p1) {
  s16x4 lo = __builtin_amdgcn_ds_read_tr16_b64_v4i16((LAS s16x4*)p0);
  s16x4 hi = __builtin_amdgcn_ds_read_tr16_b64_v4i16((LAS s16x4*)p1);
  u32x2 a = __builtin_bit_cast(u32x2, lo), b = __builtin_bit_cast(u32x2, hi);
  u32x4 r; r[0] = a[0]; r[1] = a[1]; r[2] = b[0]; r[3] = b[1];
  return r;
}
DI unsigned k1_tr_off(int lane, int t) {
  const int hh = lane >> 5, blk = (lane >> 4) & 1, q = (lane & 15) >> 2, p = lane & 3;
  int u = 7 * t + 4 * blk + p;
  if (blk == 1 && p == 3) u = 7 * t;
  return (8 * hh + q) * 256 + (((u >> 1) ^ (q << 2)) * 16) + (u & 1) * 8;
}

constexpr int K2_B_OFF   = 0;
constexpr int K2_A_OFF   = 114688;
constexpr int K2_CSP_OFF = K2_A_OFF + 32768;
constexpr int K2_CS_OFF  = K2_CSP_OFF + 2048;
constexpr int K2_M_OFF   = K2_CS_OFF + 512;
constexpr int K2_SY_OFF  = K2_M_OFF + 512;
constexpr int K2_END     = K2_SY_OFF + 64;
static_assert(K2_END <= K1_AUX_OFF, "lds map");
constexpr int K2_LDS     = K1_LDS;
constexpr unsigned SPIN_CAP = 1u << 20;
#define POLL_SLEEP 4
#define PH 2
#define PG3 2
#define PRIO_G3 __builtin_amdgcn_s_setprio(PG3)
#define PRIO_HI __builtin_amdgcn_s_setprio(PH)
#define PRIO_LO __builtin_amdgcn_s_setprio(PL)
constexpr size_t WS_FX1 = WS_CNT, WS_FH3 = WS_CNT + 4096, WS_FO = WS_CNT + 8192, WS_FCS = WS_CNT + 12288;
constexpr size_t WS_EPOCH = WS_CNT + 12288 + 1024;
constexpr size_t WS_GX1 = 589824, WS_GH3 = 622592, WS_GO = 655360;
constexpr size_t WS_F1 = 524288;
constexpr size_t WS_CS3 = 24576;

DI void gsync(LAS unsigned* f, int w, unsigned epoch) {
  asm volatile("s_waitcnt lgkmcnt(0)" ::: "memory");
  __hip_atomic_store(f + w, epoch, __ATOMIC_RELAXED, __HIP_MEMORY_SCOPE_WORKGROUP);
  for (unsigned s = 0; s < SPIN_CAP; s++) {
    u32x4 v;
    asm volatile("ds_read_b128 %0, %1\n\ts_waitcnt lgkmcnt(0)" : "=v"(v) : "v"((unsigned)(uintptr_t)f) : "memory");
    const unsigned a = v[0], b = v[1], c = v[2], d = v[3];
    const unsigned m = min(min(a, b), min(c, d));
    if ((unsigned)__builtin_amdgcn_readfirstlane(m) >= epoch) break;
    __builtin_amdgcn_s_sleep(1);
  }
}
DI void flag_set_t(char* p, int lane, unsigned tag) {
  if (lane == 0) __hip_atomic_store((GAS unsigned*)p, tag, __ATOMIC_RELAXED, __HIP_MEMORY_SCOPE_AGENT);
}
#define flag_set(p, lane) flag_set_t(p, lane, tag)
#define flags_wait(p) flags_wait_t(p, tag)
DI char* f1_flag(char* ws, int type, int sub, int tile) { return ws + WS_F1 + ((type * 4 + sub) * 16 + (tile >> 2)) * 128; }
DI void block_publish(LAS unsigned* cnt, char* p, int lane, unsigned tag, unsigned last = 7u) {
  if (lane == 0) {
    const unsigned old = __hip_atomic_fetch_add(cnt, 1u, __ATOMIC_RELAXED, __HIP_MEMORY_SCOPE_WORKGROUP);
    if (old == last) __hip_atomic_store((GAS unsigned*)p, tag, __ATOMIC_RELAXED, __HIP_MEMORY_SCOPE_AGENT);
  }
}
DI void flags_wait_t(const char* p, unsigned tag) {
  for (unsigned s = 0; s < SPIN_CAP; s++) {
    const unsigned v = __hip_atomic_load((const GAS unsigned*)p, __ATOMIC_RELAXED, __HIP_MEMORY_SCOPE_AGENT);
    if (__all(v == tag)) break;
    __builtin_amdgcn_s_sleep(POLL_SLEEP);
  }
  asm volatile("" ::: "memory");
}
DI u32x4 mkrsrc(const void* p, unsigned bytes) {
  const uint64_t a = (uint64_t)(uintptr_t)p;
  u32x4 r; r[0] = (unsigned)a; r[1] = (unsigned)(a >> 32) & 0xffffu; r[2] = bytes; r[3] = 0x00020000u;
  return r;
}
#define BLOAD(dst, voff, rs, soff, imm) \
  asm volatile("buffer_load_dwordx4 %0, %1, %2, %3 offen offset:%4" : "=v"(dst) : "v"(voff), "s"(rs), "s"(soff), "i"(imm) : "memory")
#define BLOAD_SC1(dst, voff, rs, soff, imm) \
  asm volatile("buffer_load_dwordx4 %0, %1, %2, %3 offen offset:%4 sc1" : "=v"(dst) : "v"(voff), "s"(rs), "s"(soff), "i"(imm) : "memory")
DI void st16_sc1(u32x4 v, __amdgpu_buffer_rsrc_t rs, unsigned off) { __builtin_amdgcn_raw_buffer_store_b128(v, rs, off, 0, 16); }
DI void st8_sc1(u32x2 v, __amdgpu_buffer_rsrc_t rs, unsigned off) { __builtin_amdgcn_raw_buffer_store_b64(v, rs, off, 0, 16); }

DI void tile_put(LAS char* p, const f32x16& t, int lane) {
#pragma unroll
  for (int g = 0; g < 4; g++) { f32x4 v; v[0] = t[4 * g]; v[1] = t[4 * g + 1]; v[2] = t[4 * g + 2]; v[3] = t[4 * g + 3]; *(LAS f32x4*)(p + g * 1024 + lane * 16) = v; }
}
DI void tile_add(const LAS char* p, f32x16& t, int lane) {
#pragma unroll
  for (int g = 0; g < 4; g++) { f32x4 v = *(const LAS f32x4*)(p + g * 1024 + lane * 16); t[4 * g] += v[0]; t[4 * g + 1] += v[1]; t[4 * g + 2] += v[2]; t[4 * g + 3] += v[3]; }
}
DI void reduce_scatter(LAS char* buf, f32x16 (&t)[4], int w, int lane, int x0, int x1, LAS unsigned* f, unsigned& epoch) {
  tile_put(buf + (((w ^ x0) * 2 + 0) * 4096), t[2], lane);
  tile_put(buf + (((w ^ x1) * 2 + 1) * 4096), t[3], lane);
  gsync(f, w, ++epoch);
  tile_add(buf + (((w ^ 2 ^ x0) * 2 + 0) * 4096), t[0], lane);
  tile_add(buf + (((w ^ 2 ^ x1) * 2 + 1) * 4096), t[1], lane);
  asm volatile("s_waitcnt lgkmcnt(0)" ::: "memory");
  tile_put(buf + (((w ^ 2 ^ x0) * 2 + 0) * 4096), t[1], lane);
  gsync(f, w, ++epoch);
  tile_add(buf + (((w ^ 3 ^ x0) * 2 + 0) * 4096), t[0], lane);
  asm volatile("s_waitcnt lgkmcnt(0)" ::: "memory");
}

DI unsigned w64_dma_voff(int lane, unsigned ldw) { const int rin = lane >> 3, pc = lane & 7; return rin * ldw + ((pc ^ (((rin >> 1) & 1) << 2)) * 16); }
DI unsigned w64_tr_off(int lane, int nt) {
  const int hh = lane >> 5, blk = (lane >> 4) & 1, q = (lane & 15) >> 2, p = lane & 3;
  return (8 * hh + q) * 128 + ((nt ^ ((q >> 1) & 1)) * 64) + blk * 32 + p * 8;
}

#define ATT_EXP4(SC, B, P, D)                                                                                    \
    SC[B] = __builtin_amdgcn_exp2f(SC[B]); SC[B + 1] = __builtin_amdgcn_exp2f(SC[B + 1]);                       \
    SC[B + 2] = __builtin_amdgcn_exp2f(SC[B + 2]); SC[B + 3] = __builtin_amdgcn_exp2f(SC[B + 3]);               \
    P[D] = pack2(SC[B], SC[B + 1]); P[D + 1] = pack2(SC[B + 2], SC[B + 3]);                                     \
    __builtin_amdgcn_sched_barrier(0);
#define ATT_STEP(SC, SN, PC0, PC1, PP0, PP1, KN0, KN1, QN0, QN1, VP0, VP1, OP)                                  \
  {                                                                                                             \
    SN = mfma(KN0, QN0, zero16());                                                                              \
    __builtin_amdgcn_sched_barrier(0);                                                                          \
    ATT_EXP4(SC, 0, PC0, 0)                                                                                     \
    SN = mfma(KN1, QN1, SN);                                                                                    \
    __builtin_amdgcn_sched_barrier(0);                                                                          \
    ATT_EXP4(SC, 4, PC0, 2)                                                                                     \
    OP = mfma(VP0, PP0, OP);                                                                                    \
    __builtin_amdgcn_sched_barrier(0);                                                                          \
    ATT_EXP4(SC, 8, PC1, 0)                                                                                     \
    OP = mfma(VP1, PP1, OP);                                                                                    \
    __builtin_amdgcn_sched_barrier(0);                                                                          \
    ATT_EXP4(SC, 12, PC1, 2)                                                                                    \
  }

DI float max16(const f32x16& s) {
  float a = fmaxf(fmaxf(s[0], s[1]), s[2]), b = fmaxf(fmaxf(s[3], s[4]), s[5]), c = fmaxf(fmaxf(s[6], s[7]), s[8]);
  float d = fmaxf(fmaxf(s[9], s[10]), s[11]), e = fmaxf(fmaxf(s[12], s[13]), s[14]);
  return fmaxf(fmaxf(fmaxf(a, b), c), fmaxf(fmaxf(d, e), s[15]));
}

template <int KIND>
DI void phase1(LAS char* lds, const char* __restrict__ x, const char* __restrict__ wsrc, const int ldw, const char* __restrict__ gate_w,
               char* __restrict__ ws, const int type, const int sub, const int tg, const int wv, const int stbase_, const char* touch) {
  const unsigned lds0 = (unsigned)(uintptr_t)lds;
  const int tid = threadIdx.x, lane = tid & 63, w = wv & 3, np = wv >> 2;
  const int r = lane & 31, hh = lane >> 5;
  const int tile = tg * 4 + w;
  const int drow = lane >> 4, dlc = (lane & 15) ^ (drow << 2);
  const unsigned dvoff = drow * ldw + (dlc < 14 ? dlc : 0) * 16;
  const uint64_t dmask = ~((3ull << 14) | (3ull << (16 + 10)) | (3ull << (32 + 6)) | (3ull << (48 + 2)));
  unsigned xv[2];
  {
    const int rin = lane >> 3, pc = lane & 7;
#pragma unroll
    for (int e = 0; e < 2; e++) xv[e] = rin * 896 + ((pc ^ ((4 * e + (rin >> 1)) & 7)) * 16);
  }
  const char* xbase = x + (size_t)(tg * 128 + w * 32 + np * 16) * 896;
  const char* wbase = wsrc + (size_t)(8 * wv) * ldw;
  auto issue_half = [&](int st, int jj) {
    const unsigned slot = lds0 + (st & 3) * K1_SLOT;
    dma16_masked(dvoff, wbase + (size_t)(64 * st + 4 * jj) * ldw, slot + (8 * wv + 4 * jj) * 256, dmask);
    dma16(xv[jj], xbase + jj * (8 * 896) + st * 128, slot + 16384 + w * 4096 + (2 * np + jj) * 1024);
  };
  unsigned gw0 = 0;
  if (KIND == 2) {
    const unsigned go = (tid < 448 ? tid : 447) * 2;
    asm volatile("global_load_ushort %0, %1, %2" : "=v"(gw0) : "v"(go), "s"(gate_w) : "memory");
  }
  issue_half(0, 0); issue_half(0, 1); issue_half(1, 0); issue_half(1, 1);
  unsigned xb[4];
#pragma unroll
  for (int ks = 0; ks < 4; ks++) xb[ks] = 16384 + w * 4096 + r * 128 + (((2 * ks + hh) ^ ((r >> 1) & 7)) * 16);
  const unsigned wb0 = k1_tr_off(lane, 2 * np), wb1 = k1_tr_off(lane, 2 * np + 1);
  const bool isaux = ((lane >> 4) & 1) == 1 && (lane & 3) == 3;
  const unsigned ab = isaux ? K1_AUX_OFF + (8 * hh + ((lane & 15) >> 2)) * 8 : wb1;
  const unsigned kstep = isaux ? 128u : 4096u, hoff = isaux ? 32u : 1024u;
  const unsigned sstep = isaux ? 512u : (unsigned)K1_SLOT, wrapst = isaux ? 512u : (unsigned)(-3 * K1_SLOT);
  unsigned a3[4];
#pragma unroll
  for (int ks = 0; ks < 4; ks++) a3[ks] = ab + ks * kstep;
  f32x16 acc[2], cs[2]; acc[0] = zero16(); acc[1] = zero16(); cs[0] = zero16(); cs[1] = zero16();
  u32x4 ones; ones[0] = ones[1] = ones[2] = ones[3] = 0x3F803F80u;
  u32x4 wf[2][4][2], xf[2][4], cf[2][2];
  float sqa[4] = {0.f, 0.f, 0.f, 0.f};
  unsigned sink;
  auto read_stage = [&](int st) {
    const int so = (st & 3) * K1_SLOT;
#pragma unroll
    for (int ks = 0; ks < 4; ks++) {
      xf[st & 1][ks] = *(const LAS u32x4*)(lds + xb[ks] + so);
      wf[st & 1][ks][0] = tr_frag(lds + wb0 + so + ks * 4096, 1024);
      if (KIND == 2) { wf[st & 1][ks][1] = tr_frag2(lds + a3[ks], lds + a3[ks] + hoff); a3[ks] += (st == 3) ? wrapst : sstep; }
      else wf[st & 1][ks][1] = tr_frag(lds + wb1 + so + ks * 4096, 1024);
    }
    if (KIND == 2) { cf[st & 1][0] = tr_frag(lds + wb0 + so + w * 4096, 1024); cf[st & 1][1] = tr_frag(lds + wb1 + so + w * 4096, 1024); }
  };
  asm volatile("s_waitcnt vmcnt(4)" : "+v"(gw0) :: "memory");
  if (KIND == 2) {
    u32x2 a; a[0] = 0x3F80u | (gw0 << 16); a[1] = 0u;
    *(LAS u32x2*)(lds + K1_AUX_OFF + (tid < 448 ? tid : 447) * 8) = a;
  }
  asm volatile("s_waitcnt lgkmcnt(0)\n\ts_barrier" ::: "memory");
  read_stage(0);
  issue_half(2, 0); issue_half(2, 1); issue_half(3, 0); issue_half(3, 1);
#pragma unroll
  for (int st = 0; st < 7; st++) {
    __builtin_amdgcn_sched_barrier(0);
    if (st < 3)       asm volatile("s_waitcnt vmcnt(8) lgkmcnt(0)\n\ts_barrier" ::: "memory");
    else if (st == 3) asm volatile("s_waitcnt vmcnt(9) lgkmcnt(0)\n\ts_barrier" ::: "memory");
    else if (st == 4) asm volatile("s_waitcnt vmcnt(5) lgkmcnt(0)\n\ts_barrier" ::: "memory");
    else if (st == 5) asm volatile("s_waitcnt vmcnt(1) lgkmcnt(0)\n\ts_barrier" ::: "memory");
    else              asm volatile("s_waitcnt lgkmcnt(0)\n\ts_barrier" ::: "memory");
    if (st < 6) read_stage(st + 1);
    __builtin_amdgcn_sched_barrier(0);
#pragma unroll
    for (int ks = 0; ks < 4; ks++) {
      acc[0] = KIND == 1 ? mfma(xf[st & 1][ks], wf[st & 1][ks][0], acc[0]) : mfma(wf[st & 1][ks][0], xf[st & 1][ks], acc[0]);
      if (KIND == 2) sq2(np ? xf[st & 1][ks][2] : xf[st & 1][ks][0], sqa[0], sqa[1]);
      acc[1] = KIND == 1 ? mfma(xf[st & 1][ks], wf[st & 1][ks][1], acc[1]) : mfma(wf[st & 1][ks][1], xf[st & 1][ks], acc[1]);
      if (KIND == 2) sq2(np ? xf[st & 1][ks][3] : xf[st & 1][ks][1], sqa[2], sqa[3]);
      if (st + 4 < 7 && (ks == 0 || ks == 2)) issue_half(st + 4, ks >> 1);
      if (st == TOUCH_ST && ks == 3) asm volatile("global_load_dword %0, %1, off" : "=v"(sink) : "v"(touch) : "memory");
    }
    if (KIND == 2) {
      cs[0] = mfma(ones, cf[st & 1][0], cs[0]); cs[1] = mfma(ones, cf[st & 1][1], cs[1]);
      asm volatile("" : "+v"(sqa[0]), "+v"(sqa[1]), "+v"(sqa[2]), "+v"(sqa[3]));
    }
  }
  __builtin_amdgcn_sched_barrier(0);
  asm volatile("s_waitcnt vmcnt(0)" : "+v"(sink) :: "memory");
  if (KIND < 2) {
    const float sc = type == 0 ? 0.27264314f : 1.0f;
    const __amdgpu_buffer_rsrc_t rq = __builtin_amdgcn_make_buffer_rsrc(ws + WS_Q, 0, 6u << 20, 0x00020000);
#pragma unroll
    for (int j = 0; j < 2; j++) {
      const int head = sub * 4 + 2 * np + j;
      u32x4 f0, f1;
#pragma unroll
      for (int d = 0; d < 4; d++) {
        f0[d] = pack2(acc[j][2 * d] * sc, acc[j][2 * d + 1] * sc);
        f1[d] = pack2(acc[j][8 + 2 * d] * sc, acc[j][8 + 2 * d + 1] * sc);
      }
      if (KIND == 1) {
        const unsigned v = r == 28 ? 0x3F803F80u : 0u;
#pragma unroll
        for (int d = 0; d < 4; d++) { f0[d] = r >= 28 ? v : f0[d]; f1[d] = r >= 28 ? v : f1[d]; }
      } else {
        f1[2] = hh ? (type == 1 ? 0x3F803F80u : 0u) : f1[2];
        f1[3] = hh ? (type == 1 ? 0x00003F80u : 0u) : f1[3];
      }
      const unsigned o = (unsigned)type * (2u << 20) + (unsigned)(head * 64 + tile) * 2048 + lane * 16;
      st16_sc1(f0, rq, o); st16_sc1(f1, rq, o + 1024);
    }
  } else {
    float sq = (sqa[0] + sqa[1]) + (sqa[2] + sqa[3]);
    sq += other_half(sq, hh);
    if (hh == 0) {
      *(LAS float*)(lds + K1_SQ_OFF + ((np * 4 + w) * 32 + r) * 4) = sq;
      *(LAS float*)(lds + K1_CS_OFF + (w * 128 + (2 * np) * 32 + r) * 4) = cs[0][0];
      *(LAS float*)(lds + K1_CS_OFF + (w * 128 + (2 * np + 1) * 32 + r) * 4) = cs[1][0];
    }
    if (sub == 0 && np == 0 && hh == 1) {
      const float gt = __builtin_amdgcn_rcpf(1.0f + __builtin_amdgcn_exp2f(-1.442695041f * acc[1][13]));
      __hip_atomic_store((GAS float*)(ws + WS_GATE) + tile * 32 + r, gt, __ATOMIC_RELAXED, __HIP_MEMORY_SCOPE_AGENT);
    }
    float sx = acc[1][12];
    { float o = other_half(sx, hh); sx = hh ? sx : o; }
    const float mean = sx * (1.0f / 448.0f);
    asm volatile("s_waitcnt lgkmcnt(0)\n\ts_barrier" ::: "memory");
    sq += *(LAS float*)(lds + K1_SQ_OFF + (((np ^ 1) * 4 + w) * 32 + r) * 4);
    const float var = fmaxf(sq * (1.0f / 448.0f) - mean * mean, 0.f) + 1e-5f;
    const float rstd = __builtin_amdgcn_rsqf(var);
    const float ga = -2.302208198f * rstd, gb = -0.1029432397f * rstd * rstd * rstd, irs = var * rstd;
    const __amdgpu_buffer_rsrc_t rh = __builtin_amdgcn_make_buffer_rsrc(ws + WS_H, 0, TBYTES, 0x00020000);
    unsigned pa[2][4][2];
#pragma unroll
    for (int j = 0; j < 2; j++) {
      const int t = 2 * np + j;
#pragma unroll
      for (int g = 0; g < 4; g++) {
        const LAS char* cp = lds + K1_CS_OFF + (t * 32 + 8 * g + 4 * hh) * 4;
        const f32x4 c = (*(const LAS f32x4*)cp + *(const LAS f32x4*)(cp + 512)) + (*(const LAS f32x4*)(cp + 1024) + *(const LAS f32x4*)(cp + 1536));
        pa[j][g][0] = gelu2(acc[j][4 * g], acc[j][4 * g + 1], c[0], c[1], -mean, ga, gb, irs);
        pa[j][g][1] = gelu2(acc[j][4 * g + 2], acc[j][4 * g + 3], c[2], c[3], -mean, ga, gb, irs);
      }
    }
    const unsigned dst = (unsigned)(tile * 56 + 14 * sub + 7 * np) * 512 + r * 16;
    u32x4 v0, v1, v2, v3;
#pragma unroll
    for (int d = 0; d < 2; d++) {
      auto s0 = __builtin_amdgcn_permlane32_swap(pa[0][0][d], pa[0][1][d], false, false);
      v0[d] = s0[0]; v0[2 + d] = s0[1];
      auto s1 = __builtin_amdgcn_permlane32_swap(pa[0][3][d], pa[0][2][d], false, false);
      v1[d] = s1[0]; v1[2 + d] = hh ? pa[0][2][d] : pa[1][0][d];
      auto s2 = __builtin_amdgcn_permlane32_swap(pa[1][0][d], pa[1][2][d], false, false);
      v2[d] = hh ? pa[1][1][d] : s2[1]; v2[2 + d] = hh ? s2[0] : pa[1][1][d];
      auto s3 = __builtin_amdgcn_permlane32_swap(pa[1][2][d], pa[1][2][d], false, false);
      v3[d] = s3[1]; v3[2 + d] = pa[1][3][d];
    }
    st16_sc1(v0, rh, dst + hh * 512);
    st16_sc1(v1, rh, dst + (hh ? 2 : 3) * 512);
    st16_sc1(v2, rh, dst + (4 + hh) * 512);
    st16_sc1(v3, rh, hh ? 0x80000000u : dst + 6 * 512);
  }
}

__global__ __launch_bounds__(512, 2) void fused_kernel(const char* __restrict__ x, const char* __restrict__ wqkv,
                                                       const char* __restrict__ w1, const char* __restrict__ gate_w,
                                                       const char* __restrict__ wo,
                                                       const char* __restrict__ w2, const char* __restrict__ w3,
                                                       const char* __restrict__ w4, char* __restrict__ ws,
                                                       char* __restrict__ out) {
  extern __shared__ __attribute__((aligned(16))) char smem[];
  LAS char* lds = (LAS char*)smem;
  const unsigned lds0 = (unsigned)(uintptr_t)lds;
  LAS unsigned* sy = (LAS unsigned*)(lds + K2_SY_OFF);
  const int tid = threadIdx.x, lane = tid & 63;
  const int wv = __builtin_amdgcn_readfirstlane(tid >> 6);
  const int w = wv & 3;
  const int b = blockIdx.x, xcd = b & 7, bi = b >> 3;
  const int r = lane & 31, hh = lane >> 5;
  const bool has_gemm = bi < 28;
  const int nb = bi % 7, tb64 = xcd * 4 + bi / 7;
  const int ni = w & 1, tj = w >> 1;
  const int p1type = bi < 24 ? bi % 3 : 3, p1tg = (xcd >> 2) * 8 + (bi < 24 ? bi / 3 : bi - 24);
  STAMP_DECL(256 + blockIdx.x) STAMP(0);
  if (tid < 16) sy[tid] = 0u;
  {
    const int type = p1type, sub = xcd & 3, tg = p1tg;
    const int sb_ = 0;
    int k = (b * 8 + wv) * 16 + (lane & 15); k = k < 31360 ? k : 31359;
    const char* touch = k < 3136 ? w2 + (size_t)k * 128 : k < 15680 ? w3 + (size_t)(k - 3136) * 128 : k < 28224 ? w4 + (size_t)(k - 15680) * 128 : wo + (size_t)(k - 28224) * 128;
    {
      uint64_t pc;
      asm volatile("s_getpc_b64 %0" : "=s"(pc));
      const int i = (bi * 8 + wv) * 2 + (lane & 1);
      if (lane >= 32) touch = (const char*)(uintptr_t)((pc & ~127ull) + (uint64_t)i * 128);
    }
    if (type < 2)       phase1<0>(lds, x, wqkv + (type * 448 + sub * 112) * 2, 2688, gate_w, ws, type, sub, tg, wv, sb_, touch);
    else if (type == 2) phase1<1>(lds, x, wqkv + (2 * 448 + sub * 112) * 2, 2688, gate_w, ws, type, sub, tg, wv, sb_, touch);
    else                phase1<2>(lds, x, w1 + sub * 224, 896, gate_w, ws, type, sub, tg, wv, sb_, touch);
  }
#define EPOCH_LOAD unsigned epoch; asm volatile("global_load_dword %0, %1, %2 sc1" : "=v"(epoch) : "v"(0u), "s"(ws + WS_EPOCH) : "memory")
#define EPOCH_WAIT(N) asm volatile("s_waitcnt vmcnt(" #N ")" : "+v"(epoch) :: "memory"); epoch = __builtin_amdgcn_readfirstlane(epoch); \
  const unsigned tag = (epoch * 0x9E3779B1u) ^ 0x5bd1e995u
  char* const my_f1 = f1_flag(ws, p1type, xcd & 3, p1tg * 4);
  STAMP(15);

  if (wv < 4) {
    const int head = 2 * xcd + (bi >> 4), qb = bi & 15;
    EPOCH_LOAD; EPOCH_WAIT(0);
    block_publish(sy + 12, my_f1, lane, tag);
    STAMP(13);
    const __amdgpu_buffer_rsrc_t rq = __builtin_amdgcn_make_buffer_rsrc(ws + WS_Q, 0, 6u << 20, 0x00020000);
    const unsigned qo = (unsigned)(head * 64 + qb * 4) * 2048 + lane * 16;
    const unsigned kho = (2u << 20) + (unsigned)(head * 64) * 2048 + lane * 16, vho = kho + (2u << 20);
    const unsigned ko = kho + w * 16 * 2048, vo = vho + w * 16 * 2048;
#define LDQ(off) __builtin_amdgcn_raw_buffer_load_b128(rq, (off), 0, 16)
    unsigned ea = 0;
    {
      const int hs = head >> 2;
      flags_wait(lane < 8 ? f1_flag(ws, 1 + (lane >> 2), hs, w * 16 + (lane & 3) * 4) : f1_flag(ws, 0, hs, qb * 4));
    }
    STAMP(14);
    __builtin_amdgcn_s_setprio(3);
    u32x4 q[4][2];
#pragma unroll
    for (int i = 0; i < 4; i++) { q[i][0] = LDQ(qo + (w ^ i) * 2048); q[i][1] = LDQ(qo + (w ^ i) * 2048 + 1024); }
    u32x4 kb[4][2], vb[4][2];
#pragma unroll
    for (int j = 0; j < 3; j++) { kb[j][0] = LDQ(ko + j * 2048); kb[j][1] = LDQ(ko + j * 2048 + 1024); vb[j][0] = LDQ(vo + j * 2048); vb[j][1] = LDQ(vo + j * 2048 + 1024); }
    f32x16 o[4];
    {
      LAS float* mx = (LAS float*)(lds + K2_M_OFF);
      f32x16 s = mfma(kb[0][0], q[0][0], zero16());
      s = mfma(kb[0][1], q[0][1], s);
      const float m = max16(s);
      if (hh) mx[w * 32 + r] = m;
      gsync(sy, w, ++ea);
#pragma unroll
      for (int i = 0; i < 4; i++) {
        const float mm = mx[(w ^ i) * 32 + r];
        q[i][1][2] = hh ? (pack2(-mm, 0.f)) : q[i][1][2];
      }
    }
    STAMP(1);
    __builtin_amdgcn_s_setprio(0);
    {
#pragma unroll
      for (int i = 0; i < 4; i++) o[i] = zero16();
      f32x16 sA, sB;
      u32x4 pA0, pA1, pB0, pB1;
      sA = mfma(kb[0][0], q[0][0], zero16());
      sA = mfma(kb[0][1], q[0][1], sA);
      pB0 = pB1 = (u32x4){0u, 0u, 0u, 0u};
      vb[3][0] = vb[3][1] = (u32x4){0u, 0u, 0u, 0u};
#pragma unroll 1
      for (int kq = 0; kq < 4; kq++) {
#pragma unroll
        for (int j = 0; j < 4; j++) {
          const int kt = kq * 4 + j;
          const int jp = (j + 3) & 3, jn = (j + 1) & 3;
          ATT_STEP(sA, sB, pA0, pA1, pB0, pB1, kb[j][0], kb[j][1], q[1][0], q[1][1], vb[jp][0], vb[jp][1], o[3]);
          {
            const int kn = kt + 3 < 16 ? kt + 3 : 15;
            __builtin_amdgcn_s_setprio(3);
            kb[jp][0] = LDQ(ko + kn * 2048); kb[jp][1] = LDQ(ko + kn * 2048 + 1024); vb[jp][0] = LDQ(vo + kn * 2048); vb[jp][1] = LDQ(vo + kn * 2048 + 1024);
            __builtin_amdgcn_sched_barrier(0);
            __builtin_amdgcn_s_setprio(0);
          }
          ATT_STEP(sB, sA, pB0, pB1, pA0, pA1, kb[j][0], kb[j][1], q[2][0], q[2][1], vb[j][0], vb[j][1], o[0]);
          ATT_STEP(sA, sB, pA0, pA1, pB0, pB1, kb[j][0], kb[j][1], q[3][0], q[3][1], vb[j][0], vb[j][1], o[1]);
          ATT_STEP(sB, sA, pB0, pB1, pA0, pA1, kb[jn][0], kb[jn][1], q[0][0], q[0][1], vb[j][0], vb[j][1], o[2]);
        }
      }
      o[3] = mfma(vb[3][0], pB0, o[3]);
      o[3] = mfma(vb[3][1], pB1, o[3]);
    }
    STAMP(2);
    __builtin_amdgcn_s_setprio(3);
    flags_wait(f1_flag(ws, 3, 0, qb * 4));
    const float gate_q = __hip_atomic_load((const GAS float*)(ws + WS_GATE) + (qb * 4 + w) * 32 + r, __ATOMIC_RELAXED, __HIP_MEMORY_SCOPE_AGENT);
    reduce_scatter(lds + K2_A_OFF, o, w, lane, 0, 0, sy, ea);
    float l = o[0][12];
    { float t = other_half(l, hh); l = hh ? l : t; }
    {
      float amax = 0.f;
#pragma unroll
      for (int i = 0; i < 12; i++) amax = fmaxf(amax, fabsf(o[0][i]));
      if (!hh) amax = fmaxf(fmaxf(amax, fabsf(o[0][12])), fmaxf(fabsf(o[0][13]), fmaxf(fabsf(o[0][14]), fabsf(o[0][15]))));
      const bool ok = (l > 1.0e-10f) && (l < 1.0e30f) && (amax < 1.0e37f);
      if (__any(!ok)) {
        flags_wait(f1_flag(ws, 1 + (lane >> 5), head >> 2, (lane & 15) * 4));
        u32x4 q0 = q[0][0], q1 = q[0][1];
        q1[2] = hh ? 0u : q1[2];
        float tm = -3.0e38f;
#pragma unroll 1
        for (int kt = 0; kt < 64; kt++) {
          f32x16 s = mfma(LDQ(kho + kt * 2048), q0, zero16());
          s = mfma(LDQ(kho + kt * 2048 + 1024), q1, s);
          tm = fmaxf(tm, max16(s));
        }
        tm = -fmaxf(tm, other_half(tm, hh));
        const unsigned h0 = pack2(tm, 0.f) & 0xffffu; const float f0 = __builtin_bit_cast(float, h0 << 16);
        const unsigned h1 = pack2(tm - f0, 0.f) & 0xffffu; const float f1 = __builtin_bit_cast(float, h1 << 16);
        const unsigned h2 = pack2((tm - f0) - f1, 0.f) & 0xffffu;
        q1[2] = hh ? (h0 | (h1 << 16)) : q1[2];
        q1[3] = hh ? h2 : q1[3];
        f32x16 oo = zero16();
#pragma unroll 1
        for (int kt = 0; kt < 64; kt++) {
          f32x16 s = mfma(LDQ(kho + kt * 2048), q0, zero16());
          s = mfma(LDQ(kho + kt * 2048 + 1024), q1, s);
          u32x4 p0, p1;
#pragma unroll
          for (int d = 0; d < 4; d++) {
            p0[d] = pack2(__builtin_amdgcn_exp2f(s[2 * d]), __builtin_amdgcn_exp2f(s[2 * d + 1]));
            p1[d] = pack2(__builtin_amdgcn_exp2f(s[8 + 2 * d]), __builtin_amdgcn_exp2f(s[8 + 2 * d + 1]));
          }
          oo = mfma(LDQ(vho + kt * 2048), p0, oo);
          oo = mfma(LDQ(vho + kt * 2048 + 1024), p1, oo);
        }
        o[0] = oo;
        l = oo[12];
        { float t = other_half(l, hh); l = hh ? l : t; }
      }
    }
    {
      const __amdgpu_buffer_rsrc_t ro = __builtin_amdgcn_make_buffer_rsrc(ws + WS_O, 0, TBYTES, 0x00020000);
      const float inv = gate_q * __builtin_amdgcn_rcpf(l);
      const unsigned base = (unsigned)(qb * 4 + w) * (56 * 512) + r * 16;
#pragma unroll
      for (int g = 0; g < 4; g++) {
        u32x2 v; v[0] = pack2(o[0][4 * g] * inv, o[0][4 * g + 1] * inv); v[1] = pack2(o[0][4 * g + 2] * inv, o[0][4 * g + 3] * inv);
        const int u = 7 * head + 2 * g + hh;
        if (g < 3 || hh == 0) st8_sc1(v, ro, base + (u >> 1) * 512 + (u & 1) * 8);
      }
    }
    asm volatile("s_waitcnt vmcnt(0)" ::: "memory");
    block_publish(sy + 13, ws + WS_GO + (qb * 16 + head) * 128, lane, tag, 3u);
    STAMP(3);
    return;
  }

  if (!has_gemm) {
    EPOCH_LOAD; EPOCH_WAIT(0);
    block_publish(sy + 12, my_f1, lane, tag);
    if (b == 224 && w == 3) {
#pragma unroll 1
      for (int i = 0; i < 4; i++) flags_wait(ws + WS_F1 + (i * 64 + lane) * 128);
      if (lane == 0) __hip_atomic_store((GAS unsigned*)(ws + WS_EPOCH), epoch + 1u, __ATOMIC_RELAXED, __HIP_MEMORY_SCOPE_AGENT);
    }
    const int id = (xcd * 4 + bi - 28) * 2 + w;
    if (w < 2 && id < 56) {
      const unsigned reg = K2_B_OFF + w * 28672;
      const unsigned dv = (lane >> 2) * 3584 + (lane & 3) * 16;
      const char* src = w3 + id * 64;
#pragma unroll
      for (int j = 0; j < 28; j++) dma16(dv, src + (size_t)j * (16 * 3584), lds0 + reg + j * 1024);
      asm volatile("s_waitcnt vmcnt(0)" ::: "memory");
      const unsigned to = reg + (8 * hh + ((lane & 15) >> 2)) * 64 + ((lane >> 4) & 1) * 32 + (lane & 3) * 8;
      u32x4 ones; ones[0] = ones[1] = ones[2] = ones[3] = 0x3F803F80u;
      f32x16 cs = zero16();
#pragma unroll
      for (int s = 0; s < 28; s++) cs = mfma(ones, tr_frag(lds + to + s * 1024, 256), cs);
      if (hh == 0) __hip_atomic_store((GAS float*)(ws + WS_CS3) + id * 32 + r, cs[0], __ATOMIC_RELAXED, __HIP_MEMORY_SCOPE_AGENT);
      asm volatile("s_waitcnt vmcnt(0)" ::: "memory");
      flag_set(ws + WS_FCS + id * 4, lane);
    }
    return;
  }
  PRIO_LO;
  unsigned eb = 0;
  LAS unsigned* syb = sy + 4;
  const int nb3 = bi % 14, tb128 = xcd * 2 + bi / 14;
  const unsigned dv64 = w64_dma_voff(lane, 896);
  const unsigned t0 = w64_tr_off(lane, ni), t1 = w64_tr_off(lane, ni ^ 1);
  const unsigned dv3 = (lane >> 4) * 3584 + (((lane & 15) ^ ((lane >> 4) << 2)) * 16);
  const char* w3src = w3 + nb3 * 256;
  const unsigned ring = K2_B_OFF + w * 28672;
  const char* w4src = w4 + (size_t)(448 * w) * 896 + nb * 128;
  unsigned x1p[8];
  const unsigned sl = K2_B_OFF + 57344 + w * 14336;
  EPOCH_LOAD;
  EPOCH_WAIT(0);
  block_publish(sy + 12, my_f1, lane, tag);
  STAMP(14);
  {
    const char* wsrc = w2 + (size_t)(112 * w) * 896 + nb * 128;
#pragma unroll
    for (int j = 0; j < 14; j++) dma16(dv64, wsrc + j * (8 * 896), lds0 + sl + j * 1024);
  }
#pragma unroll
  for (int j = 0; j < 14; j++) { const int row0 = 56 * w + 4 * j; dma16(dv3, w3src + (size_t)row0 * 3584, lds0 + K2_B_OFF + row0 * 256); }
  {
    const u32x4 rh = mkrsrc(ws + WS_H, TBYTES);
    const int delta = w == 0 ? -1 : w == 1 ? 1 : w == 2 ? -2 : 2;
    u32x4 af[7][2];
    unsigned av[2];
#pragma unroll
    for (int e = 0; e < 2; e++) {
      const int tok = (tb64 * 2 + (tj ^ e)) * 32 + r + delta;
      av[e] = (tok >= 0 && tok < SEQ) ? (unsigned)(((tok >> 5) * 56 + 14 * w + hh) * 512 + (tok & 31) * 16) : 0x80000000u;
    }
    {
      int tl = tb64 * 2 - 1 + (lane & 3); tl = tl < 0 ? 0 : tl > 63 ? 63 : tl;
      flags_wait(f1_flag(ws, 3, w, tl));
    }
    asm volatile("s_waitcnt vmcnt(0)" ::: "memory");
    STAMP(10);
#pragma unroll
    for (int s = 0; s < 7; s++) {
      BLOAD_SC1(af[s][0], av[0], rh, (s >> 2) * 4096, (s & 3) * 1024);
      BLOAD_SC1(af[s][1], av[1], rh, (s >> 2) * 4096, (s & 3) * 1024);
    }
    u32x2 xr[4];
    {
      const char* xp = x + (size_t)((tb64 * 2 + tj) * 32) * 896 + (nb * 64 + ni * 32) * 2;
      const unsigned xo = r * 896 + hh * 8;
#pragma unroll
      for (int g = 0; g < 4; g++) asm volatile("global_load_dwordx2 %0, %1, %2 offset:%3" : "=v"(xr[g]) : "v"(xo), "s"(xp), "i"(g * 16) : "memory");
    }
    PRIO_HI;
    f32x16 t[4];
#pragma unroll
    for (int i = 0; i < 4; i++) t[i] = zero16();
    u32x4 f0[7], f1[7];
#pragma unroll
    for (int s = 0; s < 7; s++) { f0[s] = tr_frag(lds + sl + t0 + s * 2048, 512); f1[s] = tr_frag(lds + sl + t1 + s * 2048, 512); }
    STAMP(1);
#pragma unroll
    for (int s = 0; s < 7; s++) {
      asm volatile("s_waitcnt vmcnt(%2)" : "+v"(af[s][0]), "+v"(af[s][1]) : "i"(16 - 2 * s) : "memory");
      t[0] = mfma(f0[s], af[s][0], t[0]); t[1] = mfma(f1[s], af[s][0], t[1]);
      t[2] = mfma(f0[s], af[s][1], t[2]); t[3] = mfma(f1[s], af[s][1], t[3]);
      __builtin_amdgcn_sched_barrier(0);
    }
    STAMP(11);
    asm volatile("s_waitcnt vmcnt(0) lgkmcnt(0)" : "+v"(xr[0]), "+v"(xr[1]), "+v"(xr[2]), "+v"(xr[3]) :: "memory");
#pragma unroll
    for (int i = 1; i < 4; i++) tile_put(lds + sl + (i - 1) * 4096, t[i], lane);
    gsync(syb, w, ++eb);
#pragma unroll
    for (int i = 1; i < 4; i++) tile_add(lds + K2_B_OFF + 57344 + (w ^ i) * 14336 + (i - 1) * 4096, t[0], lane);
    float s1 = 0.f, s2 = 0.f;
#pragma unroll
    for (int g = 0; g < 4; g++) {
      const float a0 = __builtin_bit_cast(float, xr[g][0] << 16) + t[0][4 * g], a1 = __builtin_bit_cast(float, xr[g][0] & 0xffff0000u) + t[0][4 * g + 1];
      const float a2 = __builtin_bit_cast(float, xr[g][1] << 16) + t[0][4 * g + 2], a3 = __builtin_bit_cast(float, xr[g][1] & 0xffff0000u) + t[0][4 * g + 3];
      x1p[2 * g] = pack2(a0, a1); x1p[2 * g + 1] = pack2(a2, a3);
    }
    const __amdgpu_buffer_rsrc_t rx = __builtin_amdgcn_make_buffer_rsrc(ws + WS_X1, 0, TBYTES, 0x00020000);
    const int tile = tb64 * 2 + tj;
#pragma unroll
    for (int gp = 0; gp < 2; gp++) {
      auto a = __builtin_amdgcn_permlane32_swap(x1p[4 * gp], x1p[4 * gp + 2], false, false);
      auto c = __builtin_amdgcn_permlane32_swap(x1p[4 * gp + 1], x1p[4 * gp + 3], false, false);
      u32x4 v; v[0] = a[0]; v[1] = c[0]; v[2] = a[1]; v[3] = c[1];
      st16_sc1(v, rx, (unsigned)(tile * 56 + nb * 8 + ni * 4 + 2 * gp + hh) * 512 + r * 16);
    }
#pragma unroll
    for (int j = 0; j < 8; j++) {
      const float lo = __builtin_bit_cast(float, x1p[j] << 16), hi = __builtin_bit_cast(float, x1p[j] & 0xffff0000u);
      s1 += lo + hi; s2 = __builtin_fmaf(lo, lo, s2); s2 = __builtin_fmaf(hi, hi, s2);
    }
    s1 += other_half(s1, hh); s2 += other_half(s2, hh);
    const __amdgpu_buffer_rsrc_t rs = __builtin_amdgcn_make_buffer_rsrc(ws + WS_STATS, 0, 64 * 14 * 32 * 8, 0x00020000);
    { u32x2 v; v[0] = __builtin_bit_cast(unsigned, s1); v[1] = __builtin_bit_cast(unsigned, s2); st8_sc1(v, rs, hh ? 0x80000000u : (unsigned)((tile * 14 + nb * 2 + ni) * 32 + r) * 8); }
    STAMP(12);
    gsync(syb, w, ++eb);
#pragma unroll
    for (int j = 0; j < 14; j++) { const int row0 = 224 + 56 * w + 4 * j; dma16(dv3, w3src + (size_t)row0 * 3584, lds0 + K2_B_OFF + row0 * 256); }
    asm volatile("s_waitcnt vmcnt(14)" ::: "memory");
    block_publish(sy + 14, ws + WS_GX1 + (tb64 * 8 + nb) * 128, lane, tag, 3u);
    STAMP(2);
  }
  {
    asm volatile("s_waitcnt vmcnt(0)" ::: "memory");
    gsync(syb, w, ++eb);
    unsigned tb[4];
    {
      const int blk = (lane >> 4) & 1, q = (lane & 15) >> 2, p = lane & 3;
#pragma unroll
      for (int n = 0; n < 4; n++) tb[n] = K2_B_OFF + (8 * hh + q) * 256 + ((n ^ q) * 64) + blk * 32 + p * 8;
    }
    STAMP(3);
    const int tile = tb128 * 4 + w;
    flags_wait(lane < 16 ? ws + WS_GX1 + ((tile >> 1) * 8 + (lane < 7 ? lane : 0)) * 128 : lane < 32 ? ws + WS_FCS + (nb3 * 4 + (lane & 3)) * 4
               : f1_flag(ws, 3, 0, lane < 48 ? tile : tb64 * 2 + tj));
    STAMP(4);
    const u32x4 rx = mkrsrc(ws + WS_X1, TBYTES);
    const u32x4 rsd = mkrsrc(ws + WS_STATS, 64 * 14 * 32 * 8);
    const u32x4 rcd = mkrsrc(ws + WS_CS3, 1792 * 4);
    u32x2 sv[7], cv; float g3;
    {
      const unsigned so = (unsigned)((tile * 14 + hh * 7) * 32 + r) * 8, co = (unsigned)(nb3 * 128 + lane * 2) * 4;
#pragma unroll
      for (int j = 0; j < 7; j++) asm volatile("buffer_load_dwordx2 %0, %1, %2, 0 offen offset:%3 sc1" : "=v"(sv[j]) : "v"(so), "s"(rsd), "i"(j * 256) : "memory");
      asm volatile("buffer_load_dwordx2 %0, %1, %2, 0 offen sc1" : "=v"(cv) : "v"(co), "s"(rcd) : "memory");
      const unsigned go = (unsigned)(tile * 32 + r) * 4;
      asm volatile("global_load_dword %0, %1, %2 sc1" : "=v"(g3) : "v"(go), "s"(ws + WS_GATE) : "memory");
    }
    constexpr int AW = 16;
    u32x4 af[AW];
    const unsigned av = (unsigned)(tile * 56) * 512 + lane * 16;
#pragma unroll
    for (int s = 0; s < AW; s++) BLOAD_SC1(af[s], av, rx, (s >> 2) * 4096, (s & 3) * 1024);
    const __amdgpu_buffer_rsrc_t r3 = __builtin_amdgcn_make_buffer_rsrc(ws + WS_H3, 0, T3BYTES, 0x00020000);
    STAMP(13);
    PRIO_G3;
    u32x4 wfr[3][4];
#pragma unroll
    for (int k = 0; k < 2; k++)
#pragma unroll
      for (int n = 0; n < 4; n++) wfr[k][n] = tr_frag(lds + tb[n] + k * 4096, 1024);
    f32x16 acc[4];
#pragma unroll
    for (int n = 0; n < 4; n++) acc[n] = zero16();
#pragma unroll
    for (int s = 0; s < 28; s++) {
      asm volatile("s_waitcnt vmcnt(%1)" : "+v"(af[s % AW]) : "i"(s + AW <= 28 ? AW - 1 : 27 - s) : "memory");
#pragma unroll
      for (int n = 0; n < 4; n++) {
        acc[n] = mfma(wfr[s % 3][n], af[s % AW], acc[n]);
        if (s + 2 < 28) wfr[(s + 2) % 3][n] = tr_frag(lds + tb[n] + (s + 2) * 4096, 1024);
        __builtin_amdgcn_sched_barrier(0);
      }
      if (s + AW < 28) BLOAD_SC1(af[s % AW], av, rx, ((s + AW) >> 2) * 4096, ((s + AW) & 3) * 1024);
      __builtin_amdgcn_sched_barrier(0);
    }
    STAMP(8);
    PRIO_HI;
    asm volatile("" : "+v"(sv[0]), "+v"(sv[1]), "+v"(sv[2]), "+v"(sv[3]), "+v"(sv[4]), "+v"(sv[5]), "+v"(sv[6]), "+v"(cv), "+v"(g3));
    const unsigned csl = K2_CSP_OFF + w * 512;
    *(LAS u32x2*)(lds + csl + lane * 8) = cv;
    float nmean, ga, gb, irs;
    {
      float s1 = 0.f, s2 = 0.f;
#pragma unroll
      for (int j = 0; j < 7; j++) { const unsigned u0 = sv[j][0], u1 = sv[j][1]; s1 += __builtin_bit_cast(float, u0); s2 += __builtin_bit_cast(float, u1); }
      s1 += other_half(s1, hh); s2 += other_half(s2, hh);
      const float mean = s1 * (1.0f / 448.0f);
      const float var = fmaxf(s2 * (1.0f / 448.0f) - mean * mean, 0.f) + 1e-5f;
      const float rstd = __builtin_amdgcn_rsqf(var);
      nmean = -mean; ga = -2.302208198f * rstd; gb = -0.1029432397f * rstd * rstd * rstd;
      irs = var * rstd * __builtin_amdgcn_rcpf(fmaxf(1.0f - g3, 1.0e-30f));
    }
#pragma unroll
    for (int n = 0; n < 4; n++) {
#pragma unroll
      for (int gp = 0; gp < 2; gp++) {
        unsigned pk[4];
#pragma unroll
        for (int g2 = 0; g2 < 2; g2++) {
          const int g = 2 * gp + g2;
          const f32x4 c4 = *(const LAS f32x4*)(lds + csl + (n * 32 + 8 * g + 4 * hh) * 4);
          pk[2 * g2] = gelu2(acc[n][4 * g], acc[n][4 * g + 1], c4[0], c4[1], nmean, ga, gb, irs);
          pk[2 * g2 + 1] = gelu2(acc[n][4 * g + 2], acc[n][4 * g + 3], c4[2], c4[3], nmean, ga, gb, irs);
        }
        auto a = __builtin_amdgcn_permlane32_swap(pk[0], pk[2], false, false);
        auto c = __builtin_amdgcn_permlane32_swap(pk[1], pk[3], false, false);
        u32x4 v; v[0] = a[0]; v[1] = c[0]; v[2] = a[1]; v[3] = c[1];
        st16_sc1(v, r3, (unsigned)(tile * 224 + nb3 * 16 + n * 4 + 2 * gp + hh) * 512 + r * 16);
      }
    }
    PRIO_HI;
    STAMP(5);
    gsync(syb, w, ++eb);
    asm volatile("s_waitcnt vmcnt(0)" ::: "memory");
    block_publish(sy + 15, ws + WS_GH3 + (tb128 * 16 + nb3) * 128, lane, tag, 3u);
    PRIO_LO;
  }
  {
    const char* wsrc = w4src;
#pragma unroll
    for (int j = 0; j < 28; j++) dma16(dv64, wsrc + j * (8 * 896), lds0 + ring + j * 1024);
    const float gate = __hip_atomic_load((const GAS float*)(ws + WS_GATE) + (tb64 * 2 + tj) * 32 + r, __ATOMIC_RELAXED, __HIP_MEMORY_SCOPE_AGENT);
    {
      const int blk0 = (7 * w) >> 1;
      const int k = lane & 3;
      flags_wait(ws + WS_GH3 + ((tb64 >> 1) * 16 + blk0 + k) * 128);
    }
    asm volatile("s_waitcnt vmcnt(0)" ::: "memory");
    STAMP(6);
    const u32x4 r3 = mkrsrc(ws + WS_H3, T3BYTES);
    unsigned av[2];
#pragma unroll
    for (int e = 0; e < 2; e++) av[e] = (unsigned)((tb64 * 2 + (tj ^ e)) * 224 + 56 * w) * 512 + lane * 16;
    u32x4 af[14][2];
#pragma unroll
    for (int s = 0; s < 14; s++)
#pragma unroll
      for (int e = 0; e < 2; e++) BLOAD_SC1(af[s][e], av[e], r3, (s >> 2) * 4096, (s & 3) * 1024);
    f32x16 t[4];
#pragma unroll
    for (int i = 0; i < 4; i++) t[i] = zero16();
    u32x4 f0[2], f1[2];
    const char* wosrc = wo + (size_t)(112 * w) * 896 + nb * 128;
    f0[0] = tr_frag(lds + ring + t0, 512); f1[0] = tr_frag(lds + ring + t1, 512);
#pragma unroll
    for (int s = 0; s < 28; s++) {
      const int sl = s % 14;
      asm volatile("s_waitcnt lgkmcnt(0)" : "+v"(f0[s & 1]), "+v"(f1[s & 1]) :: "memory");
      if (s + 1 < 28) {
        const int nl = (s + 1) % 14;
        if (s == 13) asm volatile("s_waitcnt vmcnt(50)" ::: "memory");
        else if (s > 13) asm volatile("s_waitcnt vmcnt(%0)" :: "i"(2 + 4 * (13 - nl) + 2 * (s - 14 < 7 ? s - 14 : 7)) : "memory");
        f0[(s + 1) & 1] = tr_frag(lds + ring + t0 + nl * 2048, 512); f1[(s + 1) & 1] = tr_frag(lds + ring + t1 + nl * 2048, 512);
      }
      if (s < 14) {
        dma16(dv64, wsrc + (28 + 2 * s) * (8 * 896), lds0 + ring + (2 * s) * 1024);
        dma16(dv64, wsrc + (29 + 2 * s) * (8 * 896), lds0 + ring + (2 * s + 1) * 1024);
        asm volatile("s_waitcnt vmcnt(%2)" : "+v"(af[s][0]), "+v"(af[s][1]) : "i"(2 * (13 - s) + 4 * s + 2) : "memory");
      } else {
        if (s < 21) {
          dma16(dv64, wosrc + (2 * sl) * (8 * 896), lds0 + ring + (2 * sl) * 1024);
          dma16(dv64, wosrc + (2 * sl + 1) * (8 * 896), lds0 + ring + (2 * sl + 1) * 1024);
        }
        asm volatile("s_waitcnt vmcnt(%2)" : "+v"(af[sl][0]), "+v"(af[sl][1]) : "i"(4 * (13 - sl) + 2 * (s - 13 < 7 ? s - 13 : 7)) : "memory");
      }
      t[0] = mfma(f0[s & 1], af[sl][0], t[0]); t[1] = mfma(f1[s & 1], af[sl][0], t[1]);
      t[2] = mfma(f0[s & 1], af[sl][1], t[2]); t[3] = mfma(f1[s & 1], af[sl][1], t[3]);
      if (s < 14) {
        BLOAD_SC1(af[s][0], av[0], r3, ((s + 14) >> 2) * 4096, ((s + 14) & 3) * 1024);
        BLOAD_SC1(af[s][1], av[1], r3, ((s + 14) >> 2) * 4096, ((s + 14) & 3) * 1024);
      }
      __builtin_amdgcn_sched_barrier(0);
    }
    STAMP(7);
    PRIO_HI;
    flags_wait(ws + WS_GO + ((tb64 >> 1) * 16 + 4 * w + (lane & 3)) * 128);
    asm volatile("s_waitcnt vmcnt(0)" ::: "memory");
    {
      const u32x4 ro = mkrsrc(ws + WS_O, TBYTES);
      unsigned ov[2];
#pragma unroll
      for (int e = 0; e < 2; e++) ov[e] = (unsigned)((tb64 * 2 + (tj ^ e)) * 56 + 14 * w) * 512 + lane * 16;
#pragma unroll
      for (int s = 0; s < 7; s++)
#pragma unroll
        for (int e = 0; e < 2; e++) BLOAD_SC1(af[s][e], ov[e], ro, (s >> 2) * 4096, (s & 3) * 1024);
      u32x4 g0[7], g1[7];
#pragma unroll
      for (int s = 0; s < 7; s++) { g0[s] = tr_frag(lds + ring + t0 + s * 2048, 512); g1[s] = tr_frag(lds + ring + t1 + s * 2048, 512); }
#pragma unroll
      for (int s = 0; s < 7; s++) {
        asm volatile("s_waitcnt vmcnt(%2)" : "+v"(af[s][0]), "+v"(af[s][1]) : "i"(2 * (6 - s)) : "memory");
        t[0] = mfma(g0[s], af[s][0], t[0]); t[1] = mfma(g1[s], af[s][0], t[1]);
        t[2] = mfma(g0[s], af[s][1], t[2]); t[3] = mfma(g1[s], af[s][1], t[3]);
      }
    }
    asm volatile("s_waitcnt lgkmcnt(0)" ::: "memory");
#pragma unroll
    for (int i = 1; i < 4; i++) tile_put(lds + ring + (i - 1) * 4096, t[i], lane);
    gsync(syb, w, ++eb);
#pragma unroll
    for (int i = 1; i < 4; i++) tile_add(lds + K2_B_OFF + (w ^ i) * 28672 + (i - 1) * 4096, t[0], lane);
    unsigned pk[8];
    const float og = 1.0f - gate;
#pragma unroll
    for (int j = 0; j < 8; j++)
      pk[j] = pack2(__builtin_fmaf(og, __builtin_bit_cast(float, x1p[j] << 16), t[0][2 * j]), __builtin_fmaf(og, __builtin_bit_cast(float, x1p[j] & 0xffff0000u), t[0][2 * j + 1]));
    char* op = out + (size_t)((tb64 * 2 + tj) * 32 + r) * 896 + (nb * 64 + ni * 32) * 2 + hh * 16;
#pragma unroll
    for (int gp = 0; gp < 2; gp++) {
      auto a2 = __builtin_amdgcn_permlane32_swap(pk[4 * gp], pk[4 * gp + 2], false, false);
      auto c2 = __builtin_amdgcn_permlane32_swap(pk[4 * gp + 1], pk[4 * gp + 3], false, false);
      u32x4 v; v[0] = a2[0]; v[1] = c2[0]; v[2] = a2[1]; v[3] = c2[1];
      *(u32x4*)(op + gp * 32) = v;
    }
    STAMP(9);
  }
}

extern "C" void kernel_launch(void* const* d_in, const int* in_sizes, int n_in,
                              void* d_out, int out_size, void* d_ws, size_t ws_size,
                              hipStream_t stream) {
  const char* x = (const char*)d_in[0]; const char* wqkv = (const char*)d_in[1]; const char* wo = (const char*)d_in[2];
  const char* w1 = (const char*)d_in[4]; const char* w2 = (const char*)d_in[5]; const char* w3 = (const char*)d_in[6];
  const char* w4 = (const char*)d_in[7]; const char* gw = (const char*)d_in[8];
  (void)hipFuncSetAttribute((const void*)fused_kernel, hipFuncAttributeMaxDynamicSharedMemorySize, K2_LDS);
  fused_kernel<<<256, 512, K2_LDS, stream>>>(x, wqkv, w1, gw, wo, w2, w3, w4, (char*)d_ws, (char*)d_out);
}
```

```cpp
#include <hip/hip_runtime.h>
#include <stdint.h>

#define CODE_LINES 400

typedef __attribute__((ext_vector_type(8))) __bf16 bf16x8;
typedef __attribute__((ext_vector_type(2))) __bf16 bf16x2;
typedef __attribute__((ext_vector_type(4))) short s16x4;
typedef __attribute__((ext_vector_type(16))) float f32x16;
typedef __attribute__((ext_vector_type(4))) float f32x4;
typedef __attribute__((ext_vector_type(2))) float f32x2;
typedef __attribute__((ext_vector_type(4))) unsigned u32x4;
typedef __attribute__((ext_vector_type(2))) unsigned u32x2;
#define LAS __attribute__((address_space(3)))
#define GAS __attribute__((address_space(1)))
#define DI __device__ __forceinline__

constexpr int SEQ = 2048, DM = 448, NH = 16, DH = 28, FF = 1792;

constexpr size_t WS_CNT   = 0;
constexpr size_t WS_GATE  = 16384;
constexpr size_t WS_STATS = 32768;
constexpr size_t WS_Q     = 1u << 20;
constexpr size_t WS_K     = WS_Q + (2u << 20);
constexpr size_t WS_V     = WS_K + (2u << 20);
constexpr size_t WS_H     = WS_V + (2u << 20);
constexpr size_t WS_X1    = WS_H + (2u << 20);
constexpr size_t WS_O     = WS_X1 + (2u << 20);
constexpr size_t WS_H3    = WS_O + (2u << 20);
constexpr unsigned TBYTES = 64u * 56u * 512u;
constexpr unsigned T3BYTES = 64u * 224u * 512u;

DI f32x16 mfma(u32x4 a, u32x4 b, f32x16 c) {
  return __builtin_amdgcn_mfma_f32_32x32x16_bf16(__builtin_bit_cast(bf16x8, a), __builtin_bit_cast(bf16x8, b), c, 0, 0, 0);
}
DI unsigned pack2(float a, float b) {
  f32x2 v; v[0] = a; v[1] = b;
  return __builtin_bit_cast(unsigned, __builtin_convertvector(v, bf16x2));
}
DI u32x4 tr_frag(const LAS char* p, int hi_off) {
  s16x4 lo = __builtin_amdgcn_ds_read_tr16_b64_v4i16((LAS s16x4*)p);
  s16x4 hi = __builtin_amdgcn_ds_read_tr16_b64_v4i16((LAS s16x4*)(p + hi_off));
  u32x2 a = __builtin_bit_cast(u32x2, lo), b = __builtin_bit_cast(u32x2, hi);
  u32x4 r; r[0] = a[0]; r[1] = a[1]; r[2] = b[0]; r[3] = b[1];
  return r;
}
DI unsigned gelu2(float a0, float a1, float c0, float c1, float nmean, float ga, float gb, float irs) {
  const f32x2 a = {a0, a1}, c = {c0, c1}, nm = {nmean, nmean}, ga2 = {ga, ga}, gb2 = {gb, gb}, ir = {irs, irs};
  const f32x2 t = __builtin_elementwise_fma(nm, c, a);
  const f32x2 z = t * __builtin_elementwise_fma(t * t, gb2, ga2);
  f32x2 e; e[0] = __builtin_amdgcn_exp2f(z[0]); e[1] = __builtin_amdgcn_exp2f(z[1]);
  const f32x2 d = __builtin_elementwise_fma(e, ir, ir);
  f32x2 rr; rr[0] = __builtin_amdgcn_rcpf(d[0]); rr[1] = __builtin_amdgcn_rcpf(d[1]);
  const f32x2 y = t * rr;
  return __builtin_bit_cast(unsigned, __builtin_convertvector(y, bf16x2));
}
DI float other_half(float v, int hh) {
  unsigned u = __builtin_bit_cast(unsigned, v);
  auto r = __builtin_amdgcn_permlane32_swap(u, u, false, false);
  return __builtin_bit_cast(float, hh ? r[0] : r[1]);
}
DI f32x4 sq4(unsigned a, unsigned b, f32x4 c) {
  u32x2 v; v[0] = a; v[1] = b;
  const s16x4 t = __builtin_bit_cast(s16x4, v);
  return __builtin_amdgcn_mfma_f32_4x4x4bf16_1k(t, t, c, 0, 0, 0);
}
DI f32x16 zero16() { f32x16 z; for (int i = 0; i < 16; i++) z[i] = 0.f; return z; }

DI void dma16_masked(unsigned voff, const void* sbase, unsigned ldsdst, uint64_t mask) {
  asm volatile("s_mov_b32 m0, %2\n\ts_mov_b64 exec, %3\n\tglobal_load_lds_dwordx4 %0, %1\n\ts_mov_b64 exec, -1"
               :: "v"(voff), "s"(sbase), "s"(ldsdst), "s"(mask) : "memory");
}
DI void dma16(unsigned voff, const void* sbase, unsigned ldsdst) {
  asm volatile("s_mov_b32 m0, %2\n\ts_nop 0\n\tglobal_load_lds_dwordx4 %0, %1"
               :: "v"(voff), "s"(sbase), "s"(ldsdst) : "memory");
}
DI void dma16x2(unsigned voff0, unsigned voff1, const void* sbase, unsigned ldsdst) {
  asm volatile("s_mov_b32 m0, %3\n\ts_nop 0\n\tglobal_load_lds_dwordx4 %0, %2\n\ts_add_u32 m0, m0, 0x400\n\ts_nop 0\n\tglobal_load_lds_dwordx4 %1, %2"
               :: "v"(voff0), "v"(voff1), "s"(sbase), "s"(ldsdst) : "memory", "scc");
}

constexpr int K1_SLOT    = 32768;
constexpr int K1_AUX_OFF = 151040;
constexpr int K1_CS_OFF  = K1_AUX_OFF + 3584;
constexpr int K1_SQ_OFF  = K1_CS_OFF + 2048;
constexpr int K1_CS2_OFF = K1_SQ_OFF + 1024;
constexpr int K1_LDS     = K1_CS2_OFF + 2048;

DI u32x4 tr_frag2(const LAS char* p0, const LAS char* p1) {
  s16x4 lo = __builtin_amdgcn_ds_read_tr16_b64_v4i16((LAS s16x4*)p0);
  s16x4 hi = __builtin_amdgcn_ds_read_tr16_b64_v4i16((LAS s16x4*)p1);
  u32x2 a = __builtin_bit_cast(u32x2, lo), b = __builtin_bit_cast(u32x2, hi);
  u32x4 r; r[0] = a[0]; r[1] = a[1]; r[2] = b[0]; r[3] = b[1];
  return r;
}
DI unsigned k1_tr_off(int lane, int t) {
  const int hh = lane >> 5, blk = (lane >> 4) & 1, q = (lane & 15) >> 2, p = lane & 3;
  int u = 7 * t + 4 * blk + p;
  if (blk == 1 && p == 3) u = 7 * t;
  return (8 * hh + q) * 256 + (((u >> 1) ^ (q << 2)) * 16) + (u & 1) * 8;
}

constexpr int K2_B_OFF   = 0;
constexpr int K2_A_OFF   = 114688;
constexpr int K2_CSP_OFF = K2_A_OFF + 32768;
constexpr int K2_CS_OFF  = K2_CSP_OFF + 2048;
constexpr int K2_M_OFF   = K2_CS_OFF + 512;
constexpr int K2_SY_OFF  = K2_M_OFF + 512;
constexpr int K2_END     = K2_SY_OFF + 64;
static_assert(K2_END <= K1_AUX_OFF, "lds map");
constexpr int K2_LDS     = K1_LDS;
constexpr unsigned SPIN_CAP = 1u << 20;
#define POLL_SLEEP 4
constexpr size_t WS_FX1 = WS_CNT, WS_FH3 = WS_CNT + 4096, WS_FO = WS_CNT + 8192, WS_FCS = WS_CNT + 12288;
constexpr size_t WS_EPOCH = WS_CNT + 12288 + 1024;
constexpr size_t WS_GX1 = 589824, WS_GH3 = 622592, WS_GO = 655360;
constexpr size_t WS_F1 = 524288;
constexpr size_t WS_CS3 = 24576;

DI void gsync(LAS unsigned* f, int w, unsigned epoch) {
  asm volatile("s_waitcnt lgkmcnt(0)" ::: "memory");
  __hip_atomic_store(f + w, epoch, __ATOMIC_RELAXED, __HIP_MEMORY_SCOPE_WORKGROUP);
  for (unsigned s = 0; s < SPIN_CAP; s++) {
    u32x4 v;
    asm volatile("ds_read_b128 %0, %1\n\ts_waitcnt lgkmcnt(0)" : "=v"(v) : "v"((unsigned)(uintptr_t)f) : "memory");
    const unsigned a = v[0], b = v[1], c = v[2], d = v[3];
    const unsigned m = min(min(a, b), min(c, d));
    if ((unsigned)__builtin_amdgcn_readfirstlane(m) >= epoch) break;
    __builtin_amdgcn_s_sleep(1);
  }
}
DI void flag_set_t(char* p, int lane, unsigned tag) {
  if (lane == 0) __hip_atomic_store((GAS unsigned*)p, tag, __ATOMIC_RELAXED, __HIP_MEMORY_SCOPE_AGENT);
}
#define flag_set(p, lane) flag_set_t(p, lane, tag)
#define flags_wait(p) flags_wait_t(p, tag)
DI char* f1_flag(char* ws, int type, int sub, int tile) { return ws + WS_F1 + ((type * 4 + sub) * 16 + (tile >> 2)) * 128; }
DI void block_publish(LAS unsigned* cnt, char* p, int lane, unsigned tag, unsigned last = 7u) {
  if (lane == 0) {
    const unsigned old = __hip_atomic_fetch_add(cnt, 1u, __ATOMIC_RELAXED, __HIP_MEMORY_SCOPE_WORKGROUP);
    if (old == last) __hip_atomic_store((GAS unsigned*)p, tag, __ATOMIC_RELAXED, __HIP_MEMORY_SCOPE_AGENT);
  }
}
DI void flags_wait_t(const char* p, unsigned tag) {
  for (unsigned s = 0; s < SPIN_CAP; s++) {
    const unsigned v = __hip_atomic_load((const GAS unsigned*)p, __ATOMIC_RELAXED, __HIP_MEMORY_SCOPE_AGENT);
    if (__all(v == tag)) break;
    __builtin_amdgcn_s_sleep(POLL_SLEEP);
  }
  asm volatile("" ::: "memory");
}
DI u32x4 mkrsrc(const void* p, unsigned bytes) {
  const uint64_t a = (uint64_t)(uintptr_t)p;
  u32x4 r; r[0] = (unsigned)a; r[1] = (unsigned)(a >> 32) & 0xffffu; r[2] = bytes; r[3] = 0x00020000u;
  return r;
}
#define BLOAD(dst, voff, rs, soff, imm) \
  asm volatile("buffer_load_dwordx4 %0, %1, %2, %3 offen offset:%4" : "=v"(dst) : "v"(voff), "s"(rs), "s"(soff), "i"(imm) : "memory")
#define BLOAD_SC1(dst, voff, rs, soff, imm) \
  asm volatile("buffer_load_dwordx4 %0, %1, %2, %3 offen offset:%4 sc1" : "=v"(dst) : "v"(voff), "s"(rs), "s"(soff), "i"(imm) : "memory")
DI void st16_sc1(u32x4 v, __amdgpu_buffer_rsrc_t rs, unsigned off) { __builtin_amdgcn_raw_buffer_store_b128(v, rs, off, 0, 16); }
DI void st8_sc1(u32x2 v, __amdgpu_buffer_rsrc_t rs, unsigned off) { __builtin_amdgcn_raw_buffer_store_b64(v, rs, off, 0, 16); }

DI void tile_put(LAS char* p, const f32x16& t, int lane) {
#pragma unroll
  for (int g = 0; g < 4; g++) { f32x4 v; v[0] = t[4 * g]; v[1] = t[4 * g + 1]; v[2] = t[4 * g + 2]; v[3] = t[4 * g + 3]; *(LAS f32x4*)(p + g * 1024 + lane * 16) = v; }
}
DI void tile_add(const LAS char* p, f32x16& t, int lane) {
#pragma unroll
  for (int g = 0; g < 4; g++) { f32x4 v = *(const LAS f32x4*)(p + g * 1024 + lane * 16); t[4 * g] += v[0]; t[4 * g + 1] += v[1]; t[4 * g + 2] += v[2]; t[4 * g + 3] += v[3]; }
}
DI void reduce_scatter(LAS char* buf, f32x16 (&t)[4], int w, int lane, int x0, int x1, LAS unsigned* f, unsigned& epoch) {
  tile_put(buf + (((w ^ x0) * 2 + 0) * 4096), t[2], lane);
  tile_put(buf + (((w ^ x1) * 2 + 1) * 4096), t[3], lane);
  gsync(f, w, ++epoch);
  tile_add(buf + (((w ^ 2 ^ x0) * 2 + 0) * 4096), t[0], lane);
  tile_add(buf + (((w ^ 2 ^ x1) * 2 + 1) * 4096), t[1], lane);
  asm volatile("s_waitcnt lgkmcnt(0)" ::: "memory");
  tile_put(buf + (((w ^ 2 ^ x0) * 2 + 0) * 4096), t[1], lane);
  gsync(f, w, ++epoch);
  tile_add(buf + (((w ^ 3 ^ x0) * 2 + 0) * 4096), t[0], lane);
  asm volatile("s_waitcnt lgkmcnt(0)" ::: "memory");
}

DI unsigned w64_dma_voff(int lane, unsigned ldw) { const int rin = lane >> 3, pc = lane & 7; return rin * ldw + ((pc ^ (((rin >> 1) & 1) << 2)) * 16); }
DI unsigned w64_tr_off(int lane, int nt) {
  const int hh = lane >> 5, blk = (lane >> 4) & 1, q = (lane & 15) >> 2, p = lane & 3;
  return (8 * hh + q) * 128 + ((nt ^ ((q >> 1) & 1)) * 64) + blk * 32 + p * 8;
}

#define ATT_EXP4(SC, B, P, D)                                                                                    \
    SC[B] = __builtin_amdgcn_exp2f(SC[B]); SC[B + 1] = __builtin_amdgcn_exp2f(SC[B + 1]);                       \
    SC[B + 2] = __builtin_amdgcn_exp2f(SC[B + 2]); SC[B + 3] = __builtin_amdgcn_exp2f(SC[B + 3]);               \
    P[D] = pack2(SC[B], SC[B + 1]); P[D + 1] = pack2(SC[B + 2], SC[B + 3]);                                     \
    __builtin_amdgcn_sched_barrier(0);
#define ATT_STEP(SC, SN, PC0, PC1, PP0, PP1, KN0, KN1, QN0, QN1, VP0, VP1, OP)                                  \
  {                                                                                                             \
    SN = mfma(KN0, QN0, zero16());                                                                              \
    __builtin_amdgcn_sched_barrier(0);                                                                          \
    ATT_EXP4(SC, 0, PC0, 0)                                                                                     \
    SN = mfma(KN1, QN1, SN);                                                                                    \
    __builtin_amdgcn_sched_barrier(0);                                                                          \
    ATT_EXP4(SC, 4, PC0, 2)                                                                                     \
    OP = mfma(VP0, PP0, OP);                                                                                    \
    __builtin_amdgcn_sched_barrier(0);                                                                          \
    ATT_EXP4(SC, 8, PC1, 0)                                                                                     \
    OP = mfma(VP1, PP1, OP);                                                                                    \
    __builtin_amdgcn_sched_barrier(0);                                                                          \
    ATT_EXP4(SC, 12, PC1, 2)                                                                                    \
  }

DI float max16(const f32x16& s) {
  float a = fmaxf(fmaxf(s[0], s[1]), s[2]), b = fmaxf(fmaxf(s[3], s[4]), s[5]), c = fmaxf(fmaxf(s[6], s[7]), s[8]);
  float d = fmaxf(fmaxf(s[9], s[10]), s[11]), e = fmaxf(fmaxf(s[12], s[13]), s[14]);
  return fmaxf(fmaxf(fmaxf(a, b), c), fmaxf(fmaxf(d, e), s[15]));
}

template <int KIND>
DI void phase1(LAS char* lds, const char* __restrict__ x, const char* __restrict__ wsrc, const int ldw, const char* __restrict__ gate_w,
               char* __restrict__ ws, const int type, const int sub, const int tg, const int wv, const char* touch) {
  const unsigned lds0 = (unsigned)(uintptr_t)lds;
  const int tid = threadIdx.x, lane = tid & 63, w = wv & 3, np = wv >> 2;
  const int r = lane & 31, hh = lane >> 5;
  const int tile = tg * 4 + w;
  const int drow = lane >> 4, dlc = (lane & 15) ^ (drow << 2);
  const unsigned dvoff = drow * ldw + (dlc < 14 ? dlc : 0) * 16;
  const uint64_t dmask = ~((3ull << 14) | (3ull << (16 + 10)) | (3ull << (32 + 6)) | (3ull << (48 + 2)));
  unsigned xv[2];
  {
    const int rin = lane >> 3, pc = lane & 7;
#pragma unroll
    for (int e = 0; e < 2; e++) xv[e] = rin * 896 + ((pc ^ ((4 * e + (rin >> 1)) & 7)) * 16);
  }
  const char* xbase = x + (size_t)(tg * 128 + w * 32 + np * 16) * 896;
  const char* wbase = wsrc + (size_t)(8 * wv) * ldw;
  auto issue_half = [&](int st, int jj) {
    const unsigned slot = lds0 + (st & 3) * K1_SLOT;
    dma16_masked(dvoff, wbase + (size_t)(64 * st + 4 * jj) * ldw, slot + (8 * wv + 4 * jj) * 256, dmask);
    dma16(xv[jj], xbase + jj * (8 * 896) + st * 128, slot + 16384 + w * 4096 + (2 * np + jj) * 1024);
  };
  unsigned gw0 = 0;
  if (KIND == 2) {
    const unsigned go = (tid < 448 ? tid : 447) * 2;
    asm volatile("global_load_ushort %0, %1, %2" : "=v"(gw0) : "v"(go), "s"(gate_w) : "memory");
  }
  issue_half(0, 0); issue_half(0, 1); issue_half(1, 0); issue_half(1, 1);
  issue_half(2, 0); issue_half(2, 1);
  unsigned xb[4];
#pragma unroll
  for (int ks = 0; ks < 4; ks++) xb[ks] = 16384 + w * 4096 + r * 128 + (((2 * ks + hh) ^ ((r >> 1) & 7)) * 16);
  const unsigned wb0 = k1_tr_off(lane, 2 * np), wb1 = k1_tr_off(lane, 2 * np + 1);
  const bool isaux = ((lane >> 4) & 1) == 1 && (lane & 3) == 3;
  const unsigned ab = isaux ? K1_AUX_OFF + (8 * hh + ((lane & 15) >> 2)) * 8 : wb1;
  const unsigned kstep = isaux ? 128u : 4096u, hoff = isaux ? 32u : 1024u;
  const unsigned sstep = isaux ? 512u : (unsigned)K1_SLOT, wrapst = isaux ? 512u : (unsigned)(-3 * K1_SLOT);
  unsigned a3[4];
#pragma unroll
  for (int ks = 0; ks < 4; ks++) a3[ks] = ab + ks * kstep;
  f32x16 acc[2], cs[2]; acc[0] = zero16(); acc[1] = zero16(); cs[0] = zero16(); cs[1] = zero16();
  u32x4 ones; ones[0] = ones[1] = ones[2] = ones[3] = 0x3F803F80u;
  u32x4 wf[2][4][2], xf[2][4], cf[2][2];
  u32x2 xq[2][4];
  f32x4 sqm = {0.f, 0.f, 0.f, 0.f};
  unsigned sink;
  auto read_part = [&](int st, int ks) {
    const int so = (st & 3) * K1_SLOT;
    xf[st & 1][ks] = *(const LAS u32x4*)(lds + xb[ks] + so);
    if (KIND == 2) xq[st & 1][ks] = *(const LAS u32x2*)(lds + xb[ks] + so + np * 8);
    wf[st & 1][ks][0] = tr_frag(lds + wb0 + so + ks * 4096, 1024);
    if (KIND == 2) { wf[st & 1][ks][1] = tr_frag2(lds + a3[ks], lds + a3[ks] + hoff); a3[ks] += (st == 3) ? wrapst : sstep; }
    else wf[st & 1][ks][1] = tr_frag(lds + wb1 + so + ks * 4096, 1024);
    if (KIND == 2 && ks == 3) { cf[st & 1][0] = tr_frag(lds + wb0 + so + w * 4096, 1024); cf[st & 1][1] = tr_frag(lds + wb1 + so + w * 4096, 1024); }
  };
  auto read_stage = [&](int st) {
#pragma unroll
    for (int ks = 0; ks < 4; ks++) read_part(st, ks);
  };
  asm volatile("s_waitcnt vmcnt(8)" : "+v"(gw0) :: "memory");
  if (KIND == 2) {
    u32x2 a; a[0] = 0x3F80u | (gw0 << 16); a[1] = 0u;
    *(LAS u32x2*)(lds + K1_AUX_OFF + (tid < 448 ? tid : 447) * 8) = a;
  }
  asm volatile("s_waitcnt lgkmcnt(0)\n\ts_barrier" ::: "memory");
  read_stage(0);
  issue_half(3, 0); issue_half(3, 1);
#pragma unroll
  for (int st = 0; st < 7; st++) {
    __builtin_amdgcn_sched_barrier(0);
    if (st < 3)       asm volatile("s_waitcnt vmcnt(8) lgkmcnt(0)\n\ts_barrier" ::: "memory");
    else if (st == 3) asm volatile("s_waitcnt vmcnt(9) lgkmcnt(0)\n\ts_barrier" ::: "memory");
    else if (st == 4) asm volatile("s_waitcnt vmcnt(5) lgkmcnt(0)\n\ts_barrier" ::: "memory");
    else if (st == 5) asm volatile("s_waitcnt vmcnt(1) lgkmcnt(0)\n\ts_barrier" ::: "memory");
    else              asm volatile("s_waitcnt lgkmcnt(0)\n\ts_barrier" ::: "memory");
#pragma unroll
    for (int ks = 0; ks < 4; ks++) {
      acc[0] = KIND == 1 ? mfma(xf[st & 1][ks], wf[st & 1][ks][0], acc[0]) : mfma(wf[st & 1][ks][0], xf[st & 1][ks], acc[0]);
      __builtin_amdgcn_sched_barrier(0);
      if (st < 6 && ks < 2) read_part(st + 1, 2 * ks);
      if (KIND == 2 && ks < 2) { sqm = sq4(xq[st & 1][2 * ks][0], xq[st & 1][2 * ks][1], sqm); }
      __builtin_amdgcn_sched_barrier(0);
      acc[1] = KIND == 1 ? mfma(xf[st & 1][ks], wf[st & 1][ks][1], acc[1]) : mfma(wf[st & 1][ks][1], xf[st & 1][ks], acc[1]);
      __builtin_amdgcn_sched_barrier(0);
      if (st < 6 && ks < 2) read_part(st + 1, 2 * ks + 1);
      if (KIND == 2 && ks < 2) { sqm = sq4(xq[st & 1][2 * ks + 1][0], xq[st & 1][2 * ks + 1][1], sqm); }
      if (st + 4 < 7 && ks >= 2) issue_half(st + 4, ks - 2);
      if (st == 2 && ks == 3) asm volatile("global_load_dword %0, %1, off" : "=v"(sink) : "v"(touch) : "memory");
      __builtin_amdgcn_sched_barrier(0);
    }
    if (KIND == 2) {
      cs[0] = mfma(ones, cf[st & 1][0], cs[0]); cs[1] = mfma(ones, cf[st & 1][1], cs[1]);
    }
  }
  __builtin_amdgcn_sched_barrier(0);
  asm volatile("s_waitcnt vmcnt(0)" : "+v"(sink) :: "memory");
  if (KIND < 2) {
    const float sc = type == 0 ? 0.27264314f : 1.0f;
    const __amdgpu_buffer_rsrc_t rq = __builtin_amdgcn_make_buffer_rsrc(ws + WS_Q, 0, 6u << 20, 0x00020000);
#pragma unroll
    for (int j = 0; j < 2; j++) {
      const int head = sub * 4 + 2 * np + j;
      u32x4 f0, f1;
#pragma unroll
      for (int d = 0; d < 4; d++) {
        f0[d] = pack2(acc[j][2 * d] * sc, acc[j][2 * d + 1] * sc);
        f1[d] = pack2(acc[j][8 + 2 * d] * sc, acc[j][8 + 2 * d + 1] * sc);
      }
      if (KIND == 1) {
        const unsigned v = r == 28 ? 0x3F803F80u : 0u;
#pragma unroll
        for (int d = 0; d < 4; d++) { f0[d] = r >= 28 ? v : f0[d]; f1[d] = r >= 28 ? v : f1[d]; }
      } else {
        f1[2] = hh ? (type == 1 ? 0x3F803F80u : 0u) : f1[2];
        f1[3] = hh ? (type == 1 ? 0x00003F80u : 0u) : f1[3];
      }
      const unsigned o = (unsigned)type * (2u << 20) + (unsigned)(head * 64 + tile) * 2048 + lane * 16;
      st16_sc1(f0, rq, o); st16_sc1(f1, rq, o + 1024);
    }
  } else {
    float sq = (lane & 2) ? ((lane & 1) ? sqm[3] : sqm[2]) : ((lane & 1) ? sqm[1] : sqm[0]);
    sq += other_half(sq, hh);
    if (hh == 0) {
      *(LAS float*)(lds + K1_SQ_OFF + ((np * 4 + w) * 32 + r) * 4) = sq;
      *(LAS float*)(lds + K1_CS_OFF + (w * 128 + (2 * np) * 32 + r) * 4) = cs[0][0];
      *(LAS float*)(lds + K1_CS_OFF + (w * 128 + (2 * np + 1) * 32 + r) * 4) = cs[1][0];
    }
    if (sub == 0 && np == 0 && hh == 1) {
      const float gt = __builtin_amdgcn_rcpf(1.0f + __builtin_amdgcn_exp2f(-1.442695041f * acc[1][13]));
      __hip_atomic_store((GAS float*)(ws + WS_GATE) + tile * 32 + r, gt, __ATOMIC_RELAXED, __HIP_MEMORY_SCOPE_AGENT);
    }
    float sx = acc[1][12];
    { float o = other_half(sx, hh); sx = hh ? sx : o; }
    const float mean = sx * (1.0f / 448.0f);
    asm volatile("s_waitcnt lgkmcnt(0)\n\ts_barrier" ::: "memory");
    sq += *(LAS float*)(lds + K1_SQ_OFF + (((np ^ 1) * 4 + w) * 32 + r) * 4);
    {
      const LAS float* cp = (const LAS float*)(lds + K1_CS_OFF) + np * 64 + lane;
      *(LAS float*)(lds + K1_CS2_OFF + (wv * 64 + lane) * 4) = (cp[0] + cp[128]) + (cp[256] + cp[384]);
    }
    const float var = fmaxf(sq * (1.0f / 448.0f) - mean * mean, 0.f) + 1e-5f;
    const float rstd = __builtin_amdgcn_rsqf(var);
    const float ga = -2.302208198f * rstd, gb = -0.1029432397f * rstd * rstd * rstd, irs = var * rstd;
    const __amdgpu_buffer_rsrc_t rh = __builtin_amdgcn_make_buffer_rsrc(ws + WS_H, 0, TBYTES, 0x00020000);
    unsigned pa[2][4][2];
#pragma unroll
    for (int j = 0; j < 2; j++) {
      const int t = 2 * np + j;
#pragma unroll
      for (int g = 0; g < 4; g++) {
        const f32x4 c = *(const LAS f32x4*)(lds + K1_CS2_OFF + (wv * 64 + j * 32 + 8 * g + 4 * hh) * 4);
        pa[j][g][0] = gelu2(acc[j][4 * g], acc[j][4 * g + 1], c[0], c[1], -mean, ga, gb, irs);
        pa[j][g][1] = gelu2(acc[j][4 * g + 2], acc[j][4 * g + 3], c[2], c[3], -mean, ga, gb, irs);
      }
    }
    const unsigned dst = (unsigned)(tile * 56 + 14 * sub + 7 * np) * 512 + r * 16;
    u32x4 v0, v1, v2, v3;
#pragma unroll
    for (int d = 0; d < 2; d++) {
      auto s0 = __builtin_amdgcn_permlane32_swap(pa[0][0][d], pa[0][1][d], false, false);
      v0[d] = s0[0]; v0[2 + d] = s0[1];
      auto s1 = __builtin_amdgcn_permlane32_swap(pa[0][3][d], pa[0][2][d], false, false);
      v1[d] = s1[0]; v1[2 + d] = hh ? pa[0][2][d] : pa[1][0][d];
      auto s2 = __builtin_amdgcn_permlane32_swap(pa[1][0][d], pa[1][2][d], false, false);
      v2[d] = hh ? pa[1][1][d] : s2[1]; v2[2 + d] = hh ? s2[0] : pa[1][1][d];
      auto s3 = __builtin_amdgcn_permlane32_swap(pa[1][2][d], pa[1][2][d], false, false);
      v3[d] = s3[1]; v3[2 + d] = pa[1][3][d];
    }
    st16_sc1(v0, rh, dst + hh * 512);
    st16_sc1(v1, rh, dst + (hh ? 2 : 3) * 512);
    st16_sc1(v2, rh, dst + (4 + hh) * 512);
    st16_sc1(v3, rh, hh ? 0x80000000u : dst + 6 * 512);
  }
}

__global__ __launch_bounds__(512, 2) void fused_kernel(const char* __restrict__ x, const char* __restrict__ wqkv,
                                                       const char* __restrict__ w1, const char* __restrict__ gate_w,
                                                       const char* __restrict__ wo,
                                                       const char* __restrict__ w2, const char* __restrict__ w3,
                                                       const char* __restrict__ w4, char* __restrict__ ws,
                                                       char* __restrict__ out) {
  extern __shared__ __attribute__((aligned(16))) char smem[];
  LAS char* lds = (LAS char*)smem;
  const unsigned lds0 = (unsigned)(uintptr_t)lds;
  LAS unsigned* sy = (LAS unsigned*)(lds + K2_SY_OFF);
  const int tid = threadIdx.x, lane = tid & 63;
  const int wv = __builtin_amdgcn_readfirstlane(tid >> 6);
  const int w = wv & 3;
  const int b = blockIdx.x, xcd = b & 7, bi = b >> 3;
  const int r = lane & 31, hh = lane >> 5;
  const bool has_gemm = bi < 28;
  const int nb = bi % 7, tb64 = xcd * 4 + bi / 7;
  const int ni = w & 1, tj = w >> 1;
  const int p1type = bi < 24 ? bi % 3 : 3, p1tg = (xcd >> 2) * 8 + (bi < 24 ? bi / 3 : bi - 24);
  if (tid < 16) sy[tid] = 0u;
  {
    const int type = p1type, sub = xcd & 3, tg = p1tg;
    int k = (b * 8 + wv) * 16 + (lane & 15); k = k < 31360 ? k : 31359;
    const char* touch = k < 3136 ? w2 + (size_t)k * 128 : k < 15680 ? w3 + (size_t)(k - 3136) * 128 : k < 28224 ? w4 + (size_t)(k - 15680) * 128 : wo + (size_t)(k - 28224) * 128;
    {
      uint64_t pc;
      asm volatile("s_getpc_b64 %0" : "=s"(pc));
      int i = (bi * 8 + wv) * 2 + (lane & 1); i = i < CODE_LINES ? i : 0;
      if (lane >= 32) touch = (const char*)(uintptr_t)((pc & ~127ull) + (uint64_t)i * 128);
    }
    if (type < 2)       phase1<0>(lds, x, wqkv + (type * 448 + sub * 112) * 2, 2688, gate_w, ws, type, sub, tg, wv, touch);
    else if (type == 2) phase1<1>(lds, x, wqkv + (2 * 448 + sub * 112) * 2, 2688, gate_w, ws, type, sub, tg, wv, touch);
    else                phase1<2>(lds, x, w1 + sub * 224, 896, gate_w, ws, type, sub, tg, wv, touch);
  }
#define EPOCH_LOAD unsigned epoch; asm volatile("global_load_dword %0, %1, %2 sc1" : "=v"(epoch) : "v"(0u), "s"(ws + WS_EPOCH) : "memory")
#define EPOCH_WAIT(N) asm volatile("s_waitcnt vmcnt(" #N ")" : "+v"(epoch) :: "memory"); epoch = __builtin_amdgcn_readfirstlane(epoch); \
  const unsigned tag = (epoch * 0x9E3779B1u) ^ 0x5bd1e995u
  char* const my_f1 = f1_flag(ws, p1type, xcd & 3, p1tg * 4);

  if (wv < 4) {
    const int head = 2 * xcd + (bi >> 4), qb = bi & 15;
    EPOCH_LOAD; EPOCH_WAIT(0);
    block_publish(sy + 12, my_f1, lane, tag);
    const __amdgpu_buffer_rsrc_t rq = __builtin_amdgcn_make_buffer_rsrc(ws + WS_Q, 0, 6u << 20, 0x00020000);
    const unsigned qo = (unsigned)(head * 64 + qb * 4) * 2048 + lane * 16;
    const unsigned kho = (2u << 20) + (unsigned)(head * 64) * 2048 + lane * 16, vho = kho + (2u << 20);
    const unsigned ko = kho + w * 16 * 2048, vo = vho + w * 16 * 2048;
#define LDQ(off) __builtin_amdgcn_raw_buffer_load_b128(rq, (off), 0, 16)
    unsigned ea = 0;
    {
      const int hs = head >> 2;
      flags_wait(lane < 8 ? f1_flag(ws, 1 + (lane >> 2), hs, w * 16 + (lane & 3) * 4) : f1_flag(ws, 0, hs, qb * 4));
    }
    __builtin_amdgcn_s_setprio(3);
    u32x4 q[4][2];
#pragma unroll
    for (int i = 0; i < 4; i++) { q[i][0] = LDQ(qo + (w ^ i) * 2048); q[i][1] = LDQ(qo + (w ^ i) * 2048 + 1024); }
    u32x4 kb[4][2], vb[4][2];
#pragma unroll
    for (int j = 0; j < 3; j++) { kb[j][0] = LDQ(ko + j * 2048); kb[j][1] = LDQ(ko + j * 2048 + 1024); vb[j][0] = LDQ(vo + j * 2048); vb[j][1] = LDQ(vo + j * 2048 + 1024); }
    f32x16 o[4];
    {
      LAS float* mx = (LAS float*)(lds + K2_M_OFF);
      f32x16 s = mfma(kb[0][0], q[0][0], zero16());
      s = mfma(kb[0][1], q[0][1], s);
      const float m = max16(s);
      if (hh) mx[w * 32 + r] = m;
      gsync(sy, w, ++ea);
#pragma unroll
      for (int i = 0; i < 4; i++) {
        const float mm = mx[(w ^ i) * 32 + r];
        q[i][1][2] = hh ? (pack2(-mm, 0.f)) : q[i][1][2];
      }
    }
    __builtin_amdgcn_s_setprio(0);
    {
#pragma unroll
      for (int i = 0; i < 4; i++) o[i] = zero16();
      f32x16 sA, sB;
      u32x4 pA0, pA1, pB0, pB1;
      sA = mfma(kb[0][0], q[0][0], zero16());
      sA = mfma(kb[0][1], q[0][1], sA);
      pB0 = pB1 = (u32x4){0u, 0u, 0u, 0u};
      vb[3][0] = vb[3][1] = (u32x4){0u, 0u, 0u, 0u};
#pragma unroll 1
      for (int kq = 0; kq < 4; kq++) {
#pragma unroll
        for (int j = 0; j < 4; j++) {
          const int kt = kq * 4 + j;
          const int jp = (j + 3) & 3, jn = (j + 1) & 3;
          ATT_STEP(sA, sB, pA0, pA1, pB0, pB1, kb[j][0], kb[j][1], q[1][0], q[1][1], vb[jp][0], vb[jp][1], o[3]);
          {
            const int kn = kt + 3 < 16 ? kt + 3 : 15;
            __builtin_amdgcn_s_setprio(3);
            kb[jp][0] = LDQ(ko + kn * 2048); kb[jp][1] = LDQ(ko + kn * 2048 + 1024); vb[jp][0] = LDQ(vo + kn * 2048); vb[jp][1] = LDQ(vo + kn * 2048 + 1024);
            __builtin_amdgcn_sched_barrier(0);
            __builtin_amdgcn_s_setprio(0);
          }
          ATT_STEP(sB, sA, pB0, pB1, pA0, pA1, kb[j][0], kb[j][1], q[2][0], q[2][1], vb[j][0], vb[j][1], o[0]);
          ATT_STEP(sA, sB, pA0, pA1, pB0, pB1, kb[j][0], kb[j][1], q[3][0], q[3][1], vb[j][0], vb[j][1], o[1]);
          ATT_STEP(sB, sA, pB0, pB1, pA0, pA1, kb[jn][0], kb[jn][1], q[0][0], q[0][1], vb[j][0], vb[j][1], o[2]);
        }
      }
      o[3] = mfma(vb[3][0], pB0, o[3]);
      o[3] = mfma(vb[3][1], pB1, o[3]);
    }
    __builtin_amdgcn_s_setprio(3);
    flags_wait(f1_flag(ws, 3, 0, qb * 4));
    const float gate_q = __hip_atomic_load((const GAS float*)(ws + WS_GATE) + (qb * 4 + w) * 32 + r, __ATOMIC_RELAXED, __HIP_MEMORY_SCOPE_AGENT);
    reduce_scatter(lds + K2_A_OFF, o, w, lane, 0, 0, sy, ea);
    float l = o[0][12];
    { float t = other_half(l, hh); l = hh ? l : t; }
    {
      float amax = 0.f;
#pragma unroll
      for (int i = 0; i < 12; i++) amax = fmaxf(amax, fabsf(o[0][i]));
      if (!hh) amax = fmaxf(fmaxf(amax, fabsf(o[0][12])), fmaxf(fabsf(o[0][13]), fmaxf(fabsf(o[0][14]), fabsf(o[0][15]))));
      const bool ok = (l > 1.0e-10f) && (l < 1.0e30f) && (amax < 1.0e37f);
      if (__any(!ok)) {
        flags_wait(f1_flag(ws, 1 + (lane >> 5), head >> 2, (lane & 15) * 4));
        u32x4 q0 = q[0][0], q1 = q[0][1];
        q1[2] = hh ? 0u : q1[2];
        float tm = -3.0e38f;
#pragma unroll 1
        for (int kt = 0; kt < 64; kt++) {
          f32x16 s = mfma(LDQ(kho + kt * 2048), q0, zero16());
          s = mfma(LDQ(kho + kt * 2048 + 1024), q1, s);
          tm = fmaxf(tm, max16(s));
        }
        tm = -fmaxf(tm, other_half(tm, hh));
        const unsigned h0 = pack2(tm, 0.f) & 0xffffu; const float f0 = __builtin_bit_cast(float, h0 << 16);
        const unsigned h1 = pack2(tm - f0, 0.f) & 0xffffu; const float f1 = __builtin_bit_cast(float, h1 << 16);
        const unsigned h2 = pack2((tm - f0) - f1, 0.f) & 0xffffu;
        q1[2] = hh ? (h0 | (h1 << 16)) : q1[2];
        q1[3] = hh ? h2 : q1[3];
        f32x16 oo = zero16();
#pragma unroll 1
        for (int kt = 0; kt < 64; kt++) {
          f32x16 s = mfma(LDQ(kho + kt * 2048), q0, zero16());
          s = mfma(LDQ(kho + kt * 2048 + 1024), q1, s);
          u32x4 p0, p1;
#pragma unroll
          for (int d = 0; d < 4; d++) {
            p0[d] = pack2(__builtin_amdgcn_exp2f(s[2 * d]), __builtin_amdgcn_exp2f(s[2 * d + 1]));
            p1[d] = pack2(__builtin_amdgcn_exp2f(s[8 + 2 * d]), __builtin_amdgcn_exp2f(s[8 + 2 * d + 1]));
          }
          oo = mfma(LDQ(vho + kt * 2048), p0, oo);
          oo = mfma(LDQ(vho + kt * 2048 + 1024), p1, oo);
        }
        o[0] = oo;
        l = oo[12];
        { float t = other_half(l, hh); l = hh ? l : t; }
      }
    }
    {
      const __amdgpu_buffer_rsrc_t ro = __builtin_amdgcn_make_buffer_rsrc(ws + WS_O, 0, TBYTES, 0x00020000);
      const float inv = gate_q * __builtin_amdgcn_rcpf(l);
      const unsigned base = (unsigned)(qb * 4 + w) * (56 * 512) + r * 16;
#pragma unroll
      for (int g = 0; g < 4; g++) {
        u32x2 v; v[0] = pack2(o[0][4 * g] * inv, o[0][4 * g + 1] * inv); v[1] = pack2(o[0][4 * g + 2] * inv, o[0][4 * g + 3] * inv);
        const int u = 7 * head + 2 * g + hh;
        if (g < 3 || hh == 0) st8_sc1(v, ro, base + (u >> 1) * 512 + (u & 1) * 8);
      }
    }
    asm volatile("s_waitcnt vmcnt(0)" ::: "memory");
    block_publish(sy + 13, ws + WS_GO + (qb * 16 + head) * 128, lane, tag, 3u);
    return;
  }

  if (!has_gemm) {
    EPOCH_LOAD; EPOCH_WAIT(0);
    block_publish(sy + 12, my_f1, lane, tag);
    if (b == 224 && w == 3) {
#pragma unroll 1
      for (int i = 0; i < 4; i++) flags_wait(ws + WS_F1 + (i * 64 + lane) * 128);
      if (lane == 0) __hip_atomic_store((GAS unsigned*)(ws + WS_EPOCH), epoch + 1u, __ATOMIC_RELAXED, __HIP_MEMORY_SCOPE_AGENT);
    }
    const int id = (xcd * 4 + bi - 28) * 2 + w;
    if (w < 2 && id < 56) {
      const unsigned reg = K2_B_OFF + w * 28672;
      const unsigned dv = (lane >> 2) * 3584 + (lane & 3) * 16;
      const char* src = w3 + id * 64;
#pragma unroll
      for (int j = 0; j < 28; j++) dma16(dv, src + (size_t)j * (16 * 3584), lds0 + reg + j * 1024);
      asm volatile("s_waitcnt vmcnt(0)" ::: "memory");
      const unsigned to = reg + (8 * hh + ((lane & 15) >> 2)) * 64 + ((lane >> 4) & 1) * 32 + (lane & 3) * 8;
      u32x4 ones; ones[0] = ones[1] = ones[2] = ones[3] = 0x3F803F80u;
      f32x16 cs = zero16();
#pragma unroll
      for (int s = 0; s < 28; s++) cs = mfma(ones, tr_frag(lds + to + s * 1024, 256), cs);
      if (hh == 0) __hip_atomic_store((GAS float*)(ws + WS_CS3) + id * 32 + r, cs[0], __ATOMIC_RELAXED, __HIP_MEMORY_SCOPE_AGENT);
      asm volatile("s_waitcnt vmcnt(0)" ::: "memory");
      flag_set(ws + WS_FCS + id * 4, lane);
    }
    return;
  }
  __builtin_amdgcn_s_setprio(2);
  unsigned eb = 0;
  LAS unsigned* syb = sy + 4;
  const int nb3 = bi % 14, tb128 = xcd * 2 + bi / 14;
  const unsigned dv64 = w64_dma_voff(lane, 896), dv64b = dv64 + 8 * 896;
  const unsigned t0 = w64_tr_off(lane, ni), t1 = w64_tr_off(lane, ni ^ 1);
  const unsigned dv3 = (lane >> 4) * 3584 + (((lane & 15) ^ ((lane >> 4) << 2)) * 16);
  const unsigned dv3b = dv3 + 4 * 3584;
  const char* w3src = w3 + nb3 * 256;
  const unsigned ring = K2_B_OFF + w * 28672;
  const char* w4src = w4 + (size_t)(448 * w) * 896 + nb * 128;
  unsigned x1p[8];
  const unsigned sl = K2_B_OFF + 57344 + w * 14336;
  EPOCH_LOAD;
  EPOCH_WAIT(0);
  block_publish(sy + 12, my_f1, lane, tag);
  {
    const char* wsrc = w2 + (size_t)(112 * w) * 896 + nb * 128;
#pragma unroll
    for (int j = 0; j < 14; j += 2) dma16x2(dv64, dv64b, wsrc + j * (8 * 896), lds0 + sl + j * 1024);
  }
#pragma unroll
  for (int j = 0; j < 14; j += 2) { const int row0 = 56 * w + 4 * j; dma16x2(dv3, dv3b, w3src + (size_t)row0 * 3584, lds0 + K2_B_OFF + row0 * 256); }
  {
    const u32x4 rh = mkrsrc(ws + WS_H, TBYTES);
    const int delta = w == 0 ? -1 : w == 1 ? 1 : w == 2 ? -2 : 2;
    u32x4 af[7][2];
    unsigned av[2];
#pragma unroll
    for (int e = 0; e < 2; e++) {
      const int tok = (tb64 * 2 + (tj ^ e)) * 32 + r + delta;
      av[e] = (tok >= 0 && tok < SEQ) ? (unsigned)(((tok >> 5) * 56 + 14 * w + hh) * 512 + (tok & 31) * 16) : 0x80000000u;
    }
    {
      int tl = tb64 * 2 - 1 + (lane & 3); tl = tl < 0 ? 0 : tl > 63 ? 63 : tl;
      flags_wait(f1_flag(ws, 3, w, tl));
    }
    asm volatile("s_waitcnt vmcnt(0)" ::: "memory");
#pragma unroll
    for (int s = 0; s < 7; s++) {
      BLOAD_SC1(af[s][0], av[0], rh, (s >> 2) * 4096, (s & 3) * 1024);
      BLOAD_SC1(af[s][1], av[1], rh, (s >> 2) * 4096, (s & 3) * 1024);
    }
    u32x2 xr[4];
    {
      const char* xp = x + (size_t)((tb64 * 2 + tj) * 32) * 896 + (nb * 64 + ni * 32) * 2;
      const unsigned xo = r * 896 + hh * 8;
#pragma unroll
      for (int g = 0; g < 4; g++) asm volatile("global_load_dwordx2 %0, %1, %2 offset:%3" : "=v"(xr[g]) : "v"(xo), "s"(xp), "i"(g * 16) : "memory");
    }
    f32x16 t[4];
#pragma unroll
    for (int i = 0; i < 4; i++) t[i] = zero16();
    u32x4 f0[7], f1[7];
#pragma unroll
    for (int s = 0; s < 7; s++) { f0[s] = tr_frag(lds + sl + t0 + s * 2048, 512); f1[s] = tr_frag(lds + sl + t1 + s * 2048, 512); }
#pragma unroll
    for (int s = 0; s < 7; s++) {
      asm volatile("s_waitcnt vmcnt(%2)" : "+v"(af[s][0]), "+v"(af[s][1]) : "i"(16 - 2 * s) : "memory");
      t[0] = mfma(f0[s], af[s][0], t[0]); t[1] = mfma(f1[s], af[s][0], t[1]);
      t[2] = mfma(f0[s], af[s][1], t[2]); t[3] = mfma(f1[s], af[s][1], t[3]);
      __builtin_amdgcn_sched_barrier(0);
    }
    asm volatile("s_waitcnt vmcnt(0) lgkmcnt(0)" : "+v"(xr[0]), "+v"(xr[1]), "+v"(xr[2]), "+v"(xr[3]) :: "memory");
#pragma unroll
    for (int i = 1; i < 4; i++) tile_put(lds + sl + (i - 1) * 4096, t[i], lane);
    gsync(syb, w, ++eb);
#pragma unroll
    for (int i = 1; i < 4; i++) tile_add(lds + K2_B_OFF + 57344 + (w ^ i) * 14336 + (i - 1) * 4096, t[0], lane);
    float s1 = 0.f, s2 = 0.f;
#pragma unroll
    for (int g = 0; g < 4; g++) {
      const float a0 = __builtin_bit_cast(float, xr[g][0] << 16) + t[0][4 * g], a1 = __builtin_bit_cast(float, xr[g][0] & 0xffff0000u) + t[0][4 * g + 1];
      const float a2 = __builtin_bit_cast(float, xr[g][1] << 16) + t[0][4 * g + 2], a3 = __builtin_bit_cast(float, xr[g][1] & 0xffff0000u) + t[0][4 * g + 3];
      x1p[2 * g] = pack2(a0, a1); x1p[2 * g + 1] = pack2(a2, a3);
    }
    const __amdgpu_buffer_rsrc_t rx = __builtin_amdgcn_make_buffer_rsrc(ws + WS_X1, 0, TBYTES, 0x00020000);
    const int tile = tb64 * 2 + tj;
#pragma unroll
    for (int gp = 0; gp < 2; gp++) {
      auto a = __builtin_amdgcn_permlane32_swap(x1p[4 * gp], x1p[4 * gp + 2], false, false);
      auto c = __builtin_amdgcn_permlane32_swap(x1p[4 * gp + 1], x1p[4 * gp + 3], false, false);
      u32x4 v; v[0] = a[0]; v[1] = c[0]; v[2] = a[1]; v[3] = c[1];
      st16_sc1(v, rx, (unsigned)(tile * 56 + nb * 8 + ni * 4 + 2 * gp + hh) * 512 + r * 16);
    }
#pragma unroll
    for (int j = 0; j < 8; j++) {
      const float lo = __builtin_bit_cast(float, x1p[j] << 16), hi = __builtin_bit_cast(float, x1p[j] & 0xffff0000u);
      s1 += lo + hi; s2 = __builtin_fmaf(lo, lo, s2); s2 = __builtin_fmaf(hi, hi, s2);
    }
    s1 += other_half(s1, hh); s2 += other_half(s2, hh);
    const __amdgpu_buffer_rsrc_t rs = __builtin_amdgcn_make_buffer_rsrc(ws + WS_STATS, 0, 64 * 14 * 32 * 8, 0x00020000);
    { u32x2 v; v[0] = __builtin_bit_cast(unsigned, s1); v[1] = __builtin_bit_cast(unsigned, s2); st8_sc1(v, rs, hh ? 0x80000000u : (unsigned)((tile * 14 + nb * 2 + ni) * 32 + r) * 8); }
    gsync(syb, w, ++eb);
#pragma unroll
    for (int j = 0; j < 14; j += 2) { const int row0 = 224 + 56 * w + 4 * j; dma16x2(dv3, dv3b, w3src + (size_t)row0 * 3584, lds0 + K2_B_OFF + row0 * 256); }
    asm volatile("s_waitcnt vmcnt(14)" ::: "memory");
    block_publish(sy + 14, ws + WS_GX1 + (tb64 * 8 + nb) * 128, lane, tag, 3u);
  }
  {
    asm volatile("s_waitcnt vmcnt(0)" ::: "memory");
    gsync(syb, w, ++eb);
    unsigned tb[4];
    {
      const int blk = (lane >> 4) & 1, q = (lane & 15) >> 2, p = lane & 3;
#pragma unroll
      for (int n = 0; n < 4; n++) tb[n] = K2_B_OFF + (8 * hh + q) * 256 + ((n ^ q) * 64) + blk * 32 + p * 8;
    }
    const int tile = tb128 * 4 + w;
    flags_wait(lane < 16 ? ws + WS_GX1 + ((tile >> 1) * 8 + (lane < 7 ? lane : 0)) * 128 : lane < 32 ? ws + WS_FCS + (nb3 * 4 + (lane & 3)) * 4
               : f1_flag(ws, 3, 0, lane < 48 ? tile : tb64 * 2 + tj));
    const u32x4 rx = mkrsrc(ws + WS_X1, TBYTES);
    const u32x4 rsd = mkrsrc(ws + WS_STATS, 64 * 14 * 32 * 8);
    const u32x4 rcd = mkrsrc(ws + WS_CS3, 1792 * 4);
    u32x2 sv[7], cv; float g3;
    {
      const unsigned so = (unsigned)((tile * 14 + hh * 7) * 32 + r) * 8, co = (unsigned)(nb3 * 128 + lane * 2) * 4;
#pragma unroll
      for (int j = 0; j < 7; j++) asm volatile("buffer_load_dwordx2 %0, %1, %2, 0 offen offset:%3 sc1" : "=v"(sv[j]) : "v"(so), "s"(rsd), "i"(j * 256) : "memory");
      asm volatile("buffer_load_dwordx2 %0, %1, %2, 0 offen sc1" : "=v"(cv) : "v"(co), "s"(rcd) : "memory");
      const unsigned go = (unsigned)(tile * 32 + r) * 4;
      asm volatile("global_load_dword %0, %1, %2 sc1" : "=v"(g3) : "v"(go), "s"(ws + WS_GATE) : "memory");
    }
    constexpr int AW = 16;
    u32x4 af[AW];
    const unsigned av = (unsigned)(tile * 56) * 512 + lane * 16;
#pragma unroll
    for (int s = 0; s < AW; s++) BLOAD_SC1(af[s], av, rx, (s >> 2) * 4096, (s & 3) * 1024);
    const __amdgpu_buffer_rsrc_t r3 = __builtin_amdgcn_make_buffer_rsrc(ws + WS_H3, 0, T3BYTES, 0x00020000);
    u32x4 wfr[3][4];
#pragma unroll
    for (int k = 0; k < 2; k++)
#pragma unroll
      for (int n = 0; n < 4; n++) wfr[k][n] = tr_frag(lds + tb[n] + k * 4096, 1024);
    f32x16 acc[4];
#pragma unroll
    for (int n = 0; n < 4; n++) acc[n] = zero16();
#pragma unroll
    for (int s = 0; s < 28; s++) {
      asm volatile("s_waitcnt vmcnt(%1)" : "+v"(af[s % AW]) : "i"(s + AW <= 28 ? AW - 1 : 27 - s) : "memory");
#pragma unroll
      for (int n = 0; n < 4; n++) {
        acc[n] = mfma(wfr[s % 3][n], af[s % AW], acc[n]);
        if (s + 2 < 28) wfr[(s + 2) % 3][n] = tr_frag(lds + tb[n] + (s + 2) * 4096, 1024);
        __builtin_amdgcn_sched_barrier(0);
      }
      if (s + AW < 28) BLOAD_SC1(af[s % AW], av, rx, ((s + AW) >> 2) * 4096, ((s + AW) & 3) * 1024);
      __builtin_amdgcn_sched_barrier(0);
    }
    asm volatile("" : "+v"(sv[0]), "+v"(sv[1]), "+v"(sv[2]), "+v"(sv[3]), "+v"(sv[4]), "+v"(sv[5]), "+v"(sv[6]), "+v"(cv), "+v"(g3));
    const unsigned csl = K2_CSP_OFF + w * 512;
    *(LAS u32x2*)(lds + csl + lane * 8) = cv;
    float nmean, ga, gb, irs;
    {
      float s1 = 0.f, s2 = 0.f;
#pragma unroll
      for (int j = 0; j < 7; j++) { const unsigned u0 = sv[j][0], u1 = sv[j][1]; s1 += __builtin_bit_cast(float, u0); s2 += __builtin_bit_cast(float, u1); }
      s1 += other_half(s1, hh); s2 += other_half(s2, hh);
      const float mean = s1 * (1.0f / 448.0f);
      const float var = fmaxf(s2 * (1.0f / 448.0f) - mean * mean, 0.f) + 1e-5f;
      const float rstd = __builtin_amdgcn_rsqf(var);
      nmean = -mean; ga = -2.302208198f * rstd; gb = -0.1029432397f * rstd * rstd * rstd;
      irs = var * rstd * __builtin_amdgcn_rcpf(fmaxf(1.0f - g3, 1.0e-30f));
    }
#pragma unroll
    for (int n = 0; n < 4; n++) {
#pragma unroll
      for (int gp = 0; gp < 2; gp++) {
        unsigned pk[4];
#pragma unroll
        for (int g2 = 0; g2 < 2; g2++) {
          const int g = 2 * gp + g2;
          const f32x4 c4 = *(const LAS f32x4*)(lds + csl + (n * 32 + 8 * g + 4 * hh) * 4);
          pk[2 * g2] = gelu2(acc[n][4 * g], acc[n][4 * g + 1], c4[0], c4[1], nmean, ga, gb, irs);
          pk[2 * g2 + 1] = gelu2(acc[n][4 * g + 2], acc[n][4 * g + 3], c4[2], c4[3], nmean, ga, gb, irs);
        }
        auto a = __builtin_amdgcn_permlane32_swap(pk[0], pk[2], false, false);
        auto c = __builtin_amdgcn_permlane32_swap(pk[1], pk[3], false, false);
        u32x4 v; v[0] = a[0]; v[1] = c[0]; v[2] = a[1]; v[3] = c[1];
        st16_sc1(v, r3, (unsigned)(tile * 224 + nb3 * 16 + n * 4 + 2 * gp + hh) * 512 + r * 16);
      }
    }
    gsync(syb, w, ++eb);
    asm volatile("s_waitcnt vmcnt(0)" ::: "memory");
    block_publish(sy + 15, ws + WS_GH3 + (tb128 * 16 + nb3) * 128, lane, tag, 3u);
  }
  {
    const char* wsrc = w4src;
#pragma unroll
    for (int j = 0; j < 28; j += 2) dma16x2(dv64, dv64b, wsrc + j * (8 * 896), lds0 + ring + j * 1024);
    const float gate = __hip_atomic_load((const GAS float*)(ws + WS_GATE) + (tb64 * 2 + tj) * 32 + r, __ATOMIC_RELAXED, __HIP_MEMORY_SCOPE_AGENT);
    {
      const int blk0 = (7 * w) >> 1;
      const int k = lane & 3;
      flags_wait(ws + WS_GH3 + ((tb64 >> 1) * 16 + blk0 + k) * 128);
    }
    asm volatile("s_waitcnt vmcnt(0)" ::: "memory");
    const u32x4 r3 = mkrsrc(ws + WS_H3, T3BYTES);
    unsigned av[2];
#pragma unroll
    for (int e = 0; e < 2; e++) av[e] = (unsigned)((tb64 * 2 + (tj ^ e)) * 224 + 56 * w) * 512 + lane * 16;
    u32x4 af[14][2];
#pragma unroll
    for (int s = 0; s < 14; s++)
#pragma unroll
      for (int e = 0; e < 2; e++) BLOAD_SC1(af[s][e], av[e], r3, (s >> 2) * 4096, (s & 3) * 1024);
    f32x16 t[4];
#pragma unroll
    for (int i = 0; i < 4; i++) t[i] = zero16();
    u32x4 f0[2], f1[2];
    const char* wosrc = wo + (size_t)(112 * w) * 896 + nb * 128;
    f0[0] = tr_frag(lds + ring + t0, 512); f1[0] = tr_frag(lds + ring + t1, 512);
#pragma unroll
    for (int s = 0; s < 28; s++) {
      const int sl = s % 14;
      asm volatile("s_waitcnt lgkmcnt(0)" : "+v"(f0[s & 1]), "+v"(f1[s & 1]) :: "memory");
      if (s + 1 < 28) {
        const int nl = (s + 1) % 14;
        if (s == 13) asm volatile("s_waitcnt vmcnt(50)" ::: "memory");
        else if (s > 13) asm volatile("s_waitcnt vmcnt(%0)" :: "i"(2 + 4 * (13 - nl) + 2 * (s - 14 < 7 ? s - 14 : 7)) : "memory");
        f0[(s + 1) & 1] = tr_frag(lds + ring + t0 + nl * 2048, 512); f1[(s + 1) & 1] = tr_frag(lds + ring + t1 + nl * 2048, 512);
      }
      if (s < 14) {
        dma16x2(dv64, dv64b, wsrc + (28 + 2 * s) * (8 * 896), lds0 + ring + (2 * s) * 1024);
        asm volatile("s_waitcnt vmcnt(%2)" : "+v"(af[s][0]), "+v"(af[s][1]) : "i"(2 * (13 - s) + 4 * s + 2) : "memory");
      } else {
        if (s < 21) {
          dma16x2(dv64, dv64b, wosrc + (2 * sl) * (8 * 896), lds0 + ring + (2 * sl) * 1024);
        }
        asm volatile("s_waitcnt vmcnt(%2)" : "+v"(af[sl][0]), "+v"(af[sl][1]) : "i"(4 * (13 - sl) + 2 * (s - 13 < 7 ? s - 13 : 7)) : "memory");
      }
      t[0] = mfma(f0[s & 1], af[sl][0], t[0]); t[1] = mfma(f1[s & 1], af[sl][0], t[1]);
      t[2] = mfma(f0[s & 1], af[sl][1], t[2]); t[3] = mfma(f1[s & 1], af[sl][1], t[3]);
      if (s < 14) {
        BLOAD_SC1(af[s][0], av[0], r3, ((s + 14) >> 2) * 4096, ((s + 14) & 3) * 1024);
        BLOAD_SC1(af[s][1], av[1], r3, ((s + 14) >> 2) * 4096, ((s + 14) & 3) * 1024);
      }
      __builtin_amdgcn_sched_barrier(0);
    }
    flags_wait(ws + WS_GO + ((tb64 >> 1) * 16 + 4 * w + (lane & 3)) * 128);
    asm volatile("s_waitcnt vmcnt(0)" ::: "memory");
    {
      const u32x4 ro = mkrsrc(ws + WS_O, TBYTES);
      unsigned ov[2];
#pragma unroll
      for (int e = 0; e < 2; e++) ov[e] = (unsigned)((tb64 * 2 + (tj ^ e)) * 56 + 14 * w) * 512 + lane * 16;
#pragma unroll
      for (int s = 0; s < 7; s++)
#pragma unroll
        for (int e = 0; e < 2; e++) BLOAD_SC1(af[s][e], ov[e], ro, (s >> 2) * 4096, (s & 3) * 1024);
      u32x4 g0[7], g1[7];
#pragma unroll
      for (int s = 0; s < 7; s++) { g0[s] = tr_frag(lds + ring + t0 + s * 2048, 512); g1[s] = tr_frag(lds + ring + t1 + s * 2048, 512); }
#pragma unroll
      for (int s = 0; s < 7; s++) {
        asm volatile("s_waitcnt vmcnt(%2)" : "+v"(af[s][0]), "+v"(af[s][1]) : "i"(2 * (6 - s)) : "memory");
        t[0] = mfma(g0[s], af[s][0], t[0]); t[1] = mfma(g1[s], af[s][0], t[1]);
        t[2] = mfma(g0[s], af[s][1], t[2]); t[3] = mfma(g1[s], af[s][1], t[3]);
      }
    }
    asm volatile("s_waitcnt lgkmcnt(0)" ::: "memory");
#pragma unroll
    for (int i = 1; i < 4; i++) tile_put(lds + ring + (i - 1) * 4096, t[i], lane);
    gsync(syb, w, ++eb);
#pragma unroll
    for (int i = 1; i < 4; i++) tile_add(lds + K2_B_OFF + (w ^ i) * 28672 + (i - 1) * 4096, t[0], lane);
    unsigned pk[8];
    const float og = 1.0f - gate;
#pragma unroll
    for (int j = 0; j < 8; j++)
      pk[j] = pack2(__builtin_fmaf(og, __builtin_bit_cast(float, x1p[j] << 16), t[0][2 * j]), __builtin_fmaf(og, __builtin_bit_cast(float, x1p[j] & 0xffff0000u), t[0][2 * j + 1]));
    char* op = out + (size_t)((tb64 * 2 + tj) * 32 + r) * 896 + (nb * 64 + ni * 32) * 2 + hh * 16;
#pragma unroll
    for (int gp = 0; gp < 2; gp++) {
      auto a2 = __builtin_amdgcn_permlane32_swap(pk[4 * gp], pk[4 * gp + 2], false, false);
      auto c2 = __builtin_amdgcn_permlane32_swap(pk[4 * gp + 1], pk[4 * gp + 3], false, false);
      u32x4 v; v[0] = a2[0]; v[1] = c2[0]; v[2] = a2[1]; v[3] = c2[1];
      *(u32x4*)(op + gp * 32) = v;
    }
  }
}

extern "C" void kernel_launch(void* const* d_in, const int* in_sizes, int n_in,
                              void* d_out, int out_size, void* d_ws, size_t ws_size,
                              hipStream_t stream) {
  const char* x = (const char*)d_in[0]; const char* wqkv = (const char*)d_in[1]; const char* wo = (const char*)d_in[2];
  const char* w1 = (const char*)d_in[4]; const char* w2 = (const char*)d_in[5]; const char* w3 = (const char*)d_in[6];
  const char* w4 = (const char*)d_in[7]; const char* gw = (const char*)d_in[8];
  (void)hipFuncSetAttribute((const void*)fused_kernel, hipFuncAttributeMaxDynamicSharedMemorySize, K2_LDS);
  fused_kernel<<<256, 512, K2_LDS, stream>>>(x, wqkv, w1, gw, wo, w2, w3, w4, (char*)d_ws, (char*)d_out);
}
```

```cpp
#include <hip/hip_runtime.h>
#include <stdint.h>

#define CODE_LINES 400

typedef __attribute__((ext_vector_type(8))) __bf16 bf16x8;
typedef __attribute__((ext_vector_type(2))) __bf16 bf16x2;
typedef __attribute__((ext_vector_type(4))) short s16x4;
typedef __attribute__((ext_vector_type(16))) float f32x16;
typedef __attribute__((ext_vector_type(4))) float f32x4;
typedef __attribute__((ext_vector_type(2))) float f32x2;
typedef __attribute__((ext_vector_type(4))) unsigned u32x4;
typedef __attribute__((ext_vector_type(2))) unsigned u32x2;
#define LAS __attribute__((address_space(3)))
#define GAS __attribute__((address_space(1)))
#define DI __device__ __forceinline__

constexpr int SEQ = 2048, DM = 448, NH = 16, DH = 28, FF = 1792;

constexpr size_t WS_CNT   = 0;
constexpr size_t WS_GATE  = 16384;
constexpr size_t WS_STATS = 32768;
constexpr size_t WS_Q     = 1u << 20;
constexpr size_t WS_K     = WS_Q + (2u << 20);
constexpr size_t WS_V     = WS_K + (2u << 20);
constexpr size_t WS_H     = WS_V + (2u << 20);
constexpr size_t WS_X1    = WS_H + (2u << 20);
constexpr size_t WS_O     = WS_X1 + (2u << 20);
constexpr size_t WS_H3    = WS_O + (2u << 20);
constexpr unsigned TBYTES = 64u * 56u * 512u;
constexpr unsigned T3BYTES = 64u * 224u * 512u;

DI f32x16 mfma(u32x4 a, u32x4 b, f32x16 c) {
  return __builtin_amdgcn_mfma_f32_32x32x16_bf16(__builtin_bit_cast(bf16x8, a), __builtin_bit_cast(bf16x8, b), c, 0, 0, 0);
}
DI unsigned pack2(float a, float b) {
  f32x2 v; v[0] = a; v[1] = b;
  return __builtin_bit_cast(unsigned, __builtin_convertvector(v, bf16x2));
}
DI u32x4 tr_frag(const LAS char* p, int hi_off) {
  s16x4 lo = __builtin_amdgcn_ds_read_tr16_b64_v4i16((LAS s16x4*)p);
  s16x4 hi = __builtin_amdgcn_ds_read_tr16_b64_v4i16((LAS s16x4*)(p + hi_off));
  u32x2 a = __builtin_bit_cast(u32x2, lo), b = __builtin_bit_cast(u32x2, hi);
  u32x4 r; r[0] = a[0]; r[1] = a[1]; r[2] = b[0]; r[3] = b[1];
  return r;
}
DI unsigned gelu2(float a0, float a1, float c0, float c1, float nmean, float ga, float gb, float irs) {
  const f32x2 a = {a0, a1}, c = {c0, c1}, nm = {nmean, nmean}, ga2 = {ga, ga}, gb2 = {gb, gb}, ir = {irs, irs};
  const f32x2 t = __builtin_elementwise_fma(nm, c, a);
  const f32x2 z = t * __builtin_elementwise_fma(t * t, gb2, ga2);
  f32x2 e; e[0] = __builtin_amdgcn_exp2f(z[0]); e[1] = __builtin_amdgcn_exp2f(z[1]);
  const f32x2 d = __builtin_elementwise_fma(e, ir, ir);
  f32x2 rr; rr[0] = __builtin_amdgcn_rcpf(d[0]); rr[1] = __builtin_amdgcn_rcpf(d[1]);
  const f32x2 y = t * rr;
  return __builtin_bit_cast(unsigned, __builtin_convertvector(y, bf16x2));
}
DI float other_half(float v, int hh) {
  unsigned u = __builtin_bit_cast(unsigned, v);
  auto r = __builtin_amdgcn_permlane32_swap(u, u, false, false);
  return __builtin_bit_cast(float, hh ? r[0] : r[1]);
}
DI f32x4 sq4(unsigned a, unsigned b, f32x4 c) {
  u32x2 v; v[0] = a; v[1] = b;
  const s16x4 t = __builtin_bit_cast(s16x4, v);
  return __builtin_amdgcn_mfma_f32_4x4x4bf16_1k(t, t, c, 0, 0, 0);
}
DI f32x16 zero16() { f32x16 z; for (int i = 0; i < 16; i++) z[i] = 0.f; return z; }

DI void dma16_masked(unsigned voff, const void* sbase, unsigned ldsdst, uint64_t mask) {
  asm volatile("s_mov_b32 m0, %2\n\ts_mov_b64 exec, %3\n\tglobal_load_lds_dwordx4 %0, %1\n\ts_mov_b64 exec, -1"
               :: "v"(voff), "s"(sbase), "s"(ldsdst), "s"(mask) : "memory");
}
DI void dma16(unsigned voff, const void* sbase, unsigned ldsdst) {
  asm volatile("s_mov_b32 m0, %2\n\ts_nop 0\n\tglobal_load_lds_dwordx4 %0, %1"
               :: "v"(voff), "s"(sbase), "s"(ldsdst) : "memory");
}
DI void dma16x2(unsigned voff0, unsigned voff1, const void* sbase, unsigned ldsdst) {
  asm volatile("s_mov_b32 m0, %3\n\ts_nop 0\n\tglobal_load_lds_dwordx4 %0, %2\n\ts_add_u32 m0, m0, 0x400\n\ts_nop 0\n\tglobal_load_lds_dwordx4 %1, %2"
               :: "v"(voff0), "v"(voff1), "s"(sbase), "s"(ldsdst) : "memory", "scc");
}

constexpr int K1_SLOT    = 32768;
constexpr int K1_AUX_OFF = 151040;
constexpr int K1_CS_OFF  = K1_AUX_OFF + 3584;
constexpr int K1_SQ_OFF  = K1_CS_OFF + 2048;
constexpr int K1_CS2_OFF = K1_SQ_OFF + 1024;
constexpr int K1_LDS     = K1_CS2_OFF + 2048;

DI u32x4 tr_frag2(const LAS char* p0, const LAS char* p1) {
  s16x4 lo = __builtin_amdgcn_ds_read_tr16_b64_v4i16((LAS s16x4*)p0);
  s16x4 hi = __builtin_amdgcn_ds_read_tr16_b64_v4i16((LAS s16x4*)p1);
  u32x2 a = __builtin_bit_cast(u32x2, lo), b = __builtin_bit_cast(u32x2, hi);
  u32x4 r; r[0] = a[0]; r[1] = a[1]; r[2] = b[0]; r[3] = b[1];
  return r;
}
DI unsigned k1_tr_off(int lane, int t) {
  const int hh = lane >> 5, blk = (lane >> 4) & 1, q = (lane & 15) >> 2, p = lane & 3;
  int u = 7 * t + 4 * blk + p;
  if (blk == 1 && p == 3) u = 7 * t;
  return (8 * hh + q) * 256 + (((u >> 1) ^ (q << 2)) * 16) + (u & 1) * 8;
}

constexpr int K2_B_OFF   = 0;
constexpr int K2_A_OFF   = 114688;
constexpr int K2_CSP_OFF = K2_A_OFF + 32768;
constexpr int K2_CS_OFF  = K2_CSP_OFF + 2048;
constexpr int K2_M_OFF   = K2_CS_OFF + 512;
constexpr int K2_SY_OFF  = K2_M_OFF + 512;
constexpr int K2_END     = K2_SY_OFF + 64;
static_assert(K2_END <= K1_AUX_OFF, "lds map");
constexpr int K2_LDS     = K1_LDS;
constexpr unsigned SPIN_CAP = 1u << 20;
#define POLL_SLEEP 4
constexpr size_t WS_FCS = WS_CNT + 12288;
constexpr size_t WS_EPOCH = WS_CNT + 12288 + 1024;
constexpr size_t WS_GX1 = 589824, WS_GH3 = 622592, WS_GO = 655360;
constexpr size_t WS_F1 = 524288;
constexpr size_t WS_CS3 = 24576;

DI void gsync(LAS unsigned* f, int w, unsigned epoch) {
  asm volatile("s_waitcnt lgkmcnt(0)" ::: "memory");
  __hip_atomic_store(f + w, epoch, __ATOMIC_RELAXED, __HIP_MEMORY_SCOPE_WORKGROUP);
  for (unsigned s = 0; s < SPIN_CAP; s++) {
    u32x4 v;
    asm volatile("ds_read_b128 %0, %1\n\ts_waitcnt lgkmcnt(0)" : "=v"(v) : "v"((unsigned)(uintptr_t)f) : "memory");
    const unsigned a = v[0], b = v[1], c = v[2], d = v[3];
    const unsigned m = min(min(a, b), min(c, d));
    if ((unsigned)__builtin_amdgcn_readfirstlane(m) >= epoch) break;
    __builtin_amdgcn_s_sleep(1);
  }
}
DI void flag_set_t(char* p, int lane, unsigned tag) {
  if (lane == 0) __hip_atomic_store((GAS unsigned*)p, tag, __ATOMIC_RELAXED, __HIP_MEMORY_SCOPE_AGENT);
}
#define flag_set(p, lane) flag_set_t(p, lane, tag)
#define flags_wait(p) flags_wait_t(p, tag)
DI char* f1_flag(char* ws, int type, int sub, int tile) { return ws + WS_F1 + ((type * 4 + sub) * 16 + (tile >> 2)) * 128; }
DI void block_publish(LAS unsigned* cnt, char* p, int lane, unsigned tag, unsigned last = 7u) {
  if (lane == 0) {
    const unsigned old = __hip_atomic_fetch_add(cnt, 1u, __ATOMIC_RELAXED, __HIP_MEMORY_SCOPE_WORKGROUP);
    if (old == last) __hip_atomic_store((GAS unsigned*)p, tag, __ATOMIC_RELAXED, __HIP_MEMORY_SCOPE_AGENT);
  }
}
DI void flags_wait_t(const char* p, unsigned tag) {
  for (unsigned s = 0; s < SPIN_CAP; s++) {
    const unsigned v = __hip_atomic_load((const GAS unsigned*)p, __ATOMIC_RELAXED, __HIP_MEMORY_SCOPE_AGENT);
    if (__all(v == tag)) break;
    __builtin_amdgcn_s_sleep(POLL_SLEEP);
  }
  asm volatile("" ::: "memory");
}
DI u32x4 mkrsrc(const void* p, unsigned bytes) {
  const uint64_t a = (uint64_t)(uintptr_t)p;
  u32x4 r; r[0] = (unsigned)a; r[1] = (unsigned)(a >> 32) & 0xffffu; r[2] = bytes; r[3] = 0x00020000u;
  return r;
}
#define BLOAD(dst, voff, rs, soff, imm) \
  asm volatile("buffer_load_dwordx4 %0, %1, %2, %3 offen offset:%4" : "=v"(dst) : "v"(voff), "s"(rs), "s"(soff), "i"(imm) : "memory")
#define BLOAD_SC1(dst, voff, rs, soff, imm) \
  asm volatile("buffer_load_dwordx4 %0, %1, %2, %3 offen offset:%4 sc1" : "=v"(dst) : "v"(voff), "s"(rs), "s"(soff), "i"(imm) : "memory")
DI void st16_sc1(u32x4 v, __amdgpu_buffer_rsrc_t rs, unsigned off) { __builtin_amdgcn_raw_buffer_store_b128(v, rs, off, 0, 16); }
DI void st8_sc1(u32x2 v, __amdgpu_buffer_rsrc_t rs, unsigned off) { __builtin_amdgcn_raw_buffer_store_b64(v, rs, off, 0, 16); }

DI void tile_put(LAS char* p, const f32x16& t, int lane) {
#pragma unroll
  for (int g = 0; g < 4; g++) { f32x4 v; v[0] = t[4 * g]; v[1] = t[4 * g + 1]; v[2] = t[4 * g + 2]; v[3] = t[4 * g + 3]; *(LAS f32x4*)(p + g * 1024 + lane * 16) = v; }
}
DI void tile_add(const LAS char* p, f32x16& t, int lane) {
#pragma unroll
  for (int g = 0; g < 4; g++) { f32x4 v = *(const LAS f32x4*)(p + g * 1024 + lane * 16); t[4 * g] += v[0]; t[4 * g + 1] += v[1]; t[4 * g + 2] += v[2]; t[4 * g + 3] += v[3]; }
}
DI void reduce_scatter(LAS char* buf, f32x16 (&t)[4], int w, int lane, int x0, int x1, LAS unsigned* f, unsigned& epoch) {
  tile_put(buf + (((w ^ x0) * 2 + 0) * 4096), t[2], lane);
  tile_put(buf + (((w ^ x1) * 2 + 1) * 4096), t[3], lane);
  gsync(f, w, ++epoch);
  tile_add(buf + (((w ^ 2 ^ x0) * 2 + 0) * 4096), t[0], lane);
  tile_add(buf + (((w ^ 2 ^ x1) * 2 + 1) * 4096), t[1], lane);
  asm volatile("s_waitcnt lgkmcnt(0)" ::: "memory");
  tile_put(buf + (((w ^ 2 ^ x0) * 2 + 0) * 4096), t[1], lane);
  gsync(f, w, ++epoch);
  tile_add(buf + (((w ^ 3 ^ x0) * 2 + 0) * 4096), t[0], lane);
  asm volatile("s_waitcnt lgkmcnt(0)" ::: "memory");
}

DI unsigned w64_dma_voff(int lane, unsigned ldw) { const int rin = lane >> 3, pc = lane & 7; return rin * ldw + ((pc ^ (((rin >> 1) & 1) << 2)) * 16); }
DI unsigned w64_tr_off(int lane, int nt) {
  const int hh = lane >> 5, blk = (lane >> 4) & 1, q = (lane & 15) >> 2, p = lane & 3;
  return (8 * hh + q) * 128 + ((nt ^ ((q >> 1) & 1)) * 64) + blk * 32 + p * 8;
}

#define ATT_EXP4(SC, B, P, D)                                                                                    \
    SC[B] = __builtin_amdgcn_exp2f(SC[B]); SC[B + 1] = __builtin_amdgcn_exp2f(SC[B + 1]);                       \
    SC[B + 2] = __builtin_amdgcn_exp2f(SC[B + 2]); SC[B + 3] = __builtin_amdgcn_exp2f(SC[B + 3]);               \
    P[D] = pack2(SC[B], SC[B + 1]); P[D + 1] = pack2(SC[B + 2], SC[B + 3]);                                     \
    __builtin_amdgcn_sched_barrier(0);
#define ATT_STEP(SC, SN, PC0, PC1, PP0, PP1, KN0, KN1, QN0, QN1, VP0, VP1, OP)                                  \
  {                                                                                                             \
    SN = mfma(KN0, QN0, zero16());                                                                              \
    __builtin_amdgcn_sched_barrier(0);                                                                          \
    ATT_EXP4(SC, 0, PC0, 0)                                                                                     \
    SN = mfma(KN1, QN1, SN);                                                                                    \
    __builtin_amdgcn_sched_barrier(0);                                                                          \
    ATT_EXP4(SC, 4, PC0, 2)                                                                                     \
    OP = mfma(VP0, PP0, OP);                                                                                    \
    __builtin_amdgcn_sched_barrier(0);                                                                          \
    ATT_EXP4(SC, 8, PC1, 0)                                                                                     \
    OP = mfma(VP1, PP1, OP);                                                                                    \
    __builtin_amdgcn_sched_barrier(0);                                                                          \
    ATT_EXP4(SC, 12, PC1, 2)                                                                                    \
  }

DI float max16(const f32x16& s) {
  float a = fmaxf(fmaxf(s[0], s[1]), s[2]), b = fmaxf(fmaxf(s[3], s[4]), s[5]), c = fmaxf(fmaxf(s[6], s[7]), s[8]);
  float d = fmaxf(fmaxf(s[9], s[10]), s[11]), e = fmaxf(fmaxf(s[12], s[13]), s[14]);
  return fmaxf(fmaxf(fmaxf(a, b), c), fmaxf(fmaxf(d, e), s[15]));
}

template <int KIND>
DI void phase1(LAS char* lds, const char* __restrict__ x, const char* __restrict__ wsrc, const int ldw, const char* __restrict__ gate_w,
               char* __restrict__ ws, const int type, const int sub, const int tg, const int wv, const char* touch) {
  const unsigned lds0 = (unsigned)(uintptr_t)lds;
  const int tid = threadIdx.x, lane = tid & 63, w = wv & 3, np = wv >> 2;
  const int r = lane & 31, hh = lane >> 5;
  const int tile = tg * 4 + w;
  const int drow = lane >> 4, dlc = (lane & 15) ^ (drow << 2);
  const unsigned dvoff = drow * ldw + (dlc < 14 ? dlc : 0) * 16;
  const uint64_t dmask = ~((3ull << 14) | (3ull << (16 + 10)) | (3ull << (32 + 6)) | (3ull << (48 + 2)));
  unsigned xv[2];
  {
    const int rin = lane >> 3, pc = lane & 7;
#pragma unroll
    for (int e = 0; e < 2; e++) xv[e] = rin * 896 + ((pc ^ ((4 * e + (rin >> 1)) & 7)) * 16);
  }
  const char* xbase = x + (size_t)(tg * 128 + w * 32 + np * 16) * 896;
  const char* wbase = wsrc + (size_t)(8 * wv) * ldw;
  auto issue_half = [&](int st, int jj) {
    const unsigned slot = lds0 + (st & 3) * K1_SLOT;
    dma16_masked(dvoff, wbase + (size_t)(64 * st + 4 * jj) * ldw, slot + (8 * wv + 4 * jj) * 256, dmask);
    dma16(xv[jj], xbase + jj * (8 * 896) + st * 128, slot + 16384 + w * 4096 + (2 * np + jj) * 1024);
  };
  unsigned gw0 = 0;
  if (KIND == 2) {
    const unsigned go = (tid < 448 ? tid : 447) * 2;
    asm volatile("global_load_ushort %0, %1, %2" : "=v"(gw0) : "v"(go), "s"(gate_w) : "memory");
  }
  issue_half(0, 0); issue_half(0, 1); issue_half(1, 0); issue_half(1, 1);
  issue_half(2, 0); issue_half(2, 1);
  unsigned xb[4];
#pragma unroll
  for (int ks = 0; ks < 4; ks++) xb[ks] = 16384 + w * 4096 + r * 128 + (((2 * ks + hh) ^ ((r >> 1) & 7)) * 16);
  const unsigned wb0 = k1_tr_off(lane, 2 * np), wb1 = k1_tr_off(lane, 2 * np + 1);
  const bool isaux = ((lane >> 4) & 1) == 1 && (lane & 3) == 3;
  const unsigned ab = isaux ? K1_AUX_OFF + (8 * hh + ((lane & 15) >> 2)) * 8 : wb1;
  const unsigned kstep = isaux ? 128u : 4096u, hoff = isaux ? 32u : 1024u;
  const unsigned sstep = isaux ? 512u : (unsigned)K1_SLOT, wrapst = isaux ? 512u : (unsigned)(-3 * K1_SLOT);
  unsigned a3[4];
#pragma unroll
  for (int ks = 0; ks < 4; ks++) a3[ks] = ab + ks * kstep;
  f32x16 acc[2], cs[2]; acc[0] = zero16(); acc[1] = zero16(); cs[0] = zero16(); cs[1] = zero16();
  u32x4 ones; ones[0] = ones[1] = ones[2] = ones[3] = 0x3F803F80u;
  u32x4 wf[2][4][2], xf[2][4], cf[2][2];
  u32x2 xq[2][4];
  f32x4 sqm = {0.f, 0.f, 0.f, 0.f};
  unsigned sink;
  auto read_part = [&](int st, int ks) {
    const int so = (st & 3) * K1_SLOT;
    xf[st & 1][ks] = *(const LAS u32x4*)(lds + xb[ks] + so);
    if (KIND == 2) xq[st & 1][ks] = *(const LAS u32x2*)(lds + xb[ks] + so + np * 8);
    wf[st & 1][ks][0] = tr_frag(lds + wb0 + so + ks * 4096, 1024);
    if (KIND == 2) { wf[st & 1][ks][1] = tr_frag2(lds + a3[ks], lds + a3[ks] + hoff); a3[ks] += (st == 3) ? wrapst : sstep; }
    else wf[st & 1][ks][1] = tr_frag(lds + wb1 + so + ks * 4096, 1024);
    if (KIND == 2 && ks == 3) { cf[st & 1][0] = tr_frag(lds + wb0 + so + w * 4096, 1024); cf[st & 1][1] = tr_frag(lds + wb1 + so + w * 4096, 1024); }
  };
  auto read_stage = [&](int st) {
#pragma unroll
    for (int ks = 0; ks < 4; ks++) read_part(st, ks);
  };
  asm volatile("s_waitcnt vmcnt(8)" : "+v"(gw0) :: "memory");
  if (KIND == 2) {
    u32x2 a; a[0] = 0x3F80u | (gw0 << 16); a[1] = 0u;
    *(LAS u32x2*)(lds + K1_AUX_OFF + (tid < 448 ? tid : 447) * 8) = a;
  }
  asm volatile("s_waitcnt lgkmcnt(0)\n\ts_barrier" ::: "memory");
  read_stage(0);
  issue_half(3, 0); issue_half(3, 1);
#pragma unroll
  for (int st = 0; st < 7; st++) {
    __builtin_amdgcn_sched_barrier(0);
    if (st < 3)       asm volatile("s_waitcnt vmcnt(8) lgkmcnt(0)\n\ts_barrier" ::: "memory");
    else if (st == 3) asm volatile("s_waitcnt vmcnt(9) lgkmcnt(0)\n\ts_barrier" ::: "memory");
    else if (st == 4) asm volatile("s_waitcnt vmcnt(5) lgkmcnt(0)\n\ts_barrier" ::: "memory");
    else if (st == 5) asm volatile("s_waitcnt vmcnt(1) lgkmcnt(0)\n\ts_barrier" ::: "memory");
    else              asm volatile("s_waitcnt lgkmcnt(0)\n\ts_barrier" ::: "memory");
#pragma unroll
    for (int ks = 0; ks < 4; ks++) {
      acc[0] = KIND == 1 ? mfma(xf[st & 1][ks], wf[st & 1][ks][0], acc[0]) : mfma(wf[st & 1][ks][0], xf[st & 1][ks], acc[0]);
      __builtin_amdgcn_sched_barrier(0);
      if (st < 6 && ks < 2) read_part(st + 1, 2 * ks);
      if (KIND == 2 && ks < 2) { sqm = sq4(xq[st & 1][2 * ks][0], xq[st & 1][2 * ks][1], sqm); }
      __builtin_amdgcn_sched_barrier(0);
      acc[1] = KIND == 1 ? mfma(xf[st & 1][ks], wf[st & 1][ks][1], acc[1]) : mfma(wf[st & 1][ks][1], xf[st & 1][ks], acc[1]);
      __builtin_amdgcn_sched_barrier(0);
      if (st < 6 && ks < 2) read_part(st + 1, 2 * ks + 1);
      if (KIND == 2 && ks < 2) { sqm = sq4(xq[st & 1][2 * ks + 1][0], xq[st & 1][2 * ks + 1][1], sqm); }
      if (st + 4 < 7 && ks >= 2) issue_half(st + 4, ks - 2);
      if (st == 2 && ks == 3) asm volatile("global_load_dword %0, %1, off" : "=v"(sink) : "v"(touch) : "memory");
      __builtin_amdgcn_sched_barrier(0);
    }
    if (KIND == 2) {
      cs[0] = mfma(ones, cf[st & 1][0], cs[0]); cs[1] = mfma(ones, cf[st & 1][1], cs[1]);
    }
  }
  __builtin_amdgcn_sched_barrier(0);
  asm volatile("s_waitcnt vmcnt(0)" : "+v"(sink) :: "memory");
  if (KIND < 2) {
    const float sc = type == 0 ? 0.27264314f : 1.0f;
    const __amdgpu_buffer_rsrc_t rq = __builtin_amdgcn_make_buffer_rsrc(ws + WS_Q, 0, 6u << 20, 0x00020000);
#pragma unroll
    for (int j = 0; j < 2; j++) {
      const int head = sub * 4 + 2 * np + j;
      u32x4 f0, f1;
#pragma unroll
      for (int d = 0; d < 4; d++) {
        f0[d] = pack2(acc[j][2 * d] * sc, acc[j][2 * d + 1] * sc);
        f1[d] = pack2(acc[j][8 + 2 * d] * sc, acc[j][8 + 2 * d + 1] * sc);
      }
      if (KIND == 1) {
        const unsigned v = r == 28 ? 0x3F803F80u : 0u;
#pragma unroll
        for (int d = 0; d < 4; d++) { f0[d] = r >= 28 ? v : f0[d]; f1[d] = r >= 28 ? v : f1[d]; }
      } else {
        f1[2] = hh ? (type == 1 ? 0x3F803F80u : 0u) : f1[2];
        f1[3] = hh ? (type == 1 ? 0x00003F80u : 0u) : f1[3];
      }
      const unsigned o = (unsigned)type * (2u << 20) + (unsigned)(head * 64 + tile) * 2048 + lane * 16;
      st16_sc1(f0, rq, o); st16_sc1(f1, rq, o + 1024);
    }
  } else {
    float sq = (lane & 2) ? ((lane & 1) ? sqm[3] : sqm[2]) : ((lane & 1) ? sqm[1] : sqm[0]);
    sq += other_half(sq, hh);
    if (hh == 0) {
      *(LAS float*)(lds + K1_SQ_OFF + ((np * 4 + w) * 32 + r) * 4) = sq;
      *(LAS float*)(lds + K1_CS_OFF + (w * 128 + (2 * np) * 32 + r) * 4) = cs[0][0];
      *(LAS float*)(lds + K1_CS_OFF + (w * 128 + (2 * np + 1) * 32 + r) * 4) = cs[1][0];
    }
    if (sub == 0 && np == 0 && hh == 1) {
      const float gt = __builtin_amdgcn_rcpf(1.0f + __builtin_amdgcn_exp2f(-1.442695041f * acc[1][13]));
      __hip_atomic_store((GAS float*)(ws + WS_GATE) + tile * 32 + r, gt, __ATOMIC_RELAXED, __HIP_MEMORY_SCOPE_AGENT);
    }
    float sx = acc[1][12];
    { float o = other_half(sx, hh); sx = hh ? sx : o; }
    const float mean = sx * (1.0f / 448.0f);
    asm volatile("s_waitcnt lgkmcnt(0)\n\ts_barrier" ::: "memory");
    sq += *(LAS float*)(lds + K1_SQ_OFF + (((np ^ 1) * 4 + w) * 32 + r) * 4);
    {
      const LAS float* cp = (const LAS float*)(lds + K1_CS_OFF) + np * 64 + lane;
      *(LAS float*)(lds + K1_CS2_OFF + (wv * 64 + lane) * 4) = (cp[0] + cp[128]) + (cp[256] + cp[384]);
    }
    const float var = fmaxf(sq * (1.0f / 448.0f) - mean * mean, 0.f) + 1e-5f;
    const float rstd = __builtin_amdgcn_rsqf(var);
    const float ga = -2.302208198f * rstd, gb = -0.1029432397f * rstd * rstd * rstd, irs = var * rstd;
    const __amdgpu_buffer_rsrc_t rh = __builtin_amdgcn_make_buffer_rsrc(ws + WS_H, 0, TBYTES, 0x00020000);
    unsigned pa[2][4][2];
#pragma unroll
    for (int j = 0; j < 2; j++) {
      const int t = 2 * np + j;
#pragma unroll
      for (int g = 0; g < 4; g++) {
        const f32x4 c = *(const LAS f32x4*)(lds + K1_CS2_OFF + (wv * 64 + j * 32 + 8 * g + 4 * hh) * 4);
        pa[j][g][0] = gelu2(acc[j][4 * g], acc[j][4 * g + 1], c[0], c[1], -mean, ga, gb, irs);
        pa[j][g][1] = gelu2(acc[j][4 * g + 2], acc[j][4 * g + 3], c[2], c[3], -mean, ga, gb, irs);
      }
    }
    const unsigned dst = (unsigned)(tile * 56 + 14 * sub + 7 * np) * 512 + r * 16;
    u32x4 v0, v1, v2, v3;
#pragma unroll
    for (int d = 0; d < 2; d++) {
      auto s0 = __builtin_amdgcn_permlane32_swap(pa[0][0][d], pa[0][1][d], false, false);
      v0[d] = s0[0]; v0[2 + d] = s0[1];
      auto s1 = __builtin_amdgcn_permlane32_swap(pa[0][3][d], pa[0][2][d], false, false);
      v1[d] = s1[0]; v1[2 + d] = hh ? pa[0][2][d] : pa[1][0][d];
      auto s2 = __builtin_amdgcn_permlane32_swap(pa[1][0][d], pa[1][2][d], false, false);
      v2[d] = hh ? pa[1][1][d] : s2[1]; v2[2 + d] = hh ? s2[0] : pa[1][1][d];
      auto s3 = __builtin_amdgcn_permlane32_swap(pa[1][2][d], pa[1][2][d], false, false);
      v3[d] = s3[1]; v3[2 + d] = pa[1][3][d];
    }
    st16_sc1(v0, rh, dst + hh * 512);
    st16_sc1(v1, rh, dst + (hh ? 2 : 3) * 512);
    st16_sc1(v2, rh, dst + (4 + hh) * 512);
    st16_sc1(v3, rh, hh ? 0x80000000u : dst + 6 * 512);
  }
}

__global__ __launch_bounds__(512, 2) void fused_kernel(const char* __restrict__ x, const char* __restrict__ wqkv,
                                                       const char* __restrict__ w1, const char* __restrict__ gate_w,
                                                       const char* __restrict__ wo,
                                                       const char* __restrict__ w2, const char* __restrict__ w3,
                                                       const char* __restrict__ w4, char* __restrict__ ws,
                                                       char* __restrict__ out) {
  extern __shared__ __attribute__((aligned(16))) char smem[];
  LAS char* lds = (LAS char*)smem;
  const unsigned lds0 = (unsigned)(uintptr_t)lds;
  LAS unsigned* sy = (LAS unsigned*)(lds + K2_SY_OFF);
  const int tid = threadIdx.x, lane = tid & 63;
  const int wv = __builtin_amdgcn_readfirstlane(tid >> 6);
  const int w = wv & 3;
  const int b = blockIdx.x, xcd = b & 7, bi = b >> 3;
  const int r = lane & 31, hh = lane >> 5;
  const bool has_gemm = bi < 28;
  const int nb = bi % 7, tb64 = xcd * 4 + bi / 7;
  const int ni = w & 1, tj = w >> 1;
  const int p1type = bi < 24 ? bi % 3 : 3, p1tg = (xcd >> 2) * 8 + (bi < 24 ? bi / 3 : bi - 24);
  if (tid < 16) sy[tid] = 0u;
  {
    const int type = p1type, sub = xcd & 3, tg = p1tg;
    int k = (b * 8 + wv) * 16 + (lane & 15); k = k < 31360 ? k : 31359;
    const char* touch = k < 3136 ? w2 + (size_t)k * 128 : k < 15680 ? w3 + (size_t)(k - 3136) * 128 : k < 28224 ? w4 + (size_t)(k - 15680) * 128 : wo + (size_t)(k - 28224) * 128;
    {
      uint64_t pc;
      asm volatile("s_getpc_b64 %0" : "=s"(pc));
      int i = (bi * 8 + wv) * 2 + (lane & 1); i = i < CODE_LINES ? i : 0;
      if (lane >= 32) touch = (const char*)(uintptr_t)((pc & ~127ull) + (uint64_t)i * 128);
    }
    if (type < 2)       phase1<0>(lds, x, wqkv + (type * 448 + sub * 112) * 2, 2688, gate_w, ws, type, sub, tg, wv, touch);
    else if (type == 2) phase1<1>(lds, x, wqkv + (2 * 448 + sub * 112) * 2, 2688, gate_w, ws, type, sub, tg, wv, touch);
    else                phase1<2>(lds, x, w1 + sub * 224, 896, gate_w, ws, type, sub, tg, wv, touch);
  }
#define EPOCH_LOAD unsigned epoch; asm volatile("global_load_dword %0, %1, %2 sc1" : "=v"(epoch) : "v"(0u), "s"(ws + WS_EPOCH) : "memory")
#define EPOCH_WAIT(N) asm volatile("s_waitcnt vmcnt(" #N ")" : "+v"(epoch) :: "memory"); epoch = __builtin_amdgcn_readfirstlane(epoch); \
  const unsigned tag = (epoch * 0x9E3779B1u) ^ 0x5bd1e995u
  char* const my_f1 = f1_flag(ws, p1type, xcd & 3, p1tg * 4);

  if (wv < 4) {
    const int head = 2 * xcd + (bi >> 4), qb = bi & 15;
    EPOCH_LOAD; EPOCH_WAIT(0);
    block_publish(sy + 12, my_f1, lane, tag);
    const __amdgpu_buffer_rsrc_t rq = __builtin_amdgcn_make_buffer_rsrc(ws + WS_Q, 0, 6u << 20, 0x00020000);
    const unsigned qo = (unsigned)(head * 64 + qb * 4) * 2048 + lane * 16;
    const unsigned kho = (2u << 20) + (unsigned)(head * 64) * 2048 + lane * 16, vho = kho + (2u << 20);
    const unsigned ko = kho + w * 16 * 2048, vo = vho + w * 16 * 2048;
#define LDQ(off) __builtin_amdgcn_raw_buffer_load_b128(rq, (off), 0, 16)
    unsigned ea = 0;
    {
      const int hs = head >> 2;
      flags_wait(lane < 8 ? f1_flag(ws, 1 + (lane >> 2), hs, w * 16 + (lane & 3) * 4) : f1_flag(ws, 0, hs, qb * 4));
    }
    __builtin_amdgcn_s_setprio(3);
    u32x4 q[4][2];
#pragma unroll
    for (int i = 0; i < 4; i++) { q[i][0] = LDQ(qo + (w ^ i) * 2048); q[i][1] = LDQ(qo + (w ^ i) * 2048 + 1024); }
    u32x4 kb[4][2], vb[4][2];
#pragma unroll
    for (int j = 0; j < 3; j++) { kb[j][0] = LDQ(ko + j * 2048); kb[j][1] = LDQ(ko + j * 2048 + 1024); vb[j][0] = LDQ(vo + j * 2048); vb[j][1] = LDQ(vo + j * 2048 + 1024); }
    f32x16 o[4];
    {
      LAS float* mx = (LAS float*)(lds + K2_M_OFF);
      f32x16 s = mfma(kb[0][0], q[0][0], zero16());
      s = mfma(kb[0][1], q[0][1], s);
      const float m = max16(s);
      if (hh) mx[w * 32 + r] = m;
      gsync(sy, w, ++ea);
#pragma unroll
      for (int i = 0; i < 4; i++) {
        const float mm = mx[(w ^ i) * 32 + r];
        q[i][1][2] = hh ? (pack2(-mm, 0.f)) : q[i][1][2];
      }
    }
    __builtin_amdgcn_s_setprio(0);
    {
#pragma unroll
      for (int i = 0; i < 4; i++) o[i] = zero16();
      f32x16 sA, sB;
      u32x4 pA0, pA1, pB0, pB1;
      sA = mfma(kb[0][0], q[0][0], zero16());
      sA = mfma(kb[0][1], q[0][1], sA);
      pB0 = pB1 = (u32x4){0u, 0u, 0u, 0u};
      vb[3][0] = vb[3][1] = (u32x4){0u, 0u, 0u, 0u};
#pragma unroll 1
      for (int kq = 0; kq < 4; kq++) {
#pragma unroll
        for (int j = 0; j < 4; j++) {
          const int kt = kq * 4 + j;
          const int jp = (j + 3) & 3, jn = (j + 1) & 3;
          ATT_STEP(sA, sB, pA0, pA1, pB0, pB1, kb[j][0], kb[j][1], q[1][0], q[1][1], vb[jp][0], vb[jp][1], o[3]);
          {
            const unsigned kn = kt + 3 < 16 ? (unsigned)(kt + 3) * 2048u : 0x40000000u;
            __builtin_amdgcn_s_setprio(3);
            kb[jp][0] = LDQ(ko + kn); kb[jp][1] = LDQ(ko + kn + 1024); vb[jp][0] = LDQ(vo + kn); vb[jp][1] = LDQ(vo + kn + 1024);
            __builtin_amdgcn_sched_barrier(0);
            __builtin_amdgcn_s_setprio(0);
          }
          ATT_STEP(sB, sA, pB0, pB1, pA0, pA1, kb[j][0], kb[j][1], q[2][0], q[2][1], vb[j][0], vb[j][1], o[0]);
          ATT_STEP(sA, sB, pA0, pA1, pB0, pB1, kb[j][0], kb[j][1], q[3][0], q[3][1], vb[j][0], vb[j][1], o[1]);
          ATT_STEP(sB, sA, pB0, pB1, pA0, pA1, kb[jn][0], kb[jn][1], q[0][0], q[0][1], vb[j][0], vb[j][1], o[2]);
        }
      }
      o[3] = mfma(vb[3][0], pB0, o[3]);
      o[3] = mfma(vb[3][1], pB1, o[3]);
    }
    __builtin_amdgcn_s_setprio(3);
    flags_wait(f1_flag(ws, 3, 0, qb * 4));
    const float gate_q = __hip_atomic_load((const GAS float*)(ws + WS_GATE) + (qb * 4 + w) * 32 + r, __ATOMIC_RELAXED, __HIP_MEMORY_SCOPE_AGENT);
    reduce_scatter(lds + K2_A_OFF, o, w, lane, 0, 0, sy, ea);
    float l = o[0][12];
    { float t = other_half(l, hh); l = hh ? l : t; }
    {
      float amax = 0.f;
#pragma unroll
      for (int i = 0; i < 12; i++) amax = fmaxf(amax, fabsf(o[0][i]));
      if (!hh) amax = fmaxf(fmaxf(amax, fabsf(o[0][12])), fmaxf(fabsf(o[0][13]), fmaxf(fabsf(o[0][14]), fabsf(o[0][15]))));
      const bool ok = (l > 1.0e-10f) && (l < 1.0e30f) && (amax < 1.0e37f);
      if (__any(!ok)) {
        flags_wait(f1_flag(ws, 1 + (lane >> 5), head >> 2, (lane & 15) * 4));
        u32x4 q0 = q[0][0], q1 = q[0][1];
        q1[2] = hh ? 0u : q1[2];
        float tm = -3.0e38f;
#pragma unroll 1
        for (int kt = 0; kt < 64; kt++) {
          f32x16 s = mfma(LDQ(kho + kt * 2048), q0, zero16());
          s = mfma(LDQ(kho + kt * 2048 + 1024), q1, s);
          tm = fmaxf(tm, max16(s));
        }
        tm = -fmaxf(tm, other_half(tm, hh));
        const unsigned h0 = pack2(tm, 0.f) & 0xffffu; const float f0 = __builtin_bit_cast(float, h0 << 16);
        const unsigned h1 = pack2(tm - f0, 0.f) & 0xffffu; const float f1 = __builtin_bit_cast(float, h1 << 16);
        const unsigned h2 = pack2((tm - f0) - f1, 0.f) & 0xffffu;
        q1[2] = hh ? (h0 | (h1 << 16)) : q1[2];
        q1[3] = hh ? h2 : q1[3];
        f32x16 oo = zero16();
#pragma unroll 1
        for (int kt = 0; kt < 64; kt++) {
          f32x16 s = mfma(LDQ(kho + kt * 2048), q0, zero16());
          s = mfma(LDQ(kho + kt * 2048 + 1024), q1, s);
          u32x4 p0, p1;
#pragma unroll
          for (int d = 0; d < 4; d++) {
            p0[d] = pack2(__builtin_amdgcn_exp2f(s[2 * d]), __builtin_amdgcn_exp2f(s[2 * d + 1]));
            p1[d] = pack2(__builtin_amdgcn_exp2f(s[8 + 2 * d]), __builtin_amdgcn_exp2f(s[8 + 2 * d + 1]));
          }
          oo = mfma(LDQ(vho + kt * 2048), p0, oo);
          oo = mfma(LDQ(vho + kt * 2048 + 1024), p1, oo);
        }
        o[0] = oo;
        l = oo[12];
        { float t = other_half(l, hh); l = hh ? l : t; }
      }
    }
    {
      const __amdgpu_buffer_rsrc_t ro = __builtin_amdgcn_make_buffer_rsrc(ws + WS_O, 0, TBYTES, 0x00020000);
      const float inv = gate_q * __builtin_amdgcn_rcpf(l);
      const unsigned base = (unsigned)(qb * 4 + w) * (56 * 512) + r * 16;
#pragma unroll
      for (int g = 0; g < 4; g++) {
        u32x2 v; v[0] = pack2(o[0][4 * g] * inv, o[0][4 * g + 1] * inv); v[1] = pack2(o[0][4 * g + 2] * inv, o[0][4 * g + 3] * inv);
        const int u = 7 * head + 2 * g + hh;
        if (g < 3 || hh == 0) st8_sc1(v, ro, base + (u >> 1) * 512 + (u & 1) * 8);
      }
    }
    asm volatile("s_waitcnt vmcnt(0)" ::: "memory");
    block_publish(sy + 13, ws + WS_GO + (qb * 16 + head) * 128, lane, tag, 3u);
    return;
  }

  if (!has_gemm) {
    EPOCH_LOAD; EPOCH_WAIT(0);
    block_publish(sy + 12, my_f1, lane, tag);
    if (b == 224 && w == 3) {
#pragma unroll 1
      for (int i = 0; i < 4; i++) flags_wait(ws + WS_F1 + (i * 64 + lane) * 128);
      if (lane == 0) __hip_atomic_store((GAS unsigned*)(ws + WS_EPOCH), epoch + 1u, __ATOMIC_RELAXED, __HIP_MEMORY_SCOPE_AGENT);
    }
    const int id = (xcd * 4 + bi - 28) * 2 + w;
    if (w < 2 && id < 56) {
      const unsigned reg = K2_B_OFF + w * 28672;
      const unsigned dv = (lane >> 2) * 3584 + (lane & 3) * 16;
      const char* src = w3 + id * 64;
#pragma unroll
      for (int j = 0; j < 28; j++) dma16(dv, src + (size_t)j * (16 * 3584), lds0 + reg + j * 1024);
      asm volatile("s_waitcnt vmcnt(0)" ::: "memory");
      const unsigned to = reg + (8 * hh + ((lane & 15) >> 2)) * 64 + ((lane >> 4) & 1) * 32 + (lane & 3) * 8;
      u32x4 ones; ones[0] = ones[1] = ones[2] = ones[3] = 0x3F803F80u;
      f32x16 cs = zero16();
#pragma unroll
      for (int s = 0; s < 28; s++) cs = mfma(ones, tr_frag(lds + to + s * 1024, 256), cs);
      if (hh == 0) __hip_atomic_store((GAS float*)(ws + WS_CS3) + id * 32 + r, cs[0], __ATOMIC_RELAXED, __HIP_MEMORY_SCOPE_AGENT);
      asm volatile("s_waitcnt vmcnt(0)" ::: "memory");
      flag_set(ws + WS_FCS + id * 4, lane);
    }
    return;
  }
  __builtin_amdgcn_s_setprio(2);
  unsigned eb = 0;
  LAS unsigned* syb = sy + 4;
  const int nb3 = bi % 14, tb128 = xcd * 2 + bi / 14;
  const unsigned dv64 = w64_dma_voff(lane, 896), dv64b = dv64 + 8 * 896;
  const unsigned t0 = w64_tr_off(lane, ni), t1 = w64_tr_off(lane, ni ^ 1);
  const unsigned dv3 = (lane >> 4) * 3584 + (((lane & 15) ^ ((lane >> 4) << 2)) * 16);
  const unsigned dv3b = dv3 + 4 * 3584;
  const char* w3src = w3 + nb3 * 256;
  const unsigned ring = K2_B_OFF + w * 28672;
  const char* w4src = w4 + (size_t)(448 * w) * 896 + nb * 128;
  unsigned x1p[8];
  const unsigned sl = K2_B_OFF + 57344 + w * 14336;
  EPOCH_LOAD;
  EPOCH_WAIT(0);
  block_publish(sy + 12, my_f1, lane, tag);
  {
    const char* wsrc = w2 + (size_t)(112 * w) * 896 + nb * 128;
#pragma unroll
    for (int j = 0; j < 14; j += 2) dma16x2(dv64, dv64b, wsrc + j * (8 * 896), lds0 + sl + j * 1024);
  }
#pragma unroll
  for (int j = 0; j < 14; j += 2) { const int row0 = 56 * w + 4 * j; dma16x2(dv3, dv3b, w3src + (size_t)row0 * 3584, lds0 + K2_B_OFF + row0 * 256); }
  {
    const u32x4 rh = mkrsrc(ws + WS_H, TBYTES);
    const int delta = w == 0 ? -1 : w == 1 ? 1 : w == 2 ? -2 : 2;
    u32x4 af[7][2];
    unsigned av[2];
#pragma unroll
    for (int e = 0; e < 2; e++) {
      const int tok = (tb64 * 2 + (tj ^ e)) * 32 + r + delta;
      av[e] = (tok >= 0 && tok < SEQ) ? (unsigned)(((tok >> 5) * 56 + 14 * w + hh) * 512 + (tok & 31) * 16) : 0x80000000u;
    }
    {
      int tl = tb64 * 2 - 1 + (lane & 3); tl = tl < 0 ? 0 : tl > 63 ? 63 : tl;
      flags_wait(f1_flag(ws, 3, w, tl));
    }
    asm volatile("s_waitcnt vmcnt(0)" ::: "memory");
#pragma unroll
    for (int s = 0; s < 7; s++) {
      BLOAD_SC1(af[s][0], av[0], rh, (s >> 2) * 4096, (s & 3) * 1024);
      BLOAD_SC1(af[s][1], av[1], rh, (s >> 2) * 4096, (s & 3) * 1024);
    }
    u32x4 xw[2];
    {
      const char* xp = x + (size_t)((tb64 * 2 + tj) * 32) * 896 + (nb * 64 + ni * 32) * 2;
      const unsigned xo = r * 896 + hh * 16;
#pragma unroll
      for (int gp = 0; gp < 2; gp++) asm volatile("global_load_dwordx4 %0, %1, %2 offset:%3" : "=v"(xw[gp]) : "v"(xo), "s"(xp), "i"(gp * 32) : "memory");
    }
    f32x16 t[4];
#pragma unroll
    for (int i = 0; i < 4; i++) t[i] = zero16();
    u32x4 f0[7], f1[7];
#pragma unroll
    for (int s = 0; s < 7; s++) { f0[s] = tr_frag(lds + sl + t0 + s * 2048, 512); f1[s] = tr_frag(lds + sl + t1 + s * 2048, 512); }
#pragma unroll
    for (int s = 0; s < 7; s++) {
      asm volatile("s_waitcnt vmcnt(%2)" : "+v"(af[s][0]), "+v"(af[s][1]) : "i"(14 - 2 * s) : "memory");
      t[0] = mfma(f0[s], af[s][0], t[0]); t[1] = mfma(f1[s], af[s][0], t[1]);
      t[2] = mfma(f0[s], af[s][1], t[2]); t[3] = mfma(f1[s], af[s][1], t[3]);
      __builtin_amdgcn_sched_barrier(0);
    }
    asm volatile("s_waitcnt vmcnt(0) lgkmcnt(0)" : "+v"(xw[0]), "+v"(xw[1]) :: "memory");
    u32x2 xr[4];
#pragma unroll
    for (int gp = 0; gp < 2; gp++)
#pragma unroll
      for (int d = 0; d < 2; d++) {
        const unsigned lo = xw[gp][d], hi = xw[gp][2 + d];
        auto sw = __builtin_amdgcn_permlane32_swap(lo, hi, false, false);
        xr[2 * gp][d] = sw[0]; xr[2 * gp + 1][d] = sw[1];
      }
#pragma unroll
    for (int i = 1; i < 4; i++) tile_put(lds + sl + (i - 1) * 4096, t[i], lane);
    gsync(syb, w, ++eb);
#pragma unroll
    for (int i = 1; i < 4; i++) tile_add(lds + K2_B_OFF + 57344 + (w ^ i) * 14336 + (i - 1) * 4096, t[0], lane);
    float s1 = 0.f, s2 = 0.f;
#pragma unroll
    for (int g = 0; g < 4; g++) {
      const float a0 = __builtin_bit_cast(float, xr[g][0] << 16) + t[0][4 * g], a1 = __builtin_bit_cast(float, xr[g][0] & 0xffff0000u) + t[0][4 * g + 1];
      const float a2 = __builtin_bit_cast(float, xr[g][1] << 16) + t[0][4 * g + 2], a3 = __builtin_bit_cast(float, xr[g][1] & 0xffff0000u) + t[0][4 * g + 3];
      x1p[2 * g] = pack2(a0, a1); x1p[2 * g + 1] = pack2(a2, a3);
    }
    const __amdgpu_buffer_rsrc_t rx = __builtin_amdgcn_make_buffer_rsrc(ws + WS_X1, 0, TBYTES, 0x00020000);
    const int tile = tb64 * 2 + tj;
#pragma unroll
    for (int gp = 0; gp < 2; gp++) {
      auto a = __builtin_amdgcn_permlane32_swap(x1p[4 * gp], x1p[4 * gp + 2], false, false);
      auto c = __builtin_amdgcn_permlane32_swap(x1p[4 * gp + 1], x1p[4 * gp + 3], false, false);
      u32x4 v; v[0] = a[0]; v[1] = c[0]; v[2] = a[1]; v[3] = c[1];
      st16_sc1(v, rx, (unsigned)(tile * 56 + nb * 8 + ni * 4 + 2 * gp + hh) * 512 + r * 16);
    }
#pragma unroll
    for (int j = 0; j < 8; j++) {
      const float lo = __builtin_bit_cast(float, x1p[j] << 16), hi = __builtin_bit_cast(float, x1p[j] & 0xffff0000u);
      s1 += lo + hi; s2 = __builtin_fmaf(lo, lo, s2); s2 = __builtin_fmaf(hi, hi, s2);
    }
    s1 += other_half(s1, hh); s2 += other_half(s2, hh);
    const __amdgpu_buffer_rsrc_t rs = __builtin_amdgcn_make_buffer_rsrc(ws + WS_STATS, 0, 64 * 14 * 32 * 8, 0x00020000);
    { u32x2 v; v[0] = __builtin_bit_cast(unsigned, s1); v[1] = __builtin_bit_cast(unsigned, s2); st8_sc1(v, rs, hh ? 0x80000000u : (unsigned)((tile * 14 + nb * 2 + ni) * 32 + r) * 8); }
    gsync(syb, w, ++eb);
#pragma unroll
    for (int j = 0; j < 14; j += 2) { const int row0 = 224 + 56 * w + 4 * j; dma16x2(dv3, dv3b, w3src + (size_t)row0 * 3584, lds0 + K2_B_OFF + row0 * 256); }
    asm volatile("s_waitcnt vmcnt(14)" ::: "memory");
    block_publish(sy + 14, ws + WS_GX1 + (tb64 * 8 + nb) * 128, lane, tag, 3u);
  }
  {
    asm volatile("s_waitcnt vmcnt(0)" ::: "memory");
    gsync(syb, w, ++eb);
    unsigned tb[4];
    {
      const int blk = (lane >> 4) & 1, q = (lane & 15) >> 2, p = lane & 3;
#pragma unroll
      for (int n = 0; n < 4; n++) tb[n] = K2_B_OFF + (8 * hh + q) * 256 + ((n ^ q) * 64) + blk * 32 + p * 8;
    }
    const int tile = tb128 * 4 + w;
    flags_wait(lane < 16 ? ws + WS_GX1 + ((tile >> 1) * 8 + (lane < 7 ? lane : 0)) * 128 : lane < 32 ? ws + WS_FCS + (nb3 * 4 + (lane & 3)) * 4
               : f1_flag(ws, 3, 0, lane < 48 ? tile : tb64 * 2 + tj));
    const u32x4 rx = mkrsrc(ws + WS_X1, TBYTES);
    const u32x4 rsd = mkrsrc(ws + WS_STATS, 64 * 14 * 32 * 8);
    const u32x4 rcd = mkrsrc(ws + WS_CS3, 1792 * 4);
    u32x2 sv[7], cv; float g3;
    {
      const unsigned so = (unsigned)((tile * 14 + hh * 7) * 32 + r) * 8, co = (unsigned)(nb3 * 128 + lane * 2) * 4;
#pragma unroll
      for (int j = 0; j < 7; j++) asm volatile("buffer_load_dwordx2 %0, %1, %2, 0 offen offset:%3 sc1" : "=v"(sv[j]) : "v"(so), "s"(rsd), "i"(j * 256) : "memory");
      asm volatile("buffer_load_dwordx2 %0, %1, %2, 0 offen sc1" : "=v"(cv) : "v"(co), "s"(rcd) : "memory");
      const unsigned go = (unsigned)(tile * 32 + r) * 4;
      asm volatile("global_load_dword %0, %1, %2 sc1" : "=v"(g3) : "v"(go), "s"(ws + WS_GATE) : "memory");
    }
    constexpr int AW = 16;
    u32x4 af[AW];
    const unsigned av = (unsigned)(tile * 56) * 512 + lane * 16;
#pragma unroll
    for (int s = 0; s < AW; s++) BLOAD_SC1(af[s], av, rx, (s >> 2) * 4096, (s & 3) * 1024);
    const __amdgpu_buffer_rsrc_t r3 = __builtin_amdgcn_make_buffer_rsrc(ws + WS_H3, 0, T3BYTES, 0x00020000);
    u32x4 wfr[3][4];
#pragma unroll
    for (int k = 0; k < 2; k++)
#pragma unroll
      for (int n = 0; n < 4; n++) wfr[k][n] = tr_frag(lds + tb[n] + k * 4096, 1024);
    f32x16 acc[4];
#pragma unroll
    for (int n = 0; n < 4; n++) acc[n] = zero16();
#pragma unroll
    for (int s = 0; s < 28; s++) {
      asm volatile("s_waitcnt vmcnt(%1)" : "+v"(af[s % AW]) : "i"(s + AW <= 28 ? AW - 1 : 27 - s) : "memory");
#pragma unroll
      for (int n = 0; n < 4; n++) {
        acc[n] = mfma(wfr[s % 3][n], af[s % AW], acc[n]);
        if (s + 2 < 28) wfr[(s + 2) % 3][n] = tr_frag(lds + tb[n] + (s + 2) * 4096, 1024);
        __builtin_amdgcn_sched_barrier(0);
      }
      if (s + AW < 28) BLOAD_SC1(af[s % AW], av, rx, ((s + AW) >> 2) * 4096, ((s + AW) & 3) * 1024);
      __builtin_amdgcn_sched_barrier(0);
    }
    asm volatile("" : "+v"(sv[0]), "+v"(sv[1]), "+v"(sv[2]), "+v"(sv[3]), "+v"(sv[4]), "+v"(sv[5]), "+v"(sv[6]), "+v"(cv), "+v"(g3));
    const unsigned csl = K2_CSP_OFF + w * 512;
    *(LAS u32x2*)(lds + csl + lane * 8) = cv;
    float nmean, ga, gb, irs;
    {
      float s1 = 0.f, s2 = 0.f;
#pragma unroll
      for (int j = 0; j < 7; j++) { const unsigned u0 = sv[j][0], u1 = sv[j][1]; s1 += __builtin_bit_cast(float, u0); s2 += __builtin_bit_cast(float, u1); }
      s1 += other_half(s1, hh); s2 += other_half(s2, hh);
      const float mean = s1 * (1.0f / 448.0f);
      const float var = fmaxf(s2 * (1.0f / 448.0f) - mean * mean, 0.f) + 1e-5f;
      const float rstd = __builtin_amdgcn_rsqf(var);
      nmean = -mean; ga = -2.302208198f * rstd; gb = -0.1029432397f * rstd * rstd * rstd;
      irs = var * rstd * __builtin_amdgcn_rcpf(fmaxf(1.0f - g3, 1.0e-30f));
    }
#pragma unroll
    for (int n = 0; n < 4; n++) {
#pragma unroll
      for (int gp = 0; gp < 2; gp++) {
        unsigned pk[4];
#pragma unroll
        for (int g2 = 0; g2 < 2; g2++) {
          const int g = 2 * gp + g2;
          const f32x4 c4 = *(const LAS f32x4*)(lds + csl + (n * 32 + 8 * g + 4 * hh) * 4);
          pk[2 * g2] = gelu2(acc[n][4 * g], acc[n][4 * g + 1], c4[0], c4[1], nmean, ga, gb, irs);
          pk[2 * g2 + 1] = gelu2(acc[n][4 * g + 2], acc[n][4 * g + 3], c4[2], c4[3], nmean, ga, gb, irs);
        }
        auto a = __builtin_amdgcn_permlane32_swap(pk[0], pk[2], false, false);
        auto c = __builtin_amdgcn_permlane32_swap(pk[1], pk[3], false, false);
        u32x4 v; v[0] = a[0]; v[1] = c[0]; v[2] = a[1]; v[3] = c[1];
        st16_sc1(v, r3, (unsigned)(tile * 224 + nb3 * 16 + n * 4 + 2 * gp + hh) * 512 + r * 16);
      }
    }
    gsync(syb, w, ++eb);
    asm volatile("s_waitcnt vmcnt(0)" ::: "memory");
    block_publish(sy + 15, ws + WS_GH3 + (tb128 * 16 + nb3) * 128, lane, tag, 3u);
  }
  {
    const char* wsrc = w4src;
#pragma unroll
    for (int j = 0; j < 28; j += 2) dma16x2(dv64, dv64b, wsrc + j * (8 * 896), lds0 + ring + j * 1024);
    const float gate = __hip_atomic_load((const GAS float*)(ws + WS_GATE) + (tb64 * 2 + tj) * 32 + r, __ATOMIC_RELAXED, __HIP_MEMORY_SCOPE_AGENT);
    {
      const int blk0 = (7 * w) >> 1;
      const int k = lane & 3;
      flags_wait(ws + WS_GH3 + ((tb64 >> 1) * 16 + blk0 + k) * 128);
    }
    asm volatile("s_waitcnt vmcnt(0)" ::: "memory");
    const u32x4 r3 = mkrsrc(ws + WS_H3, T3BYTES);
    unsigned av[2];
#pragma unroll
    for (int e = 0; e < 2; e++) av[e] = (unsigned)((tb64 * 2 + (tj ^ e)) * 224 + 56 * w) * 512 + lane * 16;
    u32x4 af[14][2];
#pragma unroll
    for (int s = 0; s < 14; s++)
#pragma unroll
      for (int e = 0; e < 2; e++) BLOAD_SC1(af[s][e], av[e], r3, (s >> 2) * 4096, (s & 3) * 1024);
    f32x16 t[4];
#pragma unroll
    for (int i = 0; i < 4; i++) t[i] = zero16();
    u32x4 f0[2], f1[2];
    const char* wosrc = wo + (size_t)(112 * w) * 896 + nb * 128;
    f0[0] = tr_frag(lds + ring + t0, 512); f1[0] = tr_frag(lds + ring + t1, 512);
#pragma unroll
    for (int s = 0; s < 28; s++) {
      const int sl = s % 14;
      asm volatile("s_waitcnt lgkmcnt(0)" : "+v"(f0[s & 1]), "+v"(f1[s & 1]) :: "memory");
      if (s + 1 < 28) {
        const int nl = (s + 1) % 14;
        if (s == 13) asm volatile("s_waitcnt vmcnt(50)" ::: "memory");
        else if (s > 13) asm volatile("s_waitcnt vmcnt(%0)" :: "i"(2 + 4 * (13 - nl) + 2 * (s - 14 < 7 ? s - 14 : 7)) : "memory");
        f0[(s + 1) & 1] = tr_frag(lds + ring + t0 + nl * 2048, 512); f1[(s + 1) & 1] = tr_frag(lds + ring + t1 + nl * 2048, 512);
      }
      if (s < 14) {
        dma16x2(dv64, dv64b, wsrc + (28 + 2 * s) * (8 * 896), lds0 + ring + (2 * s) * 1024);
        asm volatile("s_waitcnt vmcnt(%2)" : "+v"(af[s][0]), "+v"(af[s][1]) : "i"(2 * (13 - s) + 4 * s + 2) : "memory");
      } else {
        if (s < 21) {
          dma16x2(dv64, dv64b, wosrc + (2 * sl) * (8 * 896), lds0 + ring + (2 * sl) * 1024);
        }
        asm volatile("s_waitcnt vmcnt(%2)" : "+v"(af[sl][0]), "+v"(af[sl][1]) : "i"(4 * (13 - sl) + 2 * (s - 13 < 7 ? s - 13 : 7)) : "memory");
      }
      t[0] = mfma(f0[s & 1], af[sl][0], t[0]); t[1] = mfma(f1[s & 1], af[sl][0], t[1]);
      t[2] = mfma(f0[s & 1], af[sl][1], t[2]); t[3] = mfma(f1[s & 1], af[sl][1], t[3]);
      if (s < 14) {
        BLOAD_SC1(af[s][0], av[0], r3, ((s + 14) >> 2) * 4096, ((s + 14) & 3) * 1024);
        BLOAD_SC1(af[s][1], av[1], r3, ((s + 14) >> 2) * 4096, ((s + 14) & 3) * 1024);
      }
      __builtin_amdgcn_sched_barrier(0);
    }
    flags_wait(ws + WS_GO + ((tb64 >> 1) * 16 + 4 * w + (lane & 3)) * 128);
    asm volatile("s_waitcnt vmcnt(0)" ::: "memory");
    {
      const u32x4 ro = mkrsrc(ws + WS_O, TBYTES);
      unsigned ov[2];
#pragma unroll
      for (int e = 0; e < 2; e++) ov[e] = (unsigned)((tb64 * 2 + (tj ^ e)) * 56 + 14 * w) * 512 + lane * 16;
#pragma unroll
      for (int s = 0; s < 7; s++)
#pragma unroll
        for (int e = 0; e < 2; e++) BLOAD_SC1(af[s][e], ov[e], ro, (s >> 2) * 4096, (s & 3) * 1024);
      u32x4 g0[7], g1[7];
#pragma unroll
      for (int s = 0; s < 7; s++) { g0[s] = tr_frag(lds + ring + t0 + s * 2048, 512); g1[s] = tr_frag(lds + ring + t1 + s * 2048, 512); }
#pragma unroll
      for (int s = 0; s < 7; s++) {
        asm volatile("s_waitcnt vmcnt(%2)" : "+v"(af[s][0]), "+v"(af[s][1]) : "i"(2 * (6 - s)) : "memory");
        t[0] = mfma(g0[s], af[s][0], t[0]); t[1] = mfma(g1[s], af[s][0], t[1]);
        t[2] = mfma(g0[s], af[s][1], t[2]); t[3] = mfma(g1[s], af[s][1], t[3]);
      }
    }
    asm volatile("s_waitcnt lgkmcnt(0)" ::: "memory");
#pragma unroll
    for (int i = 1; i < 4; i++) tile_put(lds + ring + (i - 1) * 4096, t[i], lane);
    gsync(syb, w, ++eb);
#pragma unroll
    for (int i = 1; i < 4; i++) tile_add(lds + K2_B_OFF + (w ^ i) * 28672 + (i - 1) * 4096, t[0], lane);
    unsigned pk[8];
    const float og = 1.0f - gate;
#pragma unroll
    for (int j = 0; j < 8; j++)
      pk[j] = pack2(__builtin_fmaf(og, __builtin_bit_cast(float, x1p[j] << 16), t[0][2 * j]), __builtin_fmaf(og, __builtin_bit_cast(float, x1p[j] & 0xffff0000u), t[0][2 * j + 1]));
    char* op = out + (size_t)((tb64 * 2 + tj) * 32 + r) * 896 + (nb * 64 + ni * 32) * 2 + hh * 16;
#pragma unroll
    for (int gp = 0; gp < 2; gp++) {
      auto a2 = __builtin_amdgcn_permlane32_swap(pk[4 * gp], pk[4 * gp + 2], false, false);
      auto c2 = __builtin_amdgcn_permlane32_swap(pk[4 * gp + 1], pk[4 * gp + 3], false, false);
      u32x4 v; v[0] = a2[0]; v[1] = c2[0]; v[2] = a2[1]; v[3] = c2[1];
      *(u32x4*)(op + gp * 32) = v;
    }
  }
}

extern "C" void kernel_launch(void* const* d_in, const int* in_sizes, int n_in,
                              void* d_out, int out_size, void* d_ws, size_t ws_size,
                              hipStream_t stream) {
  const char* x = (const char*)d_in[0]; const char* wqkv = (const char*)d_in[1]; const char* wo = (const char*)d_in[2];
  const char* w1 = (const char*)d_in[4]; const char* w2 = (const char*)d_in[5]; const char* w3 = (const char*)d_in[6];
  const char* w4 = (const char*)d_in[7]; const char* gw = (const char*)d_in[8];
  (void)hipFuncSetAttribute((const void*)fused_kernel, hipFuncAttributeMaxDynamicSharedMemorySize, K2_LDS);
  fused_kernel<<<256, 512, K2_LDS, stream>>>(x, wqkv, w1, gw, wo, w2, w3, w4, (char*)d_ws, (char*)d_out);
}
```
